```python
import torch

def _softmax(x, axis=-1):
    return torch.softmax(x, dim=axis)
def _gelu(x):
    return torch.nn.functional.gelu(x, approximate="tanh")
def _silu(x):
    return torch.nn.functional.silu(x)
def _sigmoid(x):
    return torch.sigmoid(x)
def _layernorm(x, axis=-1, eps=1e-5):
    m = x.mean(dim=axis, keepdim=True)
    v = x.var(dim=axis, keepdim=True, unbiased=False)
    return (x - m) / torch.sqrt(v + eps)
def _scatter_add(tgt, idx, src, axis=0):
    return tgt.clone().index_add_(axis, idx, src)
def _segment_sum(src, seg, n):
    out = torch.zeros((n,) + src.shape[1:], dtype=src.dtype)
    return out.index_add_(0, seg, src)
def _pad(x, pw):
    flat = tuple(v for pair in reversed(pw) for v in pair)
    return torch.nn.functional.pad(x, flat)

kernel_name = 'parallel_sum__linear_attn_block__geglu_ffn__prompt'

def setup_inputs():

    _w = (lambda a: (lambda t: t.to(torch.bfloat16) if t.is_floating_point() else t)(torch.as_tensor(a)))
    def _A_setup():
        B, S, D = 1, 2048, 576
        return {
            "x": _w((lambda *s: torch.randn(*s, dtype=torch.bfloat16) * 0.05)(B, S, D)),
            "wqkv": _w((lambda *s: torch.randn(*s, dtype=torch.bfloat16) * 0.4)(D, 3 * D)),
            "wo": _w((lambda *s: torch.randn(*s, dtype=torch.bfloat16) * 0.2)(D, D)),
        }
    _A_in = _A_setup()

    def _B_setup():
        B, S, D, F = 1, 2048, 576, 4608
        return {
            "x": _w((lambda *s: torch.randn(*s, dtype=torch.bfloat16) * 0.05)(B, S, D)),
            "w_gate": _w((lambda *s: torch.randn(*s, dtype=torch.bfloat16) * 0.05)(D, F)),
            "w_up": _w((lambda *s: torch.randn(*s, dtype=torch.bfloat16) * 0.05)(D, F)),
            "w_down": _w((lambda *s: torch.randn(*s, dtype=torch.bfloat16) * 0.05)(F, D)),
        }
    _B_in = _B_setup()

    return {
        "A_x": _A_in["x"],
        "A_wqkv": _A_in["wqkv"],
        "A_wo": _A_in["wo"],
        "B_x": _B_in["x"],
        "B_w_gate": _B_in["w_gate"],
        "B_w_up": _B_in["w_up"],
        "B_w_down": _B_in["w_down"],
    }

def reference(A_x, A_wqkv, A_wo, B_x, B_w_gate, B_w_up, B_w_down):
    def _A_ref(x, wqkv, wo):
        B, S, D = x.shape
        H = 32; Dh = D // H
        qkv = (lambda x, *d: x.permute(*d))((x @ wqkv).reshape(B, S, 3, H, Dh), 2, 0, 3, 1, 4)
        q, k, v = qkv[0], qkv[1], qkv[2]
        phi = lambda t: torch.where(t > 0, t + 1, torch.exp(t)) + 1e-6
        pq, pk = phi(q), phi(k)
        kv = torch.einsum("bhsd,bhse->bhde", pk, v)
        z = torch.einsum("bhsd->bhd", pk)
        num = torch.einsum("bhqd,bhde->bhqe", pq, kv)
        den = torch.einsum("bhqd,bhd->bhq", pq, z)[..., None] + 1e-6
        o = num / den
        return (lambda x, *d: x.permute(*d))(o, 0, 2, 1, 3).reshape(B, S, D) @ wo

    def _B_ref(x, w_gate, w_up, w_down):
        return (_gelu(x @ w_gate) * (x @ w_up)) @ w_down

    x = A_x
    xn = _layernorm(x, axis=-1)
    return x + _A_ref(xn, A_wqkv, A_wo) + _B_ref(xn, B_w_gate, B_w_up, B_w_down)
```

```cpp
#include <hip/hip_runtime.h>
#include <hip/hip_bf16.h>
#include <stdint.h>

#define DI __device__ __forceinline__
typedef __attribute__((ext_vector_type(8))) short s16x8;
typedef __attribute__((ext_vector_type(4))) short s16x4;
typedef __attribute__((ext_vector_type(4))) float f32x4;
typedef __attribute__((ext_vector_type(4))) unsigned u32x4;
typedef __attribute__((ext_vector_type(2))) unsigned u32x2;
typedef __amdgpu_buffer_rsrc_t rsrc_t;
#define LDSP(T, a) ((__attribute__((address_space(3))) T*)(size_t)(a))

constexpr int S_ = 2048, D_ = 576, F_ = 4608, NH = 32, DH = 18;
constexpr int KS_FFN = 144, KS_ALL = 164;
constexpr size_t OFF_XF = 0;
constexpr size_t SZ_XF = (size_t)128 * 18 * 1024;
constexpr size_t OFF_HF = OFF_XF + SZ_XF;
constexpr size_t SZ_HF = (size_t)KS_ALL * 128 * 1024;
constexpr size_t OFF_PQ = OFF_HF + SZ_HF;
constexpr size_t SZ_PQ = (size_t)32 * 2048 * 20 * 2;
constexpr size_t OFF_KV = OFF_PQ + SZ_PQ;
constexpr size_t SZ_KV = (size_t)32 * 8 * 352 * 4;
constexpr size_t OFF_MW = OFF_KV + SZ_KV;
constexpr size_t SZ_MW = (size_t)640 * 576 * 2;
constexpr size_t OFF_PT = OFF_MW + SZ_MW;
constexpr size_t SZ_PT = (size_t)4 * 16 * 4 * 4 * 18 * 1024;

DI rsrc_t mk_rsrc(const void* p, unsigned bytes) {
  return __builtin_amdgcn_make_buffer_rsrc((void*)p, (short)0, (int)bytes, 0x00020000);
}
DI unsigned rfl(unsigned v) { return __builtin_amdgcn_readfirstlane(v); }
DI float bf_lo(unsigned u) { return __uint_as_float(u << 16); }
DI float bf_hi(unsigned u) { return __uint_as_float(u & 0xffff0000u); }
DI unsigned pack2(float lo, float hi) {
  typedef __attribute__((ext_vector_type(2))) __bf16 bf2;
  typedef __attribute__((ext_vector_type(2))) float f2;
  f2 v = {lo, hi};
  bf2 r = __builtin_convertvector(v, bf2);
  return __builtin_bit_cast(unsigned, r);
}
DI s16x8 mk_frag(unsigned a, unsigned b, unsigned c, unsigned d) {
  u32x4 v = {a, b, c, d};
  return __builtin_bit_cast(s16x8, v);
}
DI f32x4 mfma16(s16x8 a, s16x8 b, f32x4 c) { return __builtin_amdgcn_mfma_f32_16x16x32_bf16(a, b, c, 0, 0, 0); }
DI s16x4 trread(unsigned addr) { return __builtin_amdgcn_ds_read_tr16_b64_v4i16(LDSP(s16x4, addr)); }
DI s16x8 trfrag(unsigned a0, unsigned a1) {
  s16x4 lo = trread(a0), hi = trread(a1);
  return __builtin_shufflevector(lo, hi, 0, 1, 2, 3, 4, 5, 6, 7);
}
DI void glds16(unsigned m0v, unsigned voff, rsrc_t rs, unsigned soff) {
  asm volatile("s_mov_b32 m0, %0\n\ts_nop 0\n\tbuffer_load_dwordx4 %1, %2, %3 offen lds"
               :: "s"(m0v), "v"(voff), "s"(rs), "s"(soff) : "memory");
}
template <int IMM> DI void bload16(u32x4& d, unsigned voff, rsrc_t rs, unsigned soff) {
  asm volatile("buffer_load_dwordx4 %0, %1, %2, %3 offen offset:%4"
               : "=v"(d) : "v"(voff), "s"(rs), "s"(soff), "i"(IMM));
}
template <int N> DI void wait_vm2(u32x4& a, u32x4& b) {
  asm volatile("s_waitcnt vmcnt(%2)" : "+v"(a), "+v"(b) : "i"(N));
}
template <int N> DI void wait_bar() {
  asm volatile("s_waitcnt vmcnt(%0) lgkmcnt(0)\n\ts_barrier" :: "i"(N) : "memory");
}
DI float fexp2(float x) { return __builtin_amdgcn_exp2f(x); }
DI float frcp(float x) { return __builtin_amdgcn_rcpf(x); }
DI float phi_f(float t) { return (t > 0.f ? t + 1.f : fexp2(t * 1.4426950408889634f)) + 1e-6f; }
DI float geglu(float g, float u) {
  float e = fexp2(g * __builtin_fmaf(g * g, -0.10294323970f, -2.30220819814f));
  return (g * u) * frcp(1.f + e);
}
template <int CTRL> DI float dpp_add(float v) {
  return v + __builtin_bit_cast(float, __builtin_amdgcn_update_dpp(0, __builtin_bit_cast(int, v), CTRL, 0xf, 0xf, true));
}
DI float reduce32(float v) {
  v = dpp_add<0xB1>(v);
  v = dpp_add<0x4E>(v);
  v = dpp_add<0x141>(v);
  v = dpp_add<0x140>(v);
  const unsigned u = __float_as_uint(v);
  const auto r = __builtin_amdgcn_permlane16_swap(u, u, false, false);
  const unsigned r0 = r[0], r1 = r[1];
  return __uint_as_float(r0) + __uint_as_float(r1);
}

__global__ __launch_bounds__(256) void k0_ln(const uint16_t* __restrict__ x, u32x4* __restrict__ xf) {
  const int l = threadIdx.x & 63, w = threadIdx.x >> 6;
  const int tok = blockIdx.x * 8 + w * 2 + (l >> 5), p = l & 31;
  const u32x4* row = (const u32x4*)(x + (size_t)tok * D_);
  u32x4 c[3];
  c[0] = row[p];
  c[1] = row[p + 32];
  c[2] = row[p < 8 ? p + 64 : p];
  const bool has2 = p < 8;
  float v[24];
#pragma unroll
  for (int i = 0; i < 3; ++i)
#pragma unroll
    for (int j = 0; j < 4; ++j) { v[i * 8 + 2 * j] = bf_lo(c[i][j]); v[i * 8 + 2 * j + 1] = bf_hi(c[i][j]); }
  float s = 0.f, s2 = 0.f;
#pragma unroll
  for (int j = 0; j < 16; ++j) s += v[j];
#pragma unroll
  for (int j = 16; j < 24; ++j) s2 += v[j];
  s += has2 ? s2 : 0.f;
  const float mean = reduce32(s) * (1.f / 576.f);
  float q = 0.f, q2 = 0.f;
#pragma unroll
  for (int j = 0; j < 24; ++j) v[j] -= mean;
#pragma unroll
  for (int j = 0; j < 16; ++j) q += v[j] * v[j];
#pragma unroll
  for (int j = 16; j < 24; ++j) q2 += v[j] * v[j];
  q += has2 ? q2 : 0.f;
  const float var = reduce32(q) * (1.f / 576.f);
  const float rstd = 1.f / sqrtf(var + 1e-5f);
  const int T = tok >> 4, t16 = tok & 15;
#pragma unroll
  for (int i = 0; i < 3; ++i) {
    u32x4 o;
#pragma unroll
    for (int j = 0; j < 4; ++j) o[j] = pack2(v[i * 8 + 2 * j] * rstd, v[i * 8 + 2 * j + 1] * rstd);
    const int ch = p + 32 * i;
    if (i < 2 || has2) xf[((size_t)T * 18 + (ch >> 2)) * 64 + (ch & 3) * 16 + t16] = o;
  }
}

constexpr unsigned L1_BUF1 = 0, L1_BUF0 = 73728, L1_SCR = 147456, L1_TOTAL = 163840;

struct K1P {
  const void* xf; const void* wqkv; const void* wg; const void* wu;
  void* hf; void* pq; void* kv;
};

DI void k1_stage_ffn(unsigned ldsbase, unsigned buf, int w, unsigned voff, rsrc_t rs, unsigned colbytes) {
  const unsigned row0 = 144u * (w & 3);
  const unsigned dst = ldsbase + buf + (w >> 2) * 36864u + row0 * 64u;
#pragma unroll
  for (int i = 0; i < 9; ++i) glds16(dst + i * 1024u, voff, rs, (row0 + 16u * i) * 9216u + colbytes);
}

DI void ffn_unit(const u32x4 (&xf)[2][18], unsigned aG, unsigned aU, f32x4 (&G)[2], f32x4 (&U)[2]) {
  G[0] = G[1] = U[0] = U[1] = f32x4{0.f, 0.f, 0.f, 0.f};
#pragma unroll
  for (int s = 0; s < 18; ++s) {
    const s16x8 wg = trfrag(aG + s * 2048, aG + s * 2048 + 256);
    const s16x8 wu = trfrag(aU + s * 2048, aU + s * 2048 + 256);
    const s16x8 x0 = __builtin_bit_cast(s16x8, xf[0][s]), x1 = __builtin_bit_cast(s16x8, xf[1][s]);
    G[0] = mfma16(wg, x0, G[0]);
    G[1] = mfma16(wg, x1, G[1]);
    U[0] = mfma16(wu, x0, U[0]);
    U[1] = mfma16(wu, x1, U[1]);
  }
}
DI void ffn_epi(const f32x4 (&G)[2], const f32x4 (&U)[2], u32x2 (&h)[2]) {
#pragma unroll
  for (int T = 0; T < 2; ++T) {
    h[T][0] = pack2(geglu(G[T][0], U[T][0]), geglu(G[T][1], U[T][1]));
    h[T][1] = pack2(geglu(G[T][2], U[T][2]), geglu(G[T][3], U[T][3]));
  }
}

__global__ __launch_bounds__(512, 2) void k1_gemm(K1P p) {
  __shared__ __attribute__((aligned(16))) char smem[L1_TOTAL];
  const unsigned lb = (unsigned)(size_t)(__attribute__((address_space(3))) char*)smem;
  const int tid = threadIdx.x, l = tid & 63;
  const int w = rfl(tid >> 6);
  const int b = blockIdx.x, xcd = b & 7, r = b >> 3;
  const int j = 4 * xcd + (r & 3), ib = r >> 2;
  const int g = l >> 4, q = (l & 15) >> 2, pp = l & 3, c16 = l & 15;

  const rsrc_t rsX = mk_rsrc(p.xf, (unsigned)SZ_XF);
  const rsrc_t rsQ = mk_rsrc(p.wqkv, D_ * 3 * D_ * 2);
  const rsrc_t rsW = mk_rsrc((w >> 2) ? p.wu : p.wg, D_ * F_ * 2);
  const rsrc_t rsH = mk_rsrc(p.hf, (unsigned)SZ_HF);

  {
    const int rl = l / 9, c = l - rl * 9;
    const int sec = c / 3, cc = c - sec * 3;
    const unsigned col = (sec == 0 ? 576u : (sec == 1 ? 0u : 1152u)) + 18u * j + (cc == 2 ? 10u : 8u * cc);
    const unsigned voff = rl * 3456u + col * 2u;
#pragma unroll
    for (int i = 0; i < 11; ++i) {
      const int inst = w + 8 * i;
      if (i < 10) {
        glds16(lb + L1_BUF0 + inst * 1008u, voff, rsQ, inst * 7u * 3456u);
      } else if (inst <= 82) {
        if (inst * 7 + rl < 576) glds16(lb + L1_BUF0 + inst * 1008u, voff, rsQ, inst * 7u * 3456u);
      }
    }
  }
  u32x4 xf[2][18];
  const int T0 = 16 * ib + 2 * w;
  {
    const unsigned voff = l * 16u;
    const unsigned sb = T0 * 18432u;
#define XL(s) bload16<((s) & 3) * 1024>(xf[0][s], voff, rsX, sb + ((s) >> 2) * 4096u); \
              bload16<((s) & 3) * 1024>(xf[1][s], voff, rsX, sb + 18432u + ((s) >> 2) * 4096u);
    XL(0) XL(1) XL(2) XL(3) XL(4) XL(5) XL(6) XL(7) XL(8) XL(9) XL(10) XL(11) XL(12) XL(13) XL(14) XL(15) XL(16) XL(17)
#undef XL
  }
  const unsigned wvoff = (l >> 2) * 9216u + (((l & 3) ^ ((l >> 5) << 1)) * 16u);
  const unsigned fcol = 288u * j + (j & 1) * 32u;
  const unsigned fcol1 = 288u * j + ((j & 1) ? 0u : 256u);
  k1_stage_ffn(lb, L1_BUF1, w, wvoff, rsW, fcol);

  wait_bar<45>();

  {
    const unsigned rowb = lb + L1_BUF0 + (8 * g + q) * 144u;
    const unsigned aKA = rowb + 8 * pp;
    const unsigned aKB = rowb + 40 + 8 * pp;
    const unsigned aVA = rowb + 96 + 8 * pp;
    const unsigned aVB = rowb + (pp == 0 ? 136u : (pp == 1 ? 72u : 88u));
    f32x4 KA[2], KB[2], VA[2], VB[2];
#pragma unroll
    for (int T = 0; T < 2; ++T) KA[T] = KB[T] = VA[T] = VB[T] = f32x4{0.f, 0.f, 0.f, 0.f};
#define QS(s) { wait_vm2<43 - 2 * (s)>(xf[0][s], xf[1][s]);                                      \
      const s16x8 x0 = __builtin_bit_cast(s16x8, xf[0][s]), x1 = __builtin_bit_cast(s16x8, xf[1][s]); \
      const s16x8 ka = trfrag(aKA + (s) * 4608, aKA + (s) * 4608 + 576);                          \
      const s16x8 kb = trfrag(aKB + (s) * 4608, aKB + (s) * 4608 + 576);                          \
      const s16x8 va = trfrag(aVA + (s) * 4608, aVA + (s) * 4608 + 576);                          \
      const s16x8 vb = trfrag(aVB + (s) * 4608, aVB + (s) * 4608 + 576);                          \
      KA[0] = mfma16(x0, ka, KA[0]); KA[1] = mfma16(x1, ka, KA[1]);                               \
      KB[0] = mfma16(x0, kb, KB[0]); KB[1] = mfma16(x1, kb, KB[1]);                               \
      VA[0] = mfma16(x0, va, VA[0]); VA[1] = mfma16(x1, va, VA[1]);                               \
      VB[0] = mfma16(x0, vb, VB[0]); VB[1] = mfma16(x1, vb, VB[1]); }
    QS(0) QS(1) QS(2) QS(3) QS(4) QS(5) QS(6) QS(7) QS(8) QS(9) QS(10) QS(11) QS(12) QS(13) QS(14) QS(15) QS(16) QS(17)
#undef QS
    unsigned fKA[4], fKB[4], fVA[4], fVB[4], fQ2[4];
#pragma unroll
    for (int T = 0; T < 2; ++T)
#pragma unroll
      for (int h2 = 0; h2 < 2; ++h2) {
        fKA[2 * T + h2] = pack2(phi_f(KA[T][2 * h2]), phi_f(KA[T][2 * h2 + 1]));
        fKB[2 * T + h2] = pack2(phi_f(KB[T][2 * h2]), phi_f(KB[T][2 * h2 + 1]));
        fVA[2 * T + h2] = pack2(VA[T][2 * h2], VA[T][2 * h2 + 1]);
        const unsigned raw = pack2(VB[T][2 * h2], VB[T][2 * h2 + 1]);
        fVB[2 * T + h2] = c16 < 2 ? 0x3f803f80u : raw;
        fQ2[2 * T + h2] = pack2(phi_f(VB[T][2 * h2]), phi_f(VB[T][2 * h2 + 1]));
      }
    const s16x8 aKAf = mk_frag(fKA[0], fKA[1], fKA[2], fKA[3]), aKBf = mk_frag(fKB[0], fKB[1], fKB[2], fKB[3]);
    const s16x8 bVAf = mk_frag(fVA[0], fVA[1], fVA[2], fVA[3]), bVBf = mk_frag(fVB[0], fVB[1], fVB[2], fVB[3]);
    const f32x4 z4 = {0.f, 0.f, 0.f, 0.f};
    const f32x4 R0 = mfma16(aKAf, bVAf, z4), R1 = mfma16(aKAf, bVBf, z4);
    const f32x4 R2 = mfma16(aKBf, bVAf, z4), R3 = mfma16(aKBf, bVBf, z4);
    {
      uint16_t* pq = (uint16_t*)p.pq + ((size_t)j * S_ + 256 * ib + 32 * w) * 20;
      const int dA = c16 - 4;
      const int dB = c16 < 8 ? c16 + 8 : c16 + 6;
      const bool okA = c16 >= 4, okB = (c16 >= 4 && c16 < 8) || c16 == 10 || c16 == 11;
#pragma unroll
      for (int T = 0; T < 2; ++T)
#pragma unroll
        for (int rr = 0; rr < 4; ++rr) {
          const int tk = 16 * T + 4 * g + rr;
          const unsigned ua = fKB[2 * T + (rr >> 1)], ub = fQ2[2 * T + (rr >> 1)];
          if (okA) pq[tk * 20 + dA] = (uint16_t)((rr & 1) ? ua >> 16 : ua);
          if (okB) pq[tk * 20 + dB] = (uint16_t)((rr & 1) ? ub >> 16 : ub);
        }
    }
    asm volatile("s_waitcnt lgkmcnt(0)\n\ts_barrier" ::: "memory");
    {
      const unsigned sb = lb + L1_SCR + w * 1376u;
      const int eB = c16 == 0 ? 18 : c16 + 14;
      const bool okB = c16 == 0 || c16 == 2 || c16 == 3;
#pragma unroll
      for (int rr = 0; rr < 4; ++rr) {
        const int d = 4 * g + rr;
        *LDSP(float, sb + (d * 19 + c16) * 4) = R0[rr];
        if (okB) *LDSP(float, sb + (d * 19 + eB) * 4) = R1[rr];
        if (g == 0 && rr >= 2) {
          *LDSP(float, sb + ((14 + rr) * 19 + c16) * 4) = R2[rr];
          if (okB) *LDSP(float, sb + ((14 + rr) * 19 + eB) * 4) = R3[rr];
        }
      }
    }
  }
  wait_bar<0>();
  if (tid < 342) {
    float a = 0.f;
#pragma unroll
    for (int ww = 0; ww < 8; ++ww) a += *LDSP(float, lb + L1_SCR + ww * 1376u + tid * 4);
    ((float*)p.kv)[(j * 8 + ib) * 352 + tid] = a;
  }

  const unsigned lanec = (8 * g + q) * 64u + 8u * pp;
  const unsigned seg0 = (g & 1) * 32u;
  const int pr = j >> 1;
  const unsigned hvoff = l * 16u;
#pragma unroll 1
  for (int grp = 0; grp < 4; ++grp) {
    const unsigned cur = (grp & 1) ? L1_BUF0 : L1_BUF1, nxt = (grp & 1) ? L1_BUF1 : L1_BUF0;
    k1_stage_ffn(lb, nxt, w, wvoff, rsW, grp == 3 ? fcol1 : fcol + 64u * (grp + 1));
    const unsigned a0 = lb + cur + lanec + seg0, a1 = lb + cur + lanec + (seg0 ^ 32u);
    f32x4 G[2], U[2];
    u32x2 h0[2], h1[2];
    ffn_unit(xf, a0, a0 + 36864u, G, U);
    ffn_epi(G, U, h0);
    ffn_unit(xf, a1, a1 + 36864u, G, U);
    ffn_epi(G, U, h1);
    const unsigned ks = 9 * pr + 5 * (j & 1) + grp;
    const unsigned so = (ks * 128u + T0) * 1024u;
#pragma unroll
    for (int T = 0; T < 2; ++T) {
      const u32x4 v = {h0[T][0], h0[T][1], h1[T][0], h1[T][1]};
      __builtin_amdgcn_raw_buffer_store_b128(v, rsH, hvoff + T * 1024u, so, 0);
    }
    wait_bar<2>();
  }
  {
    const unsigned a1 = lb + L1_BUF1 + lanec + seg0;
    f32x4 G[2], U[2];
    u32x2 h1[2];
    ffn_unit(xf, a1, a1 + 36864u, G, U);
    ffn_epi(G, U, h1);
    const unsigned ks = 9 * pr + 4;
    const unsigned so = (ks * 128u + T0) * 1024u + (j & 1) * 8u;
#pragma unroll
    for (int T = 0; T < 2; ++T) __builtin_amdgcn_raw_buffer_store_b64(h1[T], rsH, hvoff + T * 1024u, so, 0);
  }
}

__global__ __launch_bounds__(512) void k1b_fin(const float* __restrict__ kvp, const uint16_t* __restrict__ pq,
                                               const uint16_t* __restrict__ wo, char* __restrict__ hf,
                                               uint16_t* __restrict__ mw) {
  __shared__ float kv[352];
  const int tid = threadIdx.x, j = blockIdx.x >> 3, ib = blockIdx.x & 7;
  if (tid < 342) {
    float a = 0.f;
#pragma unroll
    for (int i = 0; i < 8; ++i) a += kvp[(j * 8 + i) * 352 + tid];
    kv[tid] = a;
  }
  __syncthreads();
  if (tid < 256) {
    const int tok = 256 * ib + tid;
    const u32x2* src = (const u32x2*)(pq + ((size_t)j * S_ + tok) * 20);
    float v[20];
#pragma unroll
    for (int i = 0; i < 5; ++i) {
      const u32x2 u = src[i];
      v[4 * i] = bf_lo(u[0]); v[4 * i + 1] = bf_hi(u[0]); v[4 * i + 2] = bf_lo(u[1]); v[4 * i + 3] = bf_hi(u[1]);
    }
    v[18] = 0.f; v[19] = 0.f;
    float den = 0.f;
#pragma unroll
    for (int d = 0; d < 18; ++d) den += v[d] * kv[d * 19 + 18];
    const float inv = 1.f / (den + 1e-6f);
    const int T = tok >> 4, t16 = tok & 15;
#pragma unroll
    for (int i = 0; i < 5; ++i) {
      const int pi = 5 * j + i;
      const int m = pi >> 3, t = (pi >> 2) & 1, gg = pi & 3;
      u32x2 o = {pack2(v[4 * i] * inv, v[4 * i + 1] * inv), pack2(v[4 * i + 2] * inv, v[4 * i + 3] * inv)};
      *(u32x2*)(hf + ((size_t)(KS_FFN + m) * 128 + T) * 1024 + (gg * 16 + t16) * 16 + t * 8) = o;
    }
  } else {
    for (int o = tid - 256; o < 20 * 72; o += 256) {
      const int d = o / 72, n = 72 * ib + (o - d * 72);
      float a = 0.f;
      if (d < 18) {
#pragma unroll
        for (int e = 0; e < 18; ++e) a += kv[d * 19 + e] * bf_lo(wo[(18 * j + e) * D_ + n]);
      }
      mw[(20 * j + d) * D_ + n] = (uint16_t)pack2(a, 0.f);
    }
  }
}

struct K2P { const void* hf; const void* wd; const void* mw; void* part; };
constexpr unsigned L2_SLOT = 18432, L2_TOTAL = 6 * L2_SLOT;

__global__ __launch_bounds__(512, 2) void k2_gemm(K2P p) {
  __shared__ __attribute__((aligned(16))) char smem[L2_TOTAL];
  const unsigned lb = (unsigned)(size_t)(__attribute__((address_space(3))) char*)smem;
  const int tid = threadIdx.x, l = tid & 63;
  const int w = rfl(tid >> 6), wm = w & 3, wk = w >> 2;
  const int b = blockIdx.x, xcd = b & 7, r = b >> 3;
  const int sp = xcd >> 1, mi = (xcd & 1) * 8 + (r >> 2), ni = r & 3;
  const int nst = sp < 2 ? 21 : 20, p0 = sp < 2 ? 21 * sp : 42 + 20 * (sp - 2);
  const int g = l >> 4, q = (l & 15) >> 2, pp = l & 3;

  const rsrc_t rsH = mk_rsrc(p.hf, (unsigned)SZ_HF);
  const rsrc_t rsW = mk_rsrc(p.wd, F_ * D_ * 2);
  const rsrc_t rsM = mk_rsrc(p.mw, (unsigned)SZ_MW);

  unsigned vo[3];
#pragma unroll
  for (int i = 0; i < 3; ++i) {
    const int idx = i == 0 ? w : (i == 1 ? w + 8 : 16 + (w & 1));
    const int kh = idx >= 9;
    const int ci = 64 * (idx - 9 * kh) + l, rho = ci / 18, c = ci - 18 * rho;
    vo[i] = (rho + 32 * kh) * 1152u + c * 16u;
  }
  const unsigned hvoff = l * 16u;
  const int pv = p0 - (21 - nst);
  const unsigned hso = (8 * mi + 2 * wm) * 1024u + (unsigned)(2 * pv + wk) * 131072u;
  const unsigned wso = (unsigned)pv * 73728u + ni * 288u;
  const unsigned m0w = lb + w * 1024u, m0x = lb + (16 + (w & 1)) * 1024u;
  const int xsel = w >> 1;

  u32x4 hr[6][2];
#define K2_ISSUE(t)                                                                                \
  {                                                                                                \
    const bool att = pv + (t) >= KS_FFN / 2;                                                       \
    const rsrc_t rs = att ? rsM : rsW;                                                             \
    const unsigned so = wso + (t) * 73728u - (att ? (KS_FFN / 2) * 73728u : 0u);                   \
    glds16(m0w + ((t) % 6) * L2_SLOT, vo[0], rs, so);                                              \
    glds16(m0w + ((t) % 6) * L2_SLOT + 8192u, vo[1], rs, so);                                      \
    if (((t) & 3) == xsel) glds16(m0x + ((t) % 6) * L2_SLOT, vo[2], rs, so);                       \
    bload16<0>(hr[(t) % 6][0], hvoff, rsH, hso + (t) * 262144u);                                   \
    bload16<1024>(hr[(t) % 6][1], hvoff, rsH, hso + (t) * 262144u);                                \
  }

  f32x4 acc[9][2];
#pragma unroll
  for (int c = 0; c < 9; ++c) acc[c][0] = acc[c][1] = f32x4{0.f, 0.f, 0.f, 0.f};
  const unsigned lanec = lb + wk * 9216u + (4 * g + q) * 288u + 8u * pp;

#define K2_WAIT(t, n) asm volatile("s_waitcnt vmcnt(" #n ") lgkmcnt(0)\n\ts_barrier" : "+v"(hr[(t) % 6][0]), "+v"(hr[(t) % 6][1]) :: "memory");
#define K2_COMP(t)                                                                                 \
  {                                                                                                \
    const unsigned a = lanec + ((t) % 6) * L2_SLOT;                                                \
    const s16x8 h0 = __builtin_bit_cast(s16x8, hr[(t) % 6][0]), h1 = __builtin_bit_cast(s16x8, hr[(t) % 6][1]); \
    _Pragma("unroll") for (int c = 0; c < 9; ++c) {                                                \
      const s16x8 wf = trfrag(a + c * 32, a + c * 32 + 4608);                                      \
      acc[c][0] = mfma16(wf, h0, acc[c][0]);                                                       \
      acc[c][1] = mfma16(wf, h1, acc[c][1]);                                                       \
    }                                                                                              \
  }
#define K2_ST(t) K2_WAIT(t, 16) K2_ISSUE((t) + 5) K2_COMP(t)
  if (nst == 21) K2_ISSUE(0)
  K2_ISSUE(1) K2_ISSUE(2) K2_ISSUE(3) K2_ISSUE(4)
  if (nst == 21) {
    K2_WAIT(0, 16) K2_ISSUE(5) K2_COMP(0)
  } else {
    K2_ISSUE(5)
  }
  K2_ST(1) K2_ST(2) K2_ST(3) K2_ST(4) K2_ST(5) K2_ST(6) K2_ST(7) K2_ST(8)
  K2_ST(9) K2_ST(10) K2_ST(11) K2_ST(12) K2_ST(13) K2_ST(14) K2_ST(15)
  K2_WAIT(16, 16) K2_COMP(16)
  K2_WAIT(17, 12) K2_COMP(17)
  K2_WAIT(18, 8) K2_COMP(18)
  K2_WAIT(19, 4) K2_COMP(19)
  K2_WAIT(20, 0) K2_COMP(20)
#undef K2_ST
#undef K2_COMP
#undef K2_WAIT
#undef K2_ISSUE

  asm volatile("s_waitcnt lgkmcnt(0)\n\ts_barrier" ::: "memory");
  const unsigned xb = lb + wm * 18432u + l * 16u;
  if (wk == 1) {
#pragma unroll
    for (int c = 0; c < 9; ++c)
#pragma unroll
      for (int T = 0; T < 2; ++T) *LDSP(f32x4, xb + (c * 2 + T) * 1024u) = acc[c][T];
  }
  asm volatile("s_waitcnt lgkmcnt(0)\n\ts_barrier" ::: "memory");
  if (wk == 0) {
    f32x4* dst = (f32x4*)p.part + ((((size_t)sp * 16 + mi) * 4 + ni) * 4 + wm) * 18 * 64 + l;
#pragma unroll
    for (int c = 0; c < 9; ++c)
#pragma unroll
      for (int T = 0; T < 2; ++T) dst[(c * 2 + T) * 64] = acc[c][T] + *LDSP(f32x4, xb + (c * 2 + T) * 1024u);
  }
}

__global__ __launch_bounds__(256) void k3_red(const f32x4* __restrict__ part, const uint16_t* __restrict__ x,
                                              uint16_t* __restrict__ out) {
  __shared__ __attribute__((aligned(16))) float tile[32 * 148];
  const int tid = threadIdx.x, l = tid & 63, w = tid >> 6;
  const int b = blockIdx.x, wm = b & 3, ni = (b >> 2) & 3, mi = b >> 4;
  const int g = l >> 4, c16 = l & 15;
  for (int tau = w; tau < 18; tau += 4) {
    f32x4 a = {0.f, 0.f, 0.f, 0.f};
#pragma unroll
    for (int sp = 0; sp < 4; ++sp) a += part[(((((size_t)sp * 16 + mi) * 4 + ni) * 4 + wm) * 18 + tau) * 64 + l];
    const int c = tau >> 1, T = tau & 1;
    *(f32x4*)&tile[(16 * T + c16) * 148 + 16 * c + 4 * g] = a;
  }
  __syncthreads();
  for (int it = tid; it < 576; it += 256) {
    const int tk = it / 18, ch = it - tk * 18;
    const size_t o = (size_t)(128 * mi + 32 * wm + tk) * D_ + 144 * ni + 8 * ch;
    const u32x4 xv = *(const u32x4*)(x + o);
    const f32x4 a = *(const f32x4*)&tile[tk * 148 + 8 * ch], c = *(const f32x4*)&tile[tk * 148 + 8 * ch + 4];
    u32x4 ov;
    ov[0] = pack2(a[0] + bf_lo(xv[0]), a[1] + bf_hi(xv[0]));
    ov[1] = pack2(a[2] + bf_lo(xv[1]), a[3] + bf_hi(xv[1]));
    ov[2] = pack2(c[0] + bf_lo(xv[2]), c[1] + bf_hi(xv[2]));
    ov[3] = pack2(c[2] + bf_lo(xv[3]), c[3] + bf_hi(xv[3]));
    *(u32x4*)(out + o) = ov;
  }
}

extern "C" void kernel_launch(void* const* d_in, const int* in_sizes, int n_in, void* d_out, int out_size,
                              void* d_ws, size_t ws_size, hipStream_t stream) {
  const uint16_t* x = (const uint16_t*)d_in[0];
  const void* wqkv = d_in[1];
  const uint16_t* wo = (const uint16_t*)d_in[2];
  const void* wg = d_in[4];
  const void* wu = d_in[5];
  const void* wd = d_in[6];
  char* ws = (char*)d_ws;

  k0_ln<<<256, 256, 0, stream>>>(x, (u32x4*)(ws + OFF_XF));
  K1P p1;
  p1.xf = ws + OFF_XF; p1.wqkv = wqkv; p1.wg = wg; p1.wu = wu;
  p1.hf = ws + OFF_HF; p1.pq = ws + OFF_PQ; p1.kv = ws + OFF_KV;
  k1_gemm<<<256, 512, 0, stream>>>(p1);
  k1b_fin<<<256, 512, 0, stream>>>((const float*)(ws + OFF_KV), (const uint16_t*)(ws + OFF_PQ), wo, ws + OFF_HF,
                                   (uint16_t*)(ws + OFF_MW));
  K2P p2;
  p2.hf = ws + OFF_HF; p2.wd = wd; p2.mw = ws + OFF_MW; p2.part = ws + OFF_PT;
  k2_gemm<<<256, 512, 0, stream>>>(p2);
  k3_red<<<256, 256, 0, stream>>>((const f32x4*)(ws + OFF_PT), x, (uint16_t*)d_out);
}
```

```cpp
#include <hip/hip_runtime.h>
#include <hip/hip_bf16.h>
#include <stdint.h>

#define DI __device__ __forceinline__
typedef __attribute__((ext_vector_type(8))) short s16x8;
typedef __attribute__((ext_vector_type(4))) short s16x4;
typedef __attribute__((ext_vector_type(4))) float f32x4;
typedef __attribute__((ext_vector_type(4))) unsigned u32x4;
typedef __attribute__((ext_vector_type(2))) unsigned u32x2;
typedef __amdgpu_buffer_rsrc_t rsrc_t;
#define LDSP(T, a) ((__attribute__((address_space(3))) T*)(size_t)(a))

constexpr int S_ = 2048, D_ = 576, F_ = 4608, NH = 32, DH = 18;
constexpr int KS_FFN = 144, KS_ALL = 168;
constexpr size_t OFF_XF = 0;
constexpr size_t SZ_XF = (size_t)128 * 18 * 1024;
constexpr size_t OFF_WQ = OFF_XF + SZ_XF;
constexpr size_t SZ_WQ = (size_t)32 * 72 * 1024;
constexpr size_t OFF_HF = OFF_WQ + SZ_WQ;
constexpr size_t SZ_HF = (size_t)KS_ALL * 128 * 1024;
constexpr size_t OFF_KV = OFF_HF + SZ_HF;
constexpr size_t SZ_KV = (size_t)32 * 8 * 352 * 4;
constexpr size_t OFF_XB = OFF_KV + SZ_KV;
constexpr size_t SZ_XB = (size_t)32 * 8 * 8 * 9 * 1024;
constexpr size_t OFF_CT = OFF_XB + SZ_XB;
constexpr int CT_X = 0, CT_KV = 256, CT_H = 264, CT_H2 = 328, CT_N = 400;
constexpr size_t SZ_CT = (size_t)CT_N * 128;

DI rsrc_t mk_rsrc(const void* p, unsigned bytes) {
  return __builtin_amdgcn_make_buffer_rsrc((void*)p, (short)0, (int)bytes, 0x00020000);
}
DI unsigned rfl(unsigned v) { return __builtin_amdgcn_readfirstlane(v); }
DI float bf_lo(unsigned u) { return __uint_as_float(u << 16); }
DI float bf_hi(unsigned u) { return __uint_as_float(u & 0xffff0000u); }
DI unsigned pack2(float lo, float hi) {
  typedef __attribute__((ext_vector_type(2))) __bf16 bf2;
  typedef __attribute__((ext_vector_type(2))) float f2;
  f2 v = {lo, hi};
  bf2 r = __builtin_convertvector(v, bf2);
  return __builtin_bit_cast(unsigned, r);
}
DI s16x8 mk_frag(unsigned a, unsigned b, unsigned c, unsigned d) {
  u32x4 v = {a, b, c, d};
  return __builtin_bit_cast(s16x8, v);
}
DI f32x4 mfma16(s16x8 a, s16x8 b, f32x4 c) { return __builtin_amdgcn_mfma_f32_16x16x32_bf16(a, b, c, 0, 0, 0); }
DI s16x4 trread(unsigned addr) { return __builtin_amdgcn_ds_read_tr16_b64_v4i16(LDSP(s16x4, addr)); }
DI s16x8 trfrag(unsigned a0, unsigned a1) {
  s16x4 lo = trread(a0), hi = trread(a1);
  return __builtin_shufflevector(lo, hi, 0, 1, 2, 3, 4, 5, 6, 7);
}
DI void glds16(unsigned m0v, unsigned voff, rsrc_t rs, unsigned soff) {
  asm volatile("s_mov_b32 m0, %0\n\ts_nop 0\n\tbuffer_load_dwordx4 %1, %2, %3 offen lds"
               :: "s"(m0v), "v"(voff), "s"(rs), "s"(soff) : "memory");
}
DI void warm_lines(unsigned m0v, unsigned voff, rsrc_t rs) {
  asm volatile("s_mov_b32 m0, %0\n\ts_nop 0\n\tbuffer_load_dword %1, %2, 0 offen lds" :: "s"(m0v), "v"(voff), "s"(rs) : "memory");
}
template <int IMM> DI void bload16(u32x4& d, unsigned voff, rsrc_t rs, unsigned soff) {
  asm volatile("buffer_load_dwordx4 %0, %1, %2, %3 offen offset:%4"
               : "=v"(d) : "v"(voff), "s"(rs), "s"(soff), "i"(IMM));
}
template <int IMM> DI void bload16_sc1(u32x4& d, unsigned voff, rsrc_t rs, unsigned soff) {
  asm volatile("buffer_load_dwordx4 %0, %1, %2, %3 offen offset:%4 sc1"
               : "=v"(d) : "v"(voff), "s"(rs), "s"(soff), "i"(IMM));
}
template <int IMM> DI void bstore16_sc1(u32x4 v, unsigned voff, rsrc_t rs, unsigned soff) {
  asm volatile("buffer_store_dwordx4 %0, %1, %2, %3 offen offset:%4 sc1\n\ts_nop 1"
               :: "v"(v), "v"(voff), "s"(rs), "s"(soff), "i"(IMM) : "memory");
}
typedef __attribute__((address_space(1))) unsigned gu32;
DI void ctr_add(gu32* c) { __hip_atomic_fetch_add(c, 1u, __ATOMIC_RELAXED, __HIP_MEMORY_SCOPE_AGENT); }
DI void ctr_wait(gu32* c, unsigned n) {
  for (unsigned spins = 0; spins < (1u << 20); ++spins) {
    if ((unsigned)__builtin_amdgcn_readfirstlane(__hip_atomic_load(c, __ATOMIC_RELAXED, __HIP_MEMORY_SCOPE_AGENT)) >= n) break;
    __builtin_amdgcn_s_sleep(1);
  }
}
template <int N> DI void wait_vm2(u32x4& a, u32x4& b) {
  asm volatile("s_waitcnt vmcnt(%2)" : "+v"(a), "+v"(b) : "i"(N));
}
template <int N> DI void wait_bar_nolgkm() {
  asm volatile("s_waitcnt vmcnt(%0)\n\ts_barrier" :: "i"(N) : "memory");
}
template <int N> DI void wait_bar() {
  asm volatile("s_waitcnt vmcnt(%0) lgkmcnt(0)\n\ts_barrier" :: "i"(N) : "memory");
}
DI float fexp2(float x) { return __builtin_amdgcn_exp2f(x); }
DI float frcp(float x) { return __builtin_amdgcn_rcpf(x); }
DI float phi_f(float t) { return (t > 0.f ? t + 1.f : fexp2(t * 1.4426950408889634f)) + 1e-6f; }
DI float geglu(float g, float u) {
  float e = fexp2(g * __builtin_fmaf(g * g, -0.10294323970f, -2.30220819814f));
  return (g * u) * frcp(1.f + e);
}
template <int CTRL> DI float dpp_add(float v) {
  return v + __builtin_bit_cast(float, __builtin_amdgcn_update_dpp(0, __builtin_bit_cast(int, v), CTRL, 0xf, 0xf, true));
}
DI float reduce32(float v) {
  v = dpp_add<0xB1>(v);
  v = dpp_add<0x4E>(v);
  v = dpp_add<0x141>(v);
  v = dpp_add<0x140>(v);
  const unsigned u = __float_as_uint(v);
  const auto r = __builtin_amdgcn_permlane16_swap(u, u, false, false);
  const unsigned r0 = r[0], r1 = r[1];
  return __uint_as_float(r0) + __uint_as_float(r1);
}

#define NO_WARM 0
#define ABL_NOW 0
#define ABL_NOH 0
#define ABL_NOCOMP 0
#define ABL1_NOCOMP 0
#define ABL1_NOEPI 0
#define ABL1_NOSTAGE 0
#define STAMP(buf, slot)
#define STAMPR(buf, slot)
#define STAMP_DUMP(buf)
__global__ __launch_bounds__(512) void k0_ln(const uint16_t* __restrict__ x, u32x4* __restrict__ xf,
                                             const uint16_t* __restrict__ wqkv, u32x4* __restrict__ wq,
                                             unsigned* __restrict__ ctr) {
  const int l = threadIdx.x & 63, w = threadIdx.x >> 6;
  if (threadIdx.x < CT_N / 8) ctr[blockIdx.x * (CT_N / 8) + threadIdx.x] = 0u;
  if (w >= 4) {
    const int item = blockIdx.x + 256 * (w - 4);
    if (item >= 576) return;
    const int h = item / 18, s = item - 18 * h, g = l >> 4, c = l & 15;
    const int col[4] = {576 + c, c < 4 ? 576 + 14 + c : c - 4, 1152 + c,
                        c < 4 ? 1152 + 14 + c : (c < 8 ? c + 8 : 14 + (c & 3))};
    const uint16_t* src = wqkv + (size_t)(32 * s + 8 * g) * (3 * D_) + 18 * h;
    unsigned short v[4][8];
#pragma unroll
    for (int X = 0; X < 4; ++X)
#pragma unroll
      for (int e = 0; e < 8; ++e) v[X][e] = src[e * (3 * D_) + col[X]];
#pragma unroll
    for (int X = 0; X < 4; ++X) {
      u32x4 o;
#pragma unroll
      for (int e = 0; e < 4; ++e) o[e] = (unsigned)v[X][2 * e] | ((unsigned)v[X][2 * e + 1] << 16);
      wq[((size_t)item * 4 + X) * 64 + l] = o;
    }
    return;
  }
  const int tok = blockIdx.x * 8 + w * 2 + (l >> 5), p = l & 31;
  const u32x4* row = (const u32x4*)(x + (size_t)tok * D_);
  u32x4 c[3];
  c[0] = row[p];
  c[1] = row[p + 32];
  c[2] = row[p < 8 ? p + 64 : p];
  const bool has2 = p < 8;
  float v[24];
#pragma unroll
  for (int i = 0; i < 3; ++i)
#pragma unroll
    for (int j = 0; j < 4; ++j) { v[i * 8 + 2 * j] = bf_lo(c[i][j]); v[i * 8 + 2 * j + 1] = bf_hi(c[i][j]); }
  float s = 0.f, s2 = 0.f;
#pragma unroll
  for (int j = 0; j < 16; ++j) s += v[j];
#pragma unroll
  for (int j = 16; j < 24; ++j) s2 += v[j];
  s += has2 ? s2 : 0.f;
  const float mean = reduce32(s) * (1.f / 576.f);
  float q = 0.f, q2 = 0.f;
#pragma unroll
  for (int j = 0; j < 24; ++j) v[j] -= mean;
#pragma unroll
  for (int j = 0; j < 16; ++j) q += v[j] * v[j];
#pragma unroll
  for (int j = 16; j < 24; ++j) q2 += v[j] * v[j];
  q += has2 ? q2 : 0.f;
  const float var = reduce32(q) * (1.f / 576.f);
  const float rstd = 1.f / sqrtf(var + 1e-5f);
  const int T = tok >> 4, t16 = tok & 15;
#pragma unroll
  for (int i = 0; i < 3; ++i) {
    u32x4 o;
#pragma unroll
    for (int j = 0; j < 4; ++j) o[j] = pack2(v[i * 8 + 2 * j] * rstd, v[i * 8 + 2 * j + 1] * rstd);
    const int ch = p + 32 * i;
    if (i < 2 || has2) xf[((size_t)T * 18 + (ch >> 2)) * 64 + (ch & 3) * 16 + t16] = o;
  }
}

constexpr unsigned L1_BUFA = 0, L1_BUFB = 73728, L1_SCR = 147456, L1_TOTAL = 163840;
constexpr int AUX_SC1 = 16;
#define SB() __builtin_amdgcn_sched_barrier(0)
#define PIN(x) asm volatile("" : "+v"(x))

struct KFP {
  const void* xf; const void* wq; const void* wg; const void* wu; const void* wd; const void* wo; const void* x;
  void* hf; void* kv; void* xb; void* ctr; void* out;
};

struct K1S {
  u32x4 xf[2][18];
  s16x4 wlo[6], whi[6];
  f32x4 acc[2][2];
  unsigned gel[4][2][2];
  u32x4 hq[2];
  float t0, t1;
  unsigned ard;
  u32x4 kp[8];
};
struct K1G { unsigned dst, soff, sstep, vo0, vo1, vok, wm0, wvo; rsrc_t rs, rsk, wrs; };
template <int I> DI void k1_piece(const K1G& G) {
  if (!ABL1_NOSTAGE) glds16(G.dst + I * 1024u, (I & 1) ? G.vo1 : G.vo0, G.rs, G.soff + I * G.sstep);
}
template <int I> DI void k1_piece2(const K1G& G, int l) {
  const int ci = 64 * I + l, rho = ci / 18, c = ci - 18 * rho;
  glds16(G.dst + I * 1024u, rho * 1152u + c * 16u, G.rs, G.soff);
}
template <int STRIDE, int N, int H> DI void k1_read(K1S& S, unsigned a0, int slot_unused = 0) {
  constexpr int P = N / 18, KS = N % 18;
  if constexpr (H == 0 && KS == 0) { S.ard = STRIDE == 4096 ? (a0 ^ (P << 5)) : a0 + P * 36864u; PIN(S.ard); }
  if constexpr (H == 0 && KS == 9) { S.ard += 9 * STRIDE; PIN(S.ard); }
  if constexpr (H == 0) S.wlo[N % 6] = trread(S.ard + (KS % 9) * STRIDE);
  else S.whi[N % 6] = trread(S.ard + (KS % 9) * STRIDE + STRIDE / 8);
}
template <int KIND, int U, int A, int PR, int PH, int SL> DI void k1_epi(K1S& S) {
  constexpr int T = PR >> 1, R = 2 * (PR & 1);
  const float a = S.acc[A][T][R], b = S.acc[A][T][R + 1];
  if (ABL1_NOEPI) {
    if (PH == 3 && SL == 1) { if (KIND == 1) S.gel[U][T][PR & 1] = pack2(a, b); else S.hq[T][2 * (U & 1) + (PR & 1)] = pack2(a, b) ^ S.gel[U][T][PR & 1]; }
    return;
  }
  if constexpr (KIND == 1) {
    if constexpr (PH == 0 && SL == 0) { S.t0 = a * a; S.t1 = b * b; }
    if constexpr (PH == 0 && SL == 1) {
      S.t0 = __builtin_fmaf(S.t0, -0.10294323970f, -2.30220819814f);
      S.t1 = __builtin_fmaf(S.t1, -0.10294323970f, -2.30220819814f);
    }
    if constexpr (PH == 1 && SL == 0) { S.t0 *= a; S.t1 *= b; }
    if constexpr (PH == 1 && SL == 1) { S.t0 = fexp2(S.t0); S.t1 = fexp2(S.t1); }
    if constexpr (PH == 2 && SL == 0) { S.t0 += 1.f; S.t1 += 1.f; }
    if constexpr (PH == 2 && SL == 1) { S.t0 = frcp(S.t0); S.t1 = frcp(S.t1); }
    if constexpr (PH == 3 && SL == 0) { S.t0 *= a; S.t1 *= b; }
    if constexpr (PH == 3 && SL == 1) { unsigned v = pack2(S.t0, S.t1); PIN(v); S.gel[U][T][PR & 1] = v; }
    else { PIN(S.t0); PIN(S.t1); }
  } else {
    if constexpr (PH == 1 && SL == 0) { const unsigned u = S.gel[U][T][PR & 1]; S.t0 = bf_lo(u); S.t1 = bf_hi(u); PIN(S.t0); PIN(S.t1); }
    if constexpr (PH == 2 && SL == 0) { S.t0 *= a; S.t1 *= b; PIN(S.t0); PIN(S.t1); }
    if constexpr (PH == 3 && SL == 0) { unsigned v = pack2(S.t0, S.t1); PIN(v); S.hq[T][2 * (U & 1) + (PR & 1)] = v; }
  }
}
template <int KIND, int U, int A, int I = 0> DI void k1_epi_all(K1S& S) {
  if constexpr (I < 32) { k1_epi<KIND, U, A, I / 8, (I / 2) % 4, I % 2>(S); k1_epi_all<KIND, U, A, I + 1>(S); }
}
DI void k1_store_pair(K1S& S, rsrc_t rsH, unsigned voff, unsigned soff) {
  bstore16_sc1<0>(S.hq[0], voff, rsH, soff);
  bstore16_sc1<1024>(S.hq[1], voff, rsH, soff);
}
template <int KIND, int PREV, int NP, int STRIDE, int NSTRIDE, int STG, int SIG, int N>
DI void k1_step(K1S& S, unsigned ab, unsigned abn, const K1G& G, rsrc_t rsH, unsigned hvoff, unsigned hso, unsigned hsop,
                gu32* sig, gu32* sig2) {
  constexpr int P = N / 18, KS = N % 18, A = P & 1, NT = NP * 18;
  constexpr int EK = P == 0 ? (PREV == 3 ? 2 : PREV) : (NP == 2 ? 1 : KIND);
  constexpr int EU = P == 0 ? (PREV == 3 ? 0 : 3) : (NP == 2 ? 0 : P - 1);
  constexpr int EA = P == 0 ? 1 : (A ^ 1);
  constexpr bool EPI = EK != 0 && KS >= 1 && KS <= 16;
  const f32x4 z4 = {0.f, 0.f, 0.f, 0.f};
  const s16x8 wf = __builtin_shufflevector(S.wlo[N % 6], S.whi[N % 6], 0, 1, 2, 3, 4, 5, 6, 7);
  const s16x8 x0 = __builtin_bit_cast(s16x8, S.xf[0][KS]), x1 = __builtin_bit_cast(s16x8, S.xf[1][KS]);
  if (!ABL1_NOCOMP) S.acc[A][0] = mfma16(wf, x0, KS == 0 ? z4 : S.acc[A][0]);
  else if (KS == 0) { S.acc[A][0] = z4; S.acc[A][0][0] = (float)wf[0]; }
  SB();
  if constexpr (N + 5 < NT) k1_read<STRIDE, N + 5, 0>(S, ab);
  else if constexpr (NSTRIDE != 0) k1_read<NSTRIDE == 0 ? 4096 : NSTRIDE, N + 5 - NT, 0>(S, abn);
  if constexpr (EPI) k1_epi<EK, EU, EA, (KS - 1) / 4, (KS - 1) % 4, 0>(S);
  SB();
  if (!ABL1_NOCOMP) S.acc[A][1] = mfma16(wf, x1, KS == 0 ? z4 : S.acc[A][1]);
  else if (KS == 0) { S.acc[A][1] = z4; }
  SB();
  if constexpr (N + 5 < NT) k1_read<STRIDE, N + 5, 1>(S, ab);
  else if constexpr (NSTRIDE != 0) k1_read<NSTRIDE == 0 ? 4096 : NSTRIDE, N + 5 - NT, 1>(S, abn);
  if constexpr (EPI) k1_epi<EK, EU, EA, (KS - 1) / 4, (KS - 1) % 4, 1>(S);
  if constexpr (STG == 1 && NP == 4 && N % 6 == 2 && N / 6 < 9) k1_piece<N / 6>(G);
  if constexpr (STG == 1 && NP == 2 && N % 2 == 0 && N >= 2 && N / 2 <= 9) k1_piece<N / 2 - 1>(G);
  if constexpr (SIG == 1 && !NO_WARM && N == 56) warm_lines(G.wm0, G.wvo, G.wrs);
  if constexpr (STG == 2 && N % 6 == 2 && N / 6 < 9) k1_piece2<N / 6>(G, hvoff >> 4);
  if constexpr (STG == 2 && N % 6 == 5 && N / 6 < 8) bload16_sc1<0>(S.kp[N / 6], G.vok, G.rsk, (N / 6) * 1408u);
  if constexpr (PREV == 2 && N == 17) k1_store_pair(S, rsH, hvoff, hsop);
  if constexpr (PREV == 3 && N == 17) {
#pragma unroll
    for (int T = 0; T < 2; ++T) {
      const u32x2 v = {S.hq[T][0], S.hq[T][1]};
      __builtin_amdgcn_raw_buffer_store_b64(v, rsH, hvoff + T * 1024u, hsop, AUX_SC1);
    }
  }
  if constexpr (KIND == 2 && NP == 4 && N == 2 * 18 + 17) k1_store_pair(S, rsH, hvoff, hso);
  if constexpr (N == 1) asm volatile("s_barrier" ::: "memory");
  if constexpr (SIG == 2 && N == NT - 8) { if (rfl(threadIdx.x >> 6) == 0) ctr_wait(sig, 32u); }
  if constexpr (NSTRIDE != 0 && N == NT - 8) wait_bar_nolgkm<KIND == 2 ? 2 : (SIG == 1 && !NO_WARM ? 1 : 0)>();
  if constexpr (SIG == 1 && N == NT - 8) { if (threadIdx.x == 0) ctr_add(sig); }
  if constexpr (SIG == 2 && N == NT - 8) { if (threadIdx.x == 0) ctr_add(sig2); }
  SB();
  if constexpr (N + 1 < NT) k1_step<KIND, PREV, NP, STRIDE, NSTRIDE, STG, SIG, N + 1>(S, ab, abn, G, rsH, hvoff, hso, hsop, sig, sig2);
}
template <int STRIDE> DI void k1_first_reads(K1S& S, unsigned ab) {
  k1_read<STRIDE, 0, 0>(S, ab); k1_read<STRIDE, 0, 1>(S, ab); k1_read<STRIDE, 1, 0>(S, ab); k1_read<STRIDE, 1, 1>(S, ab);
  k1_read<STRIDE, 2, 0>(S, ab); k1_read<STRIDE, 2, 1>(S, ab); k1_read<STRIDE, 3, 0>(S, ab); k1_read<STRIDE, 3, 1>(S, ab);
  k1_read<STRIDE, 4, 0>(S, ab); k1_read<STRIDE, 4, 1>(S, ab);
  SB();
}
template <int KS> DI void k1_qkv_step(K1S& S, s16x8 (&wq)[2][4], f32x4 (&Q)[4][2], unsigned qa, unsigned xv, rsrc_t rsX,
                                      unsigned xsb, const K1G& G) {
  if constexpr (KS + 6 < 18) {
    bload16<((KS + 6) & 3) * 1024>(S.xf[0][KS + 6], xv, rsX, xsb + ((KS + 6) >> 2) * 4096u);
    bload16<((KS + 6) & 3) * 1024>(S.xf[1][KS + 6], xv, rsX, xsb + 18432u + ((KS + 6) >> 2) * 4096u);
  } else if constexpr (KS < 15) { k1_piece<2 * (KS - 12)>(G); k1_piece<2 * (KS - 12) + 1>(G); }
  else k1_piece<KS - 9>(G);
  constexpr int YOUNGER = ABL1_NOSTAGE ? (KS < 12 ? 12 : 2 * (17 - KS)) : (KS < 15 ? 12 : 26 - KS);
  wait_vm2<YOUNGER>(S.xf[0][KS], S.xf[1][KS]);
  const s16x8 x0 = __builtin_bit_cast(s16x8, S.xf[0][KS]), x1 = __builtin_bit_cast(s16x8, S.xf[1][KS]);
  SB();
#pragma unroll
  for (int X = 0; X < 4; ++X) {
    Q[X][0] = mfma16(x0, wq[KS & 1][X], Q[X][0]);
    SB();
    if constexpr (KS + 1 < 18) wq[(KS + 1) & 1][X] = *LDSP(s16x8, qa + ((KS + 1) * 4 + X) * 1024u);
    SB();
    Q[X][1] = mfma16(x1, wq[KS & 1][X], Q[X][1]);
    SB();
  }
  if constexpr (KS + 1 < 18) k1_qkv_step<KS + 1>(S, wq, Q, qa, xv, rsX, xsb, G);
}

constexpr int K2D = 5;
constexpr unsigned L2_SLOT = 9216, L2_NS = K2D + 2;
constexpr unsigned L2_WO = L1_BUFA + 4 * L2_SLOT;
constexpr unsigned L2_MT = L1_BUFB + 3 * L2_SLOT, L2_KV = L1_SCR;
constexpr unsigned L2_ZS = L2_KV + 4 * 352 * 4;
constexpr unsigned L2_OWN = L1_BUFA, L2_OUT = L1_BUFA + 18432;

struct K2S {
  u32x4 hr[K2D + 1][2];
  u32x4 ha[3][2];
  s16x4 wlo[6], whi[6];
  f32x4 acc[9][2];
  unsigned flag, ardm;
  u32x4 zf[3];
  f32x4 den[2];
  unsigned ard;
};
struct K2C {
  rsrc_t rsH, rsW, rsB;
  unsigned vo0, vo1, hvoff, hsoF, hsoA, wsoF, xso, lb, lanec;
  int w, sp;
  gu32* chf;
};
constexpr bool k2_att(int n) { return n >= 18; }
constexpr unsigned k2_loc(int n) {
  return n < 8 ? 4 * (n >> 1) + (n & 1) : n < 10 ? 8 + n : 4 * ((n - 10) >> 1) + 2 + (n & 1);
}
constexpr unsigned k2_slot(int s) { return s < 4 ? L1_BUFA + s * L2_SLOT : L1_BUFB + (s - 4) * L2_SLOT; }
constexpr int k2_tW(int m) { return m == 4 ? -30 : 16 * (m - 5); }
constexpr int k2_tH(int m, int half) {
  return m <= 1 ? -40 + 2 * m + half : m <= 4 ? -24 + 2 * m + half : 16 * (m - 5) + 5 + 2 * half;
}
constexpr int k2_tA(int i) { return 16 * 12 + (i < 3 ? i : 2 * i - 2); }
constexpr int k2_younger(int t0, int n) {
  int cnt = 0;
  const int tw = 16 * n + 3;
  for (int m = 4; m <= 17; ++m) if (k2_tW(m) > t0 && k2_tW(m) < tw) ++cnt;
  for (int m = 0; m <= 17; ++m)
    for (int h = 0; h < 2; ++h) if (k2_tH(m, h) > t0 && k2_tH(m, h) < tw) ++cnt;
  for (int i = 0; i < 6; ++i) if (k2_tA(i) > t0 && k2_tA(i) < tw) ++cnt;
  return cnt;
}
constexpr int k2_count(int n) {
  const int m = n + 1, th = k2_tH(m, 1), tw = m >= 4 ? k2_tW(m) : -100;
  return k2_younger(th > tw ? th : tw, n);
}
static_assert(k2_count(0) == 8 && k2_count(1) == 8 && k2_count(3) == 10 && k2_count(8) == 10 && k2_count(12) == 13 && k2_count(13) == 15, "schedule");
static_assert(k2_younger(16 * 3 + 4, 5) == 6, "schedule");
template <int T, int I> DI void k2_issue(K2S& S, const K2C& C) {
  if constexpr (T <= 17) {
    constexpr unsigned ks = k2_loc(T);
    if constexpr (I == 0) glds16(C.lb + k2_slot(T % L2_NS) + C.w * 1024u, C.vo0, C.rsW, C.wsoF + ks * 36864u);
    if constexpr (I == 1) { if ((T & 7) == C.w) glds16(C.lb + k2_slot(T % L2_NS) + 8192u, C.vo1, C.rsW, C.wsoF + ks * 36864u); }
    if constexpr (I == 2) bload16_sc1<0>(S.hr[T % (K2D + 1)][0], C.hvoff, C.rsH, C.hsoF + ks * 131072u);
    if constexpr (I == 3) bload16_sc1<1024>(S.hr[T % (K2D + 1)][1], C.hvoff, C.rsH, C.hsoF + ks * 131072u);
  }
}
constexpr int k2_T(int M) { return M < 153 ? M / 9 : 17 + (M - 153) % 4; }
constexpr int k2_c(int M) { return M < 153 ? M % 9 : (M - 153) / 4; }
template <int M, int H> DI void k2_read(K2S& S, const K2C& C) {
  if constexpr (M < 189) {
    constexpr int T = k2_T(M), c = k2_c(M);
    if constexpr (M < 153) {
      if constexpr (H == 0 && c == 0) { S.ard = C.lanec + k2_slot(T % L2_NS); PIN(S.ard); }
      if constexpr (H == 0) S.wlo[M % 6] = trread(S.ard + c * 32);
      else S.whi[M % 6] = trread(S.ard + c * 32 + 4608);
    } else {
      if constexpr (H == 0 && M == 153) { S.ard = C.lanec + k2_slot(17 % L2_NS); PIN(S.ard); S.ardm = C.lanec + L2_MT; PIN(S.ardm); }
      const unsigned a = T == 17 ? S.ard + c * 32 : S.ardm + (T - 18) * L2_SLOT + c * 32;
      if constexpr (H == 0) S.wlo[M % 6] = trread(a);
      else S.whi[M % 6] = trread(a + 4608);
    }
  }
}
template <int c> DI void k2_send(K2S& S, const K2C& C) {
  if (C.w != C.sp) {
    const u32x4 v = {pack2(S.acc[c][0][0], S.acc[c][0][1]), pack2(S.acc[c][0][2], S.acc[c][0][3]),
                     pack2(S.acc[c][1][0], S.acc[c][1][1]), pack2(S.acc[c][1][2], S.acc[c][1][3])};
    bstore16_sc1<0>(v, C.hvoff, C.rsB, C.xso + c * 1024u);
  } else {
    *LDSP(f32x4, C.lb + L2_OWN + (c * 2) * 1024u + C.hvoff) = S.acc[c][0];
    *LDSP(f32x4, C.lb + L2_OWN + (c * 2 + 1) * 1024u + C.hvoff) = S.acc[c][1];
  }
}
template <int T, int N> DI void k2_wait(K2S& S) {
  asm volatile("s_waitcnt vmcnt(%2)\n\ts_barrier" : "+v"(S.hr[T % (K2D + 1)][0]), "+v"(S.hr[T % (K2D + 1)][1]) : "i"(N) : "memory");
}
template <int I> DI void k2_norm(K2S& S, const K2C& C) {
  const int l = C.hvoff >> 4, g = l >> 4, hr = l & 3;
  if constexpr (I < 3) {
    const int d0 = 32 * I + 4 * g - 24 * hr, d1 = d0 + 16;
    const unsigned zb = C.lb + L2_ZS;
    const f32x4 a = *LDSP(f32x4, d0 >= 0 && d0 < 24 ? zb + (hr * 24 + d0) * 4 : zb + 96 * 4);
    const f32x4 b = *LDSP(f32x4, d1 >= 0 && d1 < 24 ? zb + (hr * 24 + d1) * 4 : zb + 96 * 4);
    S.zf[I] = u32x4{pack2(a[0], a[1]), pack2(a[2], a[3]), pack2(b[0], b[1]), pack2(b[2], b[3])};
    PIN(S.zf[I]);
  } else if constexpr (I == 3) {
    const f32x4 z4 = {0.f, 0.f, 0.f, 0.f};
#pragma unroll
    for (int T = 0; T < 2; ++T) {
      S.den[T] = z4;
#pragma unroll
      for (int m = 0; m < 3; ++m)
        S.den[T] = mfma16(__builtin_bit_cast(s16x8, S.zf[m]), __builtin_bit_cast(s16x8, S.ha[m][T]), S.den[T]);
    }
  } else if constexpr (I == 4) {
#pragma unroll
    for (int T = 0; T < 2; ++T) {
      f32x4 v;
#pragma unroll
      for (int h = 0; h < 4; ++h) v[h] = frcp(S.den[T][h] + 1e-6f);
      PIN(v);
      S.den[T] = v;
    }
  } else {
    constexpr int m = (I - 5) >> 1, T = (I - 5) & 1;
    const f32x4 inv = S.den[T];
    const float f0 = m == 0 ? inv[0] : m == 1 ? inv[1] : (g < 2 ? inv[2] : inv[3]);
    const float f1 = m == 0 ? (g < 2 ? inv[0] : inv[1]) : m == 1 ? inv[2] : inv[3];
    u32x4 v = S.ha[m][T];
#pragma unroll
    for (int e = 0; e < 4; ++e) {
      const float f = e < 2 ? f0 : f1;
      v[e] = pack2(bf_lo(v[e]) * f, bf_hi(v[e]) * f);
    }
    PIN(v);
    S.ha[m][T] = v;
  }
}
template <int M> DI void k2_step(K2S& S, const K2C& C) {
  constexpr int T = k2_T(M), c = k2_c(M);
  constexpr bool seq = M < 153;
  const s16x8 wf = __builtin_shufflevector(S.wlo[M % 6], S.whi[M % 6], 0, 1, 2, 3, 4, 5, 6, 7);
  const s16x8 h0 = __builtin_bit_cast(s16x8, k2_att(T) ? S.ha[k2_att(T) ? T - 18 : 0][0] : S.hr[T % (K2D + 1)][0]);
  const s16x8 h1 = __builtin_bit_cast(s16x8, k2_att(T) ? S.ha[k2_att(T) ? T - 18 : 0][1] : S.hr[T % (K2D + 1)][1]);
  const f32x4 z4 = {0.f, 0.f, 0.f, 0.f};
  S.acc[c][0] = mfma16(wf, h0, T == 0 ? z4 : S.acc[c][0]);
  SB();
  k2_read<M + 5, 0>(S, C);
  SB();
  S.acc[c][1] = mfma16(wf, h1, T == 0 ? z4 : S.acc[c][1]);
  SB();
  k2_read<M + 5, 1>(S, C);
  if constexpr (seq) {
    if constexpr (c == 0) k2_issue<T + K2D, 0>(S, C);
    if constexpr (c == 2) k2_issue<T + K2D, 1>(S, C);
    if constexpr (c == 5) k2_issue<T + K2D, 2>(S, C);
    if constexpr (c == 7) k2_issue<T + K2D, 3>(S, C);
    if constexpr (T == 12 && (c < 3 || c == 4 || c == 6 || c == 8)) {
      constexpr int i = c < 3 ? c : (c + 2) / 2;
      bload16_sc1<(i & 1) * 1024>(S.ha[i >> 1][i & 1], C.hvoff, C.rsH, C.hsoA + (i >> 1) * 131072u);
    }
    if constexpr (T == 3 && c == 4) {
      if (C.w == 0) asm volatile("global_load_dword %0, %1, off sc1" : "=v"(S.flag) : "v"(C.chf));
    }
    if constexpr (T == 5 && c == 3) {
      if (C.w == 0) {
        asm volatile("s_waitcnt vmcnt(%1)" : "+v"(S.flag) : "i"(k2_younger(16 * 3 + 4, 5)));
        if (rfl(S.flag) < 4u) ctr_wait(C.chf, 4u);
      }
    }
    if constexpr (c == 3 && T < 14) k2_wait<T + 1, k2_count(T)>(S);
    if constexpr (c == 3 && T == 0) { if (threadIdx.x == 0) ctr_add(C.chf); }
    if constexpr (c == 2 && T == 14)
      asm volatile("s_waitcnt vmcnt(0)"
                   : "+v"(S.ha[0][0]), "+v"(S.ha[0][1]), "+v"(S.ha[1][0]), "+v"(S.ha[1][1]), "+v"(S.ha[2][0]), "+v"(S.ha[2][1]),
                     "+v"(S.hr[15 % (K2D + 1)][0]), "+v"(S.hr[15 % (K2D + 1)][1]), "+v"(S.hr[16 % (K2D + 1)][0]), "+v"(S.hr[16 % (K2D + 1)][1]),
                     "+v"(S.hr[17 % (K2D + 1)][0]), "+v"(S.hr[17 % (K2D + 1)][1]));
    if constexpr (c == 3 && (T == 14 || T == 16)) asm volatile("s_barrier" ::: "memory");
    {
      constexpr int j0 = T >= 15 ? (c == 1 ? 0 : c == 4 ? 1 : c == 6 ? 2 : -1) : T == 14 ? (c == 4 ? 0 : c == 6 ? 1 : -1) : (c == 4 ? 0 : -1);
      constexpr int j1 = T >= 15 ? (c == 2 ? 0 : c == 5 ? 1 : c == 7 ? 2 : -1) : T == 14 ? (c == 5 ? 0 : c == 7 ? 1 : -1) : (c == 5 ? 0 : -1);
      constexpr int base = T >= 11 && T <= 13 ? T - 11 : T == 14 ? 3 : T == 15 ? 5 : T == 16 ? 8 : -1;
      if constexpr (base >= 0 && j0 >= 0) { if (C.w < 4) k2_norm<base >= 0 && j0 >= 0 ? base + j0 : 0>(S, C); }
      if constexpr (base >= 0 && j1 >= 0) { if (C.w >= 4) k2_norm<base >= 0 && j1 >= 0 ? base + j1 : 0>(S, C); }
    }
  } else if constexpr (T == 20 && c > 0) {
    k2_send<c - 1>(S, C);
  }
  SB();
  if constexpr (seq && c == 8 && T % 5 == 4) { STAMP(S.dg, 18 + T / 5) }
  if constexpr (M + 1 < 189) k2_step<M + 1>(S, C);
}

__global__ __launch_bounds__(512, 2) void kf_main(KFP p) {
  __shared__ __attribute__((aligned(128))) char smem[L1_TOTAL];
  const unsigned lb = (unsigned)(size_t)(__attribute__((address_space(3))) char*)smem;
  const int tid = threadIdx.x, l = tid & 63;
  const int w = rfl(tid >> 6);
  const int b = blockIdx.x, xc = b & 7, r = b >> 3;
  const int cr = r & 3, j = 4 * xc + cr, ib = r >> 2;
  const int g = l >> 4, q = (l & 15) >> 2, pp = l & 3, c16 = l & 15;

  STAMPR(p.dg, 30) STAMP(p.dg, 0)
  const rsrc_t rsX = mk_rsrc(p.xf, (unsigned)SZ_XF);
  const rsrc_t rsQ = mk_rsrc(p.wq, (unsigned)SZ_WQ);
  const rsrc_t rsG = mk_rsrc(p.wg, D_ * F_ * 2);
  const rsrc_t rsU = mk_rsrc(p.wu, D_ * F_ * 2);
  const rsrc_t rsH = mk_rsrc(p.hf, (unsigned)SZ_HF);
  gu32* const ctr = (gu32*)p.ctr;
  K1S S;

#pragma unroll
  for (int i = 0; i < 9; ++i) glds16(lb + L1_BUFA + (9 * w + i) * 1024u, l * 16u, rsQ, (j * 72u + 9 * w + i) * 1024u);
  const int T0 = 16 * ib + 2 * w;
  const unsigned hvoff = l * 16u, xsb = T0 * 18432u;
#define XL(s) bload16<((s) & 3) * 1024>(S.xf[0][s], hvoff, rsX, xsb + ((s) >> 2) * 4096u); \
              bload16<((s) & 3) * 1024>(S.xf[1][s], hvoff, rsX, xsb + 18432u + ((s) >> 2) * 4096u);
  XL(0) XL(1) XL(2) XL(3) XL(4) XL(5)
#undef XL
  K1G G;
  const unsigned wcol = (576u * xc + 128u * cr) * 2u;
  {
    const unsigned rowv = (l >> 3) * 9216u, c8 = l & 7, s0 = (l >> 4) & 1;
    const unsigned vA = rowv + ((c8 ^ (s0 << 1)) << 4), vB = rowv + ((c8 ^ ((s0 | 2) << 1)) << 4);
    G.vo0 = (w & 1) ? vB : vA;
    G.vo1 = (w & 1) ? vA : vB;
  }
  G.sstep = 8u * 9216u;
  const unsigned wsoff = 72u * w * 9216u, wdst = lb + 72u * w * 128u;
  G.rs = rsG; G.soff = wsoff + wcol; G.dst = wdst + L1_BUFB;

  STAMP(p.dg, 1)
  wait_bar<12>();
  STAMP(p.dg, 2)

  {
    const unsigned qa = lb + L1_BUFA + l * 16u;
    const f32x4 z4 = {0.f, 0.f, 0.f, 0.f};
    f32x4 Q[4][2];
#pragma unroll
    for (int X = 0; X < 4; ++X) Q[X][0] = Q[X][1] = z4;
    s16x8 wq[2][4];
#pragma unroll
    for (int X = 0; X < 4; ++X) wq[0][X] = *LDSP(s16x8, qa + X * 1024u);
    k1_qkv_step<0>(S, wq, Q, qa, hvoff, rsX, xsb, G);
    STAMP(p.dg, 3)
    unsigned fKA[4], fKB[4], fVA[4], fVB[4], fQ2[4];
#pragma unroll
    for (int T = 0; T < 2; ++T)
#pragma unroll
      for (int h2 = 0; h2 < 2; ++h2) {
        fKA[2 * T + h2] = pack2(phi_f(Q[0][T][2 * h2]), phi_f(Q[0][T][2 * h2 + 1]));
        fKB[2 * T + h2] = pack2(phi_f(Q[1][T][2 * h2]), phi_f(Q[1][T][2 * h2 + 1]));
        fVA[2 * T + h2] = pack2(Q[2][T][2 * h2], Q[2][T][2 * h2 + 1]);
        const unsigned raw = pack2(Q[3][T][2 * h2], Q[3][T][2 * h2 + 1]);
        fVB[2 * T + h2] = c16 < 2 ? 0x3f803f80u : raw;
        fQ2[2 * T + h2] = pack2(phi_f(Q[3][T][2 * h2]), phi_f(Q[3][T][2 * h2 + 1]));
      }
    const s16x8 aKA = mk_frag(fKA[0], fKA[1], fKA[2], fKA[3]), aKB = mk_frag(fKB[0], fKB[1], fKB[2], fKB[3]);
    const s16x8 bVA = mk_frag(fVA[0], fVA[1], fVA[2], fVA[3]), bVB = mk_frag(fVB[0], fVB[1], fVB[2], fVB[3]);
    const s16x8 aQ2 = mk_frag(fQ2[0], fQ2[1], fQ2[2], fQ2[3]);
    const f32x4 R0 = mfma16(aKA, bVA, z4), R1 = mfma16(aKA, bVB, z4);
    const f32x4 R2 = mfma16(aKB, bVA, z4), R3 = mfma16(aKB, bVB, z4);
    {
      const unsigned one = (c16 >> 2) == g ? (0x3f80u << (16 * (c16 & 1))) : 0u;
      const unsigned i0 = (c16 & 2) ? 0u : one, i1 = (c16 & 2) ? one : 0u;
      const int piA = 6 * cr + g - 1, piB = 6 * cr + g + 2;
      const unsigned base = (KS_FFN + 3u * xc) * 131072u + T0 * 1024u + c16 * 16u;
      const unsigned oA = g == 0 ? 0xf0000000u : base + (piA >> 3) * 131072u + (piA & 3) * 256u + ((piA >> 2) & 1) * 8u;
      const unsigned oB = g == 0 ? 0xf0000000u : base + (piB >> 3) * 131072u + (piB & 3) * 256u + ((piB >> 2) & 1) * 8u;
#pragma unroll
      for (int T = 0; T < 2; ++T) {
        const s16x8 id = T == 0 ? mk_frag(i0, i1, 0u, 0u) : mk_frag(0u, 0u, i0, i1);
        const f32x4 qa4 = mfma16(aKB, id, z4), qb4 = mfma16(aQ2, id, z4);
        const u32x2 va = {pack2(qa4[0], qa4[1]), pack2(qa4[2], qa4[3])};
        const unsigned b01 = pack2(qb4[0], qb4[1]), b23 = pack2(qb4[2], qb4[3]);
        const u32x2 vb = {g == 1 ? b01 : (g == 2 ? b23 : 0u), g == 1 ? b23 : 0u};
        __builtin_amdgcn_raw_buffer_store_b64(va, rsH, oA + T * 1024u, 0, AUX_SC1);
        __builtin_amdgcn_raw_buffer_store_b64(vb, rsH, oB + T * 1024u, 0, AUX_SC1);
      }
    }
    {
      const unsigned sb = lb + L1_SCR + w * 1408u;
      const int eB = c16 == 0 ? 18 : c16 + 14;
      const bool okB = c16 == 0 || c16 == 2 || c16 == 3;
      const unsigned dummy = lb + L1_SCR + 8 * 1408u + l * 4u;
#pragma unroll
      for (int rr = 0; rr < 4; ++rr) {
        const int d = 4 * g + rr;
        *LDSP(float, sb + (d * 19 + c16) * 4) = R0[rr];
        *LDSP(float, okB ? sb + (d * 19 + eB) * 4 : dummy) = R1[rr];
        if (rr >= 2) {
          *LDSP(float, g == 0 ? sb + ((14 + rr) * 19 + c16) * 4 : dummy) = R2[rr];
          *LDSP(float, g == 0 && okB ? sb + ((14 + rr) * 19 + eB) * 4 : dummy) = R3[rr];
        }
      }
    }
  }
  STAMP(p.dg, 4)
  wait_bar<4>();
  STAMP(p.dg, 5)

  const unsigned lane0 = lb + (8 * g + q) * 128u + 8u * pp + ((((q >> 1) & 1) | ((g & 1) << 1)) << 5);
  const unsigned abA = lane0 + L1_BUFA, abB = lane0 + L1_BUFB;
  const unsigned ks0 = 18u * xc + 4u * cr;
  const unsigned hs0 = (ks0 * 128u + T0) * 1024u;

  k1_first_reads<4096>(S, abB);
  const rsrc_t rsKV = mk_rsrc(p.kv, (unsigned)SZ_KV);
  if (tid < 88) {
    f32x4 a = *LDSP(f32x4, lb + L1_SCR + tid * 16);
#pragma unroll
    for (int ww = 1; ww < 8; ++ww) a += *LDSP(f32x4, lb + L1_SCR + ww * 1408u + tid * 16);
    bstore16_sc1<0>(__builtin_bit_cast(u32x4, a), tid * 16u, rsKV, (j * 8 + ib) * 1408u);
  }
  SB();
  G.wm0 = lb + L1_SCR + 15872u;
  G.wvo = l < 9 ? (72u * ib + 9u * w + l) * 9216u + wcol + 128u : 0xf0000000u;
  G.wrs = rsG;
  gu32* const ckv = ctr + (CT_KV + xc) * 32;
  const unsigned gvo0 = G.vo0, gvo1 = G.vo1;
  G.rs = rsU; G.soff = wsoff + wcol; G.dst = wdst + L1_BUFA;
  gu32* const chA = ctr + (CT_H + xc * 8 + ib) * 32;
  k1_step<1, 0, 4, 4096, 4096, 1, 1, 0>(S, abB, abA, G, rsH, hvoff, 0, 0, ckv, chA);
  STAMP(p.dg, 6)
  G.rs = (w >> 2) ? rsU : rsG;
  G.vo0 = G.vo1 = (l >> 2) * 9216u + (((l & 3) ^ ((l >> 5) << 1)) * 16u);
  G.sstep = 16u * 9216u;
  G.soff = 144u * (w & 3) * 9216u + (576u * xc + 512u + 32u * (cr >> 1)) * 2u;
  G.dst = lb + L1_BUFB + (w >> 2) * 36864u + 144u * (w & 3) * 64u;
  const unsigned abS = lb + L1_BUFB + (8 * g + q) * 64u + 8u * pp + (((cr & 1) ^ (g & 1)) << 5);
  k1_step<2, 1, 4, 4096, 2048, 1, 0, 0>(S, abA, abS, G, rsH, hvoff, hs0, 0, ckv, chA);
  STAMP(p.dg, 8)
  G.rs = rsG; G.vo0 = gvo0; G.vo1 = gvo1; G.sstep = 8u * 9216u; G.soff = wsoff + wcol + 128u; G.dst = wdst + L1_BUFA;
  k1_step<0, 2, 2, 2048, 4096, 1, 0, 0>(S, abS, abA, G, rsH, hvoff, 0, hs0 + 131072u, ckv, chA);
  STAMP(p.dg, 10)
  G.rs = rsU; G.soff = wsoff + wcol + 128u; G.dst = wdst + L1_BUFB;
  k1_step<1, 3, 4, 4096, 4096, 1, 2, 0>(S, abA, abB, G, rsH, hvoff, 0,
                                        ((18u * xc + 16u + (cr >> 1)) * 128u + T0) * 1024u + (cr & 1) * 8u, ckv, chA);
  STAMP(p.dg, 12)
  const rsrc_t rsW = mk_rsrc(p.wd, F_ * D_ * 2);
  const unsigned wsoF = 18u * xc * 36864u + cr * 288u;
  G.rs = w < 4 ? rsW : mk_rsrc(p.wo, D_ * D_ * 2);
  G.soff = w < 4 ? wsoF + (4 * (w >> 1) + (w & 1)) * 36864u : 18u * (4 * xc + w - 4) * 1152u + cr * 288u;
  G.dst = lb + L1_BUFA + w * L2_SLOT;
  G.rsk = rsKV;
  { const unsigned hh = tid / 88, q4 = tid - 88 * hh; G.vok = tid < 352 ? ((4 * xc + hh) * 8 * 352 + q4 * 4) * 4u : 0xf0000000u; }
  k1_step<2, 1, 4, 4096, 0, 2, 0, 0>(S, abB, 0, G, rsH, hvoff, hs0 + 2 * 131072u, 0, ckv, chA);
  asm volatile("s_waitcnt vmcnt(2)" : "+v"(S.kp[0]), "+v"(S.kp[1]), "+v"(S.kp[2]), "+v"(S.kp[3]), "+v"(S.kp[4]), "+v"(S.kp[5]), "+v"(S.kp[6]), "+v"(S.kp[7]));
  {
    f32x4 a = __builtin_bit_cast(f32x4, S.kp[0]);
#pragma unroll
    for (int i = 1; i < 8; ++i) a += __builtin_bit_cast(f32x4, S.kp[i]);
    if (tid < 352) *LDSP(f32x4, lb + L2_KV + tid * 16u) = a;
  }
  if (w == 0) ctr_wait(chA, 4u);
  k1_epi_all<2, 3, 1>(S);
  k1_store_pair(S, rsH, hvoff, hs0 + 3 * 131072u);
  STAMP(p.dg, 14) STAMPR(p.dg, 27)
  asm volatile("s_waitcnt lgkmcnt(0)\n\ts_barrier" ::: "memory");
  STAMP(p.dg, 15)

  K2S S2;
  K2C C;
  C.rsH = rsH; C.rsW = rsW; C.w = w; C.sp = xc; C.lb = lb;
  C.rsB = mk_rsrc(p.xb, (unsigned)SZ_XB);
  C.xso = (((ib * 4 + cr) * 8 + w) * 8 + xc) * 9216u;
  { const int ci = 64 * w + l, rho = ci / 18, c = ci - 18 * rho; C.vo0 = rho * 1152u + c * 16u; }
  { const int ci = 512 + l, rho = ci / 18, c = ci - 18 * rho; C.vo1 = rho * 1152u + c * 16u; }
  C.hvoff = hvoff;
  const unsigned hT = T0 * 1024u;
  C.hsoF = hT + 18u * xc * 131072u;
  C.wsoF = wsoF;
  C.lanec = lb + (4 * g + q) * 288u + 8u * pp;

  k2_issue<0, 2>(S2, C); k2_issue<0, 3>(S2, C); k2_issue<1, 2>(S2, C); k2_issue<1, 3>(S2, C);
  k2_issue<4, 0>(S2, C); k2_issue<4, 1>(S2, C);
  STAMP(p.dg, 16)
  if (tid < 100) {
    const int hh = tid / 24, d = tid - 24 * hh;
    const float t = *LDSP(float, lb + L2_KV + ((tid < 96 ? hh : 0) * 352 + (d < 18 ? d * 19 + 18 : 0)) * 4);
    *LDSP(float, lb + L2_ZS + tid * 4) = (tid < 96 && d < 18) ? t : 0.f;
  }
  const f32x4 z4 = {0.f, 0.f, 0.f, 0.f};
  {
    const int hh = w >> 1, d = 16 * (w & 1) + c16;
    const unsigned ao = lb + L2_WO + hh * L2_SLOT + (8 * g + q) * 288u + 8u * pp;
    float v[8];
    const unsigned ka0 = lb + L2_KV + (hh * 352 + (d < 18 ? d : 0) * 19 + 8 * (g < 2 ? g : 2)) * 4;
#pragma unroll
    for (int e = 0; e < 8; ++e) v[e] = *LDSP(float, ka0 + e * 4);
    s16x8 A[9];
#pragma unroll
    for (int c = 0; c < 9; ++c) A[c] = trfrag(ao + c * 32, ao + c * 32 + 4 * 288);
    unsigned bh[4], bl[4];
#pragma unroll
    for (int e2 = 0; e2 < 4; ++e2) {
      const float v0 = (d < 18 && 8 * g + 2 * e2 < 18) ? v[2 * e2] : 0.f, v1 = (d < 18 && 8 * g + 2 * e2 + 1 < 18) ? v[2 * e2 + 1] : 0.f;
      bh[e2] = pack2(v0, v1);
      bl[e2] = pack2(v0 - bf_lo(bh[e2]), v1 - bf_hi(bh[e2]));
    }
    const s16x8 Bh = mk_frag(bh[0], bh[1], bh[2], bh[3]), Bl = mk_frag(bl[0], bl[1], bl[2], bl[3]);
    const int ka = 24 * hh + d;
    const unsigned mo = d < 24 ? lb + L2_MT + (ka >> 5) * L2_SLOT + (ka & 31) * 288u + 8u * g : lb + k2_slot(L2_NS - 1) + l * 8u;
    f32x4 D[9];
#pragma unroll
    for (int c = 0; c < 9; ++c) D[c] = mfma16(A[c], Bh, z4);
#pragma unroll
    for (int c = 0; c < 9; ++c) D[c] = mfma16(A[c], Bl, D[c]);
#pragma unroll
    for (int c = 0; c < 9; ++c) *LDSP(u32x2, mo + c * 32) = u32x2{pack2(D[c][0], D[c][1]), pack2(D[c][2], D[c][3])};
  }
  SB();
  SB();
  k2_issue<2, 2>(S2, C); k2_issue<2, 3>(S2, C); k2_issue<3, 2>(S2, C); k2_issue<3, 3>(S2, C);
  k2_issue<4, 2>(S2, C); k2_issue<4, 3>(S2, C);
  asm volatile("s_waitcnt lgkmcnt(0)\n\ts_barrier" ::: "memory");
  C.chf = ctr + (CT_H2 + xc * 8 + ib) * 32;
  STAMP(p.dg, 17)
  C.hsoA = hT + (KS_FFN + 3u * xc) * 131072u;
  asm volatile("s_waitcnt vmcnt(9)" : "+v"(S2.hr[0][0]), "+v"(S2.hr[0][1]));
  k2_read<0, 0>(S2, C); k2_read<0, 1>(S2, C); k2_read<1, 0>(S2, C); k2_read<1, 1>(S2, C); k2_read<2, 0>(S2, C);
  k2_read<2, 1>(S2, C); k2_read<3, 0>(S2, C); k2_read<3, 1>(S2, C); k2_read<4, 0>(S2, C); k2_read<4, 1>(S2, C);
  SB();
  k2_step<0>(S2, C);
  STAMP(p.dg, 23) STAMPR(p.dg, 28)

  const int sp = xc, ni = cr, mi = ib;
  const int grp = mi * 4 + ni;
  const rsrc_t rsB = C.rsB;
  k2_send<8>(S2, C);
  const int tok0 = 256 * mi + 32 * sp;
  u32x4 xr[2];
  size_t xo[2];
#pragma unroll
  for (int i = 0; i < 2; ++i) {
    const int it = tid + 512 * i, itc = it < 576 ? it : 0, tk = itc / 18, ch = itc - tk * 18;
    xo[i] = (size_t)(tok0 + tk) * D_ + 144 * ni + 8 * ch;
    xr[i] = *(const u32x4*)((const uint16_t*)p.x + xo[i]);
  }
  asm volatile("s_waitcnt vmcnt(0) lgkmcnt(0)\n\ts_barrier" ::: "memory");
  STAMP(p.dg, 24)
  if (w == 0) {
    gu32* cx = ctr + (CT_X + grp * 8) * 32;
    if (l < 8) ctr_add(cx + l * 32);
    ctr_wait(cx + sp * 32, 8u);
  }
  asm volatile("s_waitcnt vmcnt(0)\n\ts_barrier" ::: "memory");
  STAMP(p.dg, 25) STAMPR(p.dg, 29)
#pragma unroll
  for (int i = 0; i < 2; ++i) {
    const int c = w + 8 * i;
    if (c < 9) {
      u32x4 v[8];
#pragma unroll
      for (int src = 0; src < 8; ++src)
        v[src] = __builtin_amdgcn_raw_buffer_load_b128(rsB, src == sp ? 0xf0000000u : hvoff + c * 1024u, ((grp * 8 + sp) * 8 + src) * 9216u, AUX_SC1);
      f32x4 a0 = *LDSP(f32x4, lb + L2_OWN + (2 * c) * 1024u + l * 16u), a1 = *LDSP(f32x4, lb + L2_OWN + (2 * c + 1) * 1024u + l * 16u);
#pragma unroll
      for (int src = 0; src < 8; ++src) {
        a0 += f32x4{bf_lo(v[src][0]), bf_hi(v[src][0]), bf_lo(v[src][1]), bf_hi(v[src][1])};
        a1 += f32x4{bf_lo(v[src][2]), bf_hi(v[src][2]), bf_lo(v[src][3]), bf_hi(v[src][3])};
      }
      *LDSP(f32x4, lb + L2_OUT + (c16 * 148 + 16 * c + 4 * g) * 4) = a0;
      *LDSP(f32x4, lb + L2_OUT + ((16 + c16) * 148 + 16 * c + 4 * g) * 4) = a1;
    }
  }
  __syncthreads();
#pragma unroll
  for (int i = 0; i < 2; ++i) {
    const int it = tid + 512 * i;
    if (it < 576) {
      const int tk = it / 18, ch = it - tk * 18;
      const f32x4 a = *LDSP(f32x4, lb + L2_OUT + (tk * 148 + 8 * ch) * 4), c = *LDSP(f32x4, lb + L2_OUT + (tk * 148 + 8 * ch + 4) * 4);
      u32x4 ov;
      ov[0] = pack2(a[0] + bf_lo(xr[i][0]), a[1] + bf_hi(xr[i][0]));
      ov[1] = pack2(a[2] + bf_lo(xr[i][1]), a[3] + bf_hi(xr[i][1]));
      ov[2] = pack2(c[0] + bf_lo(xr[i][2]), c[1] + bf_hi(xr[i][2]));
      ov[3] = pack2(c[2] + bf_lo(xr[i][3]), c[3] + bf_hi(xr[i][3]));
      *(u32x4*)((uint16_t*)p.out + xo[i]) = ov;
    }
  }
  STAMP(p.dg, 26) STAMPR(p.dg, 31) STAMP_DUMP(p.dg)
}

extern "C" void kernel_launch(void* const* d_in, const int* in_sizes, int n_in, void* d_out, int out_size,
                              void* d_ws, size_t ws_size, hipStream_t stream) {
  const uint16_t* x = (const uint16_t*)d_in[0];
  const void* wqkv = d_in[1];
  const uint16_t* wo = (const uint16_t*)d_in[2];
  const void* wg = d_in[4];
  const void* wu = d_in[5];
  const void* wd = d_in[6];
  char* ws = (char*)d_ws;

  k0_ln<<<256, 512, 0, stream>>>(x, (u32x4*)(ws + OFF_XF), (const uint16_t*)wqkv, (u32x4*)(ws + OFF_WQ),
                                    (unsigned*)(ws + OFF_CT));
  KFP p;
  p.xf = ws + OFF_XF; p.wq = ws + OFF_WQ; p.wg = wg; p.wu = wu; p.wd = wd; p.wo = wo; p.x = x;
  p.hf = ws + OFF_HF; p.kv = ws + OFF_KV; p.xb = ws + OFF_XB; p.ctr = ws + OFF_CT; p.out = d_out;
  kf_main<<<256, 512, 0, stream>>>(p);
}
```

```cpp
#include <hip/hip_runtime.h>
#include <hip/hip_bf16.h>
#include <stdint.h>

#define DI __device__ __forceinline__
typedef __attribute__((ext_vector_type(8))) short s16x8;
typedef __attribute__((ext_vector_type(4))) short s16x4;
typedef __attribute__((ext_vector_type(4))) float f32x4;
typedef __attribute__((ext_vector_type(4))) unsigned u32x4;
typedef __attribute__((ext_vector_type(2))) unsigned u32x2;
typedef __amdgpu_buffer_rsrc_t rsrc_t;
#define LDSP(T, a) ((__attribute__((address_space(3))) T*)(size_t)(a))

constexpr int S_ = 2048, D_ = 576, F_ = 4608, NH = 32, DH = 18;
constexpr int KS_FFN = 144, KS_ALL = 168;
constexpr size_t OFF_XF = 0;
constexpr size_t SZ_XF = (size_t)128 * 18 * 1024;
constexpr size_t OFF_WQ = OFF_XF + SZ_XF;
constexpr size_t SZ_WQ = (size_t)32 * 72 * 1024;
constexpr size_t OFF_HF = OFF_WQ + SZ_WQ;
constexpr size_t SZ_HF = (size_t)KS_ALL * 128 * 1024;
constexpr size_t OFF_KV = OFF_HF + SZ_HF;
constexpr size_t SZ_KV = (size_t)32 * 8 * 352 * 4;
constexpr size_t OFF_XB = OFF_KV + SZ_KV;
constexpr size_t SZ_XB = (size_t)32 * 8 * 8 * 9 * 1024;
constexpr size_t OFF_PQ = OFF_XB + SZ_XB;
constexpr size_t SZ_PQ = (size_t)256 * 8 * 2 * 1024;
constexpr size_t OFF_CT = OFF_PQ + SZ_PQ;
constexpr int CT_X = 0, CT_KV = 256, CT_H = 264, CT_H2 = 328, CT_N = 400;
constexpr size_t SZ_CT = (size_t)CT_N * 128;

DI rsrc_t mk_rsrc(const void* p, unsigned bytes) {
  return __builtin_amdgcn_make_buffer_rsrc((void*)p, (short)0, (int)bytes, 0x00020000);
}
DI unsigned rfl(unsigned v) { return __builtin_amdgcn_readfirstlane(v); }
DI float bf_lo(unsigned u) { return __uint_as_float(u << 16); }
DI float bf_hi(unsigned u) { return __uint_as_float(u & 0xffff0000u); }
DI unsigned pack2(float lo, float hi) {
  typedef __attribute__((ext_vector_type(2))) __bf16 bf2;
  typedef __attribute__((ext_vector_type(2))) float f2;
  f2 v = {lo, hi};
  bf2 r = __builtin_convertvector(v, bf2);
  return __builtin_bit_cast(unsigned, r);
}
DI s16x8 mk_frag(unsigned a, unsigned b, unsigned c, unsigned d) {
  u32x4 v = {a, b, c, d};
  return __builtin_bit_cast(s16x8, v);
}
DI f32x4 mfma16(s16x8 a, s16x8 b, f32x4 c) { return __builtin_amdgcn_mfma_f32_16x16x32_bf16(a, b, c, 0, 0, 0); }
DI s16x4 trread(unsigned addr) { return __builtin_amdgcn_ds_read_tr16_b64_v4i16(LDSP(s16x4, addr)); }
DI s16x8 trfrag(unsigned a0, unsigned a1) {
  s16x4 lo = trread(a0), hi = trread(a1);
  return __builtin_shufflevector(lo, hi, 0, 1, 2, 3, 4, 5, 6, 7);
}
template <int ROLE = 0> DI void glds16(unsigned m0v, unsigned voff, rsrc_t rs, unsigned soff) {
  asm volatile("s_mov_b32 m0, %0\n\ts_nop 0\n\tbuffer_load_dwordx4 %1, %2, %3 offen lds ; role %4"
               :: "s"(m0v), "v"(voff), "s"(rs), "s"(soff), "i"(ROLE) : "memory");
}
DI void warm_lines(unsigned m0v, unsigned voff, rsrc_t rs) {
  asm volatile("s_mov_b32 m0, %0\n\ts_nop 0\n\tbuffer_load_dword %1, %2, 0 offen lds" :: "s"(m0v), "v"(voff), "s"(rs) : "memory");
}
template <int IMM> DI void bload16(u32x4& d, unsigned voff, rsrc_t rs, unsigned soff) {
  asm volatile("buffer_load_dwordx4 %0, %1, %2, %3 offen offset:%4"
               : "=v"(d) : "v"(voff), "s"(rs), "s"(soff), "i"(IMM));
}
template <int IMM, int ROLE = 0> DI void bload16_sc1(u32x4& d, unsigned voff, rsrc_t rs, unsigned soff) {
  asm volatile("buffer_load_dwordx4 %0, %1, %2, %3 offen offset:%4 sc1 ; role %5"
               : "=v"(d) : "v"(voff), "s"(rs), "s"(soff), "i"(IMM), "i"(ROLE));
}
template <int IMM> DI void bstore16_sc1(u32x4 v, unsigned voff, rsrc_t rs, unsigned soff) {
  asm volatile("buffer_store_dwordx4 %0, %1, %2, %3 offen offset:%4 sc1\n\ts_nop 1"
               :: "v"(v), "v"(voff), "s"(rs), "s"(soff), "i"(IMM) : "memory");
}
typedef __attribute__((address_space(1))) unsigned gu32;
DI void ctr_add(gu32* c) { __hip_atomic_fetch_add(c, 1u, __ATOMIC_RELAXED, __HIP_MEMORY_SCOPE_AGENT); }
DI void ctr_wait(gu32* c, unsigned n) {
  for (unsigned spins = 0; spins < (1u << 20); ++spins) {
    if ((unsigned)__builtin_amdgcn_readfirstlane(__hip_atomic_load(c, __ATOMIC_RELAXED, __HIP_MEMORY_SCOPE_AGENT)) >= n) break;
    __builtin_amdgcn_s_sleep(1);
  }
}
template <int N> DI void wait_vm2(u32x4& a, u32x4& b) {
  asm volatile("s_waitcnt vmcnt(%2)" : "+v"(a), "+v"(b) : "i"(N));
}
template <int N> DI void wait_bar_nolgkm() {
  asm volatile("s_waitcnt vmcnt(%0)\n\ts_barrier" :: "i"(N) : "memory");
}
template <int N> DI void wait_bar() {
  asm volatile("s_waitcnt vmcnt(%0) lgkmcnt(0)\n\ts_barrier" :: "i"(N) : "memory");
}
DI float fexp2(float x) { return __builtin_amdgcn_exp2f(x); }
DI float frcp(float x) { return __builtin_amdgcn_rcpf(x); }
DI float phi_f(float t) { return (t > 0.f ? t + 1.f : fexp2(t * 1.4426950408889634f)) + 1e-6f; }
DI float geglu(float g, float u) {
  float e = fexp2(g * __builtin_fmaf(g * g, -0.10294323970f, -2.30220819814f));
  return (g * u) * frcp(1.f + e);
}
template <int CTRL> DI float dpp_add(float v) {
  return v + __builtin_bit_cast(float, __builtin_amdgcn_update_dpp(0, __builtin_bit_cast(int, v), CTRL, 0xf, 0xf, true));
}
DI float reduce32(float v) {
  v = dpp_add<0xB1>(v);
  v = dpp_add<0x4E>(v);
  v = dpp_add<0x141>(v);
  v = dpp_add<0x140>(v);
  const unsigned u = __float_as_uint(v);
  const auto r = __builtin_amdgcn_permlane16_swap(u, u, false, false);
  const unsigned r0 = r[0], r1 = r[1];
  return __uint_as_float(r0) + __uint_as_float(r1);
}

#define ABL2_NOW 0
#define ABL2_NOH 0
#define K0_WARM 0
#define STAGGER 0
#define NO_WARM 0
#define ABL_NOW 0
#define ABL_NOH 0
#define ABL_NOCOMP 0
#define ABL1_NOCOMP 0
#define ABL1_NOEPI 0
#define ABL1_NOSTAGE 0
#define STAMP(buf, slot)
#define STAMPR(buf, slot)
#define STAMP_DUMP(buf)
__global__ __launch_bounds__(768) void k0_ln(const uint16_t* __restrict__ x, u32x4* __restrict__ xf,
                                             const uint16_t* __restrict__ wqkv, u32x4* __restrict__ wq,
                                             unsigned* __restrict__ ctr, const unsigned* __restrict__ wg,
                                             const unsigned* __restrict__ wu, const unsigned* __restrict__ wd,
                                             const unsigned* __restrict__ wo) {
  const int l = threadIdx.x & 63, w = threadIdx.x >> 6;
  if (w >= 8) {
    const unsigned id = (blockIdx.x * 4 + (w - 8)) * 64 + l;
    unsigned v[2];
#pragma unroll
    for (int i = 0; i < 2; ++i) {
      const unsigned L = id + i * 65536u;
      const unsigned* base = L < 41472u ? wg : L < 82944u ? wu : L < 124416u ? wd : wo;
      const unsigned ln = L < 41472u ? L : L < 82944u ? L - 41472u : L < 124416u ? L - 82944u : (L < 129600u ? L - 124416u : 0u);
      v[i] = base[ln * 32u];
    }
    asm volatile("" :: "v"(v[0]), "v"(v[1]));
    return;
  }
  if (threadIdx.x < CT_N / 8) ctr[blockIdx.x * (CT_N / 8) + threadIdx.x] = 0u;
  if (w >= 4) {
    const int item = blockIdx.x + 256 * (w - 4);
    if (item >= 576) return;
    const int h = item / 18, s = item - 18 * h, g = l >> 4, c = l & 15;
    const int col[4] = {576 + c, c < 4 ? 576 + 14 + c : c - 4, 1152 + c,
                        c < 4 ? 1152 + 14 + c : (c < 8 ? c + 8 : 14 + (c & 3))};
    const uint16_t* src = wqkv + (size_t)(32 * s + 8 * g) * (3 * D_) + 18 * h;
    unsigned short v[4][8];
#pragma unroll
    for (int X = 0; X < 4; ++X)
#pragma unroll
      for (int e = 0; e < 8; ++e) v[X][e] = src[e * (3 * D_) + col[X]];
#pragma unroll
    for (int X = 0; X < 4; ++X) {
      u32x4 o;
#pragma unroll
      for (int e = 0; e < 4; ++e) o[e] = (unsigned)v[X][2 * e] | ((unsigned)v[X][2 * e + 1] << 16);
      wq[((size_t)item * 4 + X) * 64 + l] = o;
    }
    return;
  }
  const int tok = blockIdx.x * 8 + w * 2 + (l >> 5), p = l & 31;
  const u32x4* row = (const u32x4*)(x + (size_t)tok * D_);
  u32x4 c[3];
  c[0] = row[p];
  c[1] = row[p + 32];
  c[2] = row[p < 8 ? p + 64 : p];
  const bool has2 = p < 8;
  float v[24];
#pragma unroll
  for (int i = 0; i < 3; ++i)
#pragma unroll
    for (int j = 0; j < 4; ++j) { v[i * 8 + 2 * j] = bf_lo(c[i][j]); v[i * 8 + 2 * j + 1] = bf_hi(c[i][j]); }
  float s = 0.f, s2 = 0.f;
#pragma unroll
  for (int j = 0; j < 16; ++j) s += v[j];
#pragma unroll
  for (int j = 16; j < 24; ++j) s2 += v[j];
  s += has2 ? s2 : 0.f;
  const float mean = reduce32(s) * (1.f / 576.f);
  float q = 0.f, q2 = 0.f;
#pragma unroll
  for (int j = 0; j < 24; ++j) v[j] -= mean;
#pragma unroll
  for (int j = 0; j < 16; ++j) q += v[j] * v[j];
#pragma unroll
  for (int j = 16; j < 24; ++j) q2 += v[j] * v[j];
  q += has2 ? q2 : 0.f;
  const float var = reduce32(q) * (1.f / 576.f);
  const float rstd = 1.f / sqrtf(var + 1e-5f);
  const int T = tok >> 4, t16 = tok & 15;
#pragma unroll
  for (int i = 0; i < 3; ++i) {
    u32x4 o;
#pragma unroll
    for (int j = 0; j < 4; ++j) o[j] = pack2(v[i * 8 + 2 * j] * rstd, v[i * 8 + 2 * j + 1] * rstd);
    const int ch = p + 32 * i;
    if (i < 2 || has2) xf[((size_t)T * 18 + (ch >> 2)) * 64 + (ch & 3) * 16 + t16] = o;
  }
}

constexpr unsigned L1_BUFA = 0, L1_BUFB = 73728, L1_SCR = 147456, L1_TOTAL = 163840;
constexpr int AUX_SC1 = 16;
#define SB() __builtin_amdgcn_sched_barrier(0)
#define PIN(x) asm volatile("" : "+v"(x))

struct KFP {
  const void* xf; const void* wq; const void* wg; const void* wu; const void* wd; const void* wo; const void* x;
  void* hf; void* kv; void* xb; void* pq; void* ctr; void* out;
};

struct K1S {
  u32x4 xf[2][18];
  s16x4 wlo[6], whi[6];
  f32x4 acc[2][2];
  unsigned gel[4][2][2];
  u32x4 hq[2];
  float t0, t1;
  unsigned ard;
  u32x4 kp[8];
};
struct K1G { unsigned dst, soff, sstep, vo0, vo1, vok, wm0, wvo, wvo2; rsrc_t rs, rsk, wrs, wrs2; int w; };
template <int I, int ROLE = 0> DI void k1_piece(const K1G& G) {
  if (!ABL1_NOSTAGE) glds16<ROLE>(G.dst + I * 1024u, (I & 1) ? G.vo1 : G.vo0, G.rs, G.soff + I * G.sstep);
}
template <int I, int ROLE = 0> DI void k1_piece2(const K1G& G, int l) {
  const int ci = 64 * I + l, rho = ci / 18, c = ci - 18 * rho;
  glds16<ROLE>(G.dst + I * 1024u, rho * 1152u + c * 16u, G.rs, G.soff);
}
template <int STRIDE, int N, int H> DI void k1_read(K1S& S, unsigned a0, int slot_unused = 0) {
  constexpr int P = N / 18, KS = N % 18;
  if constexpr (H == 0 && KS == 0) { S.ard = STRIDE == 4096 ? (a0 ^ (P << 5)) : a0 + P * 36864u; PIN(S.ard); }
  if constexpr (H == 0 && KS == 9) { S.ard += 9 * STRIDE; PIN(S.ard); }
  if constexpr (H == 0) S.wlo[N % 6] = trread(S.ard + (KS % 9) * STRIDE);
  else S.whi[N % 6] = trread(S.ard + (KS % 9) * STRIDE + STRIDE / 8);
}
template <int KIND, int U, int A, int PR, int PH, int SL> DI void k1_epi(K1S& S) {
  constexpr int T = PR >> 1, R = 2 * (PR & 1);
  const float a = S.acc[A][T][R], b = S.acc[A][T][R + 1];
  if (ABL1_NOEPI) {
    if (PH == 3 && SL == 1) { if (KIND == 1) S.gel[U][T][PR & 1] = pack2(a, b); else S.hq[T][2 * (U & 1) + (PR & 1)] = pack2(a, b) ^ S.gel[U][T][PR & 1]; }
    return;
  }
  if constexpr (KIND == 1) {
    if constexpr (PH == 0 && SL == 0) { S.t0 = a * a; S.t1 = b * b; }
    if constexpr (PH == 0 && SL == 1) {
      S.t0 = __builtin_fmaf(S.t0, -0.10294323970f, -2.30220819814f);
      S.t1 = __builtin_fmaf(S.t1, -0.10294323970f, -2.30220819814f);
    }
    if constexpr (PH == 1 && SL == 0) { S.t0 *= a; S.t1 *= b; }
    if constexpr (PH == 1 && SL == 1) { S.t0 = fexp2(S.t0); S.t1 = fexp2(S.t1); }
    if constexpr (PH == 2 && SL == 0) { S.t0 += 1.f; S.t1 += 1.f; }
    if constexpr (PH == 2 && SL == 1) { S.t0 = frcp(S.t0); S.t1 = frcp(S.t1); }
    if constexpr (PH == 3 && SL == 0) { S.t0 *= a; S.t1 *= b; }
    if constexpr (PH == 3 && SL == 1) { unsigned v = pack2(S.t0, S.t1); PIN(v); S.gel[U][T][PR & 1] = v; }
    else { PIN(S.t0); PIN(S.t1); }
  } else {
    if constexpr (PH == 1 && SL == 0) { const unsigned u = S.gel[U][T][PR & 1]; S.t0 = bf_lo(u); S.t1 = bf_hi(u); PIN(S.t0); PIN(S.t1); }
    if constexpr (PH == 2 && SL == 0) { S.t0 *= a; S.t1 *= b; PIN(S.t0); PIN(S.t1); }
    if constexpr (PH == 3 && SL == 0) { unsigned v = pack2(S.t0, S.t1); PIN(v); S.hq[T][2 * (U & 1) + (PR & 1)] = v; }
  }
}
template <int KIND, int U, int A, int I = 0> DI void k1_epi_all(K1S& S) {
  if constexpr (I < 32) { k1_epi<KIND, U, A, I / 8, (I / 2) % 4, I % 2>(S); k1_epi_all<KIND, U, A, I + 1>(S); }
}
DI void k1_store_pair(K1S& S, rsrc_t rsH, unsigned voff, unsigned soff) {
  bstore16_sc1<0>(S.hq[0], voff, rsH, soff);
  bstore16_sc1<1024>(S.hq[1], voff, rsH, soff);
}
template <int KIND, int PREV, int NP, int STRIDE, int NSTRIDE, int STG, int SIG, int N>
DI void k1_step(K1S& S, unsigned ab, unsigned abn, const K1G& G, rsrc_t rsH, unsigned hvoff, unsigned hso, unsigned hsop,
                gu32* sig, gu32* sig2) {
  constexpr int P = N / 18, KS = N % 18, A = P & 1, NT = NP * 18;
  constexpr int EK = P == 0 ? (PREV == 3 ? 2 : PREV) : (NP == 2 ? 1 : KIND);
  constexpr int EU = P == 0 ? (PREV == 3 ? 0 : 3) : (NP == 2 ? 0 : P - 1);
  constexpr int EA = P == 0 ? 1 : (A ^ 1);
  constexpr bool EPI = EK != 0 && KS >= 1 && KS <= 16;
  const f32x4 z4 = {0.f, 0.f, 0.f, 0.f};
  const s16x8 wf = __builtin_shufflevector(S.wlo[N % 6], S.whi[N % 6], 0, 1, 2, 3, 4, 5, 6, 7);
  const s16x8 x0 = __builtin_bit_cast(s16x8, S.xf[0][KS]), x1 = __builtin_bit_cast(s16x8, S.xf[1][KS]);
  if (!ABL1_NOCOMP) S.acc[A][0] = mfma16(wf, x0, KS == 0 ? z4 : S.acc[A][0]);
  else if (KS == 0) { S.acc[A][0] = z4; S.acc[A][0][0] = (float)wf[0]; }
  SB();
  if constexpr (N + 5 < NT) k1_read<STRIDE, N + 5, 0>(S, ab);
  else if constexpr (NSTRIDE != 0) k1_read<NSTRIDE == 0 ? 4096 : NSTRIDE, N + 5 - NT, 0>(S, abn);
  if constexpr (EPI) k1_epi<EK, EU, EA, (KS - 1) / 4, (KS - 1) % 4, 0>(S);
  SB();
  if (!ABL1_NOCOMP) S.acc[A][1] = mfma16(wf, x1, KS == 0 ? z4 : S.acc[A][1]);
  else if (KS == 0) { S.acc[A][1] = z4; }
  SB();
  if constexpr (N + 5 < NT) k1_read<STRIDE, N + 5, 1>(S, ab);
  else if constexpr (NSTRIDE != 0) k1_read<NSTRIDE == 0 ? 4096 : NSTRIDE, N + 5 - NT, 1>(S, abn);
  if constexpr (EPI) k1_epi<EK, EU, EA, (KS - 1) / 4, (KS - 1) % 4, 1>(S);
  if constexpr (STG == 1 && NP == 4 && N % 6 == 2 && N / 6 < 9) { if (!STAGGER || G.w < 4) k1_piece<N / 6, STAGGER>(G); }
  if constexpr (STAGGER && STG == 1 && NP == 4 && N % 6 == 5 && N / 6 < 9) { if (G.w >= 4) k1_piece<N / 6, 2>(G); }
  if constexpr (STG == 1 && NP == 2 && N % 2 == 0 && N >= 2 && N / 2 <= 9) { if (!STAGGER || G.w < 4) k1_piece<N / 2 - 1, STAGGER>(G); }
  if constexpr (STAGGER && STG == 1 && NP == 2 && N % 2 == 1 && N >= 3 && N / 2 <= 9) { if (G.w >= 4) k1_piece<N / 2 - 1, 2>(G); }
  if constexpr (SIG == 1 && !NO_WARM && N == 56) warm_lines(G.wm0, G.wvo, G.wrs);
  if constexpr (SIG == 1 && !NO_WARM && N == 59) warm_lines(G.wm0, G.wvo2, G.wrs2);
  if constexpr (STG == 2 && N % 6 == 2 && N / 6 < 9) { if (!STAGGER || G.w < 4) k1_piece2<N / 6, STAGGER>(G, hvoff >> 4); }
  if constexpr (STAGGER && STG == 2 && N % 6 == 4 && N / 6 < 9) { if (G.w >= 4) k1_piece2<N / 6, 2>(G, hvoff >> 4); }
  if constexpr (STG == 2 && N % 6 == (STAGGER ? 0 : 5) && (N - (STAGGER ? 6 : 0)) / 6 < 8 && N >= 5)
    bload16_sc1<0>(S.kp[(N - (STAGGER ? 6 : 0)) / 6], G.vok, G.rsk, ((N - (STAGGER ? 6 : 0)) / 6) * 1408u);
  if constexpr (PREV == 2 && N == 17) k1_store_pair(S, rsH, hvoff, hsop);
  if constexpr (PREV == 3 && N == 17) {
#pragma unroll
    for (int T = 0; T < 2; ++T) {
      const u32x2 v = {S.hq[T][0], S.hq[T][1]};
      __builtin_amdgcn_raw_buffer_store_b64(v, rsH, hvoff + T * 1024u, hsop, AUX_SC1);
    }
  }
  if constexpr (KIND == 2 && NP == 4 && N == 2 * 18 + 17) k1_store_pair(S, rsH, hvoff, hso);
  if constexpr (N == 1) asm volatile("s_barrier" ::: "memory");
  if constexpr (SIG == 2 && N == NT - 8) { if (rfl(threadIdx.x >> 6) == 0) ctr_wait(sig, 32u); }
  if constexpr (NSTRIDE != 0 && N == NT - 8) wait_bar_nolgkm<KIND == 2 ? 2 : (SIG == 1 && !NO_WARM ? 2 : 0)>();
  if constexpr (SIG == 1 && N == NT - 8) { if (threadIdx.x == 0) ctr_add(sig); }
  if constexpr (SIG == 2 && N == NT - 8) { if (threadIdx.x == 0) ctr_add(sig2); }
  SB();
  if constexpr (N + 1 < NT) k1_step<KIND, PREV, NP, STRIDE, NSTRIDE, STG, SIG, N + 1>(S, ab, abn, G, rsH, hvoff, hso, hsop, sig, sig2);
}
template <int STRIDE> DI void k1_first_reads(K1S& S, unsigned ab) {
  k1_read<STRIDE, 0, 0>(S, ab); k1_read<STRIDE, 0, 1>(S, ab); k1_read<STRIDE, 1, 0>(S, ab); k1_read<STRIDE, 1, 1>(S, ab);
  k1_read<STRIDE, 2, 0>(S, ab); k1_read<STRIDE, 2, 1>(S, ab); k1_read<STRIDE, 3, 0>(S, ab); k1_read<STRIDE, 3, 1>(S, ab);
  k1_read<STRIDE, 4, 0>(S, ab); k1_read<STRIDE, 4, 1>(S, ab);
  SB();
}
template <int KS> DI void k1_qkv_step(K1S& S, s16x8 (&wq)[2][4], f32x4 (&Q)[4][2], unsigned qa, unsigned xv, rsrc_t rsX,
                                      unsigned xsb, const K1G& G) {
  if constexpr (KS + 6 < 18) {
    bload16<((KS + 6) & 3) * 1024>(S.xf[0][KS + 6], xv, rsX, xsb + ((KS + 6) >> 2) * 4096u);
    bload16<((KS + 6) & 3) * 1024>(S.xf[1][KS + 6], xv, rsX, xsb + 18432u + ((KS + 6) >> 2) * 4096u);
  } else if constexpr (KS < 15) { k1_piece<2 * (KS - 12)>(G); k1_piece<2 * (KS - 12) + 1>(G); }
  else k1_piece<KS - 9>(G);
  constexpr int YOUNGER = ABL1_NOSTAGE ? (KS < 12 ? 12 : 2 * (17 - KS)) : (KS < 15 ? 12 : 26 - KS);
  wait_vm2<YOUNGER>(S.xf[0][KS], S.xf[1][KS]);
  const s16x8 x0 = __builtin_bit_cast(s16x8, S.xf[0][KS]), x1 = __builtin_bit_cast(s16x8, S.xf[1][KS]);
  SB();
#pragma unroll
  for (int X = 0; X < 4; ++X) {
    Q[X][0] = mfma16(x0, wq[KS & 1][X], Q[X][0]);
    SB();
    if constexpr (KS + 1 < 18) wq[(KS + 1) & 1][X] = *LDSP(s16x8, qa + ((KS + 1) * 4 + X) * 1024u);
    SB();
    Q[X][1] = mfma16(x1, wq[KS & 1][X], Q[X][1]);
    SB();
  }
  if constexpr (KS + 1 < 18) k1_qkv_step<KS + 1>(S, wq, Q, qa, xv, rsX, xsb, G);
}

constexpr int K2D = 5;
constexpr unsigned L2_SLOT = 9216, L2_NS = K2D + 2;
constexpr unsigned L2_WO = L1_BUFA + 4 * L2_SLOT;
constexpr unsigned L2_MT = L1_BUFB + 3 * L2_SLOT, L2_KV = L1_SCR;
constexpr unsigned L2_OWN = L1_BUFB, L2_OUT = L1_BUFA;

struct K2S {
  u32x4 hr[K2D + 1][2];
  u32x4 ha[3][2];
  s16x4 wlo[6], whi[6];
  f32x4 acc[9][2];
  unsigned flag, ardm;
  unsigned ard;
};
struct K2C {
  rsrc_t rsH, rsW, rsB;
  unsigned vo0, vo1, hvoff, hsoF, hsoA, wsoF, xso, lb, lanec;
  int w, sp;
  gu32* chf;
};
constexpr bool k2_att(int n) { return n >= 18; }
constexpr unsigned k2_loc(int n) {
  return n < 8 ? 4 * (n >> 1) + (n & 1) : n < 10 ? 8 + n : 4 * ((n - 10) >> 1) + 2 + (n & 1);
}
constexpr unsigned k2_slot(int s) { return s < 4 ? L1_BUFA + s * L2_SLOT : L1_BUFB + (s - 4) * L2_SLOT; }
constexpr int k2_tW(int m) { return m == 4 ? -30 : 16 * (m - 5); }
constexpr int k2_tH(int m, int half) {
  return m <= 1 ? -40 + 2 * m + half : m <= 4 ? -24 + 2 * m + half : 16 * (m - 5) + 5 + 2 * half;
}
constexpr int k2_tA(int i) { return 16 * (6 + i) + 8; }
constexpr int k2_younger(int t0, int n) {
  int cnt = 0;
  const int tw = 16 * n + 3;
  for (int m = 4; m <= 17; ++m) if (k2_tW(m) > t0 && k2_tW(m) < tw) ++cnt;
  for (int m = 0; m <= 17; ++m)
    for (int h = 0; h < 2; ++h) if (k2_tH(m, h) > t0 && k2_tH(m, h) < tw) ++cnt;
  for (int i = 0; i < 6; ++i) if (k2_tA(i) > t0 && k2_tA(i) < tw) ++cnt;
  for (int i = 0; i < 4; ++i) if (-29 + i > t0) ++cnt;
  return cnt;
}
constexpr int k2_count(int n) {
  const int m = n + 1, th = k2_tH(m, 1), tw = m >= 4 ? k2_tW(m) : -100;
  return k2_younger(th > tw ? th : tw, n);
}
static_assert(k2_count(0) == 12 && k2_count(1) == 8 && k2_count(3) == 10 && k2_count(8) == 12 && k2_count(12) == 14 && k2_count(13) == 12, "schedule");
static_assert(k2_younger(16 * 3 + 4, 5) == 6, "schedule");
template <int T, int I, int ROLE = 0> DI void k2_issue(K2S& S, const K2C& C) {
  if constexpr (T <= 17) {
    constexpr unsigned ks = k2_loc(T);
    const unsigned wso = ABL2_NOW ? 0xf0000000u : C.wsoF + ks * 36864u, hso = ABL2_NOH ? 0xf0000000u : C.hsoF + ks * 131072u;
    if constexpr (I == 0) glds16<ROLE>(C.lb + k2_slot(T % L2_NS) + C.w * 1024u, C.vo0, C.rsW, wso);
    if constexpr (I == 1) { if ((T & 7) == C.w) glds16<3>(C.lb + k2_slot(T % L2_NS) + 8192u, C.vo1, C.rsW, wso); }
    if constexpr (I == 2) bload16_sc1<0, ROLE>(S.hr[T % (K2D + 1)][0], C.hvoff, C.rsH, hso);
    if constexpr (I == 3) bload16_sc1<1024, ROLE>(S.hr[T % (K2D + 1)][1], C.hvoff, C.rsH, hso);
  }
}
constexpr int K2_SEQ = 135;
constexpr int k2_T(int M) { return M < K2_SEQ ? M / 9 : 15 + (M - K2_SEQ) % 6; }
constexpr int k2_c(int M) { return M < K2_SEQ ? M % 9 : (M - K2_SEQ) / 6; }
template <int M, int H> DI void k2_read(K2S& S, const K2C& C) {
  if constexpr (M < 189) {
    constexpr int T = k2_T(M), c = k2_c(M);
    if constexpr (M < K2_SEQ) {
      if constexpr (H == 0 && c == 0) { S.ard = C.lanec + k2_slot(T % L2_NS); PIN(S.ard); }
      if constexpr (H == 0) S.wlo[M % 6] = trread(S.ard + c * 32);
      else S.whi[M % 6] = trread(S.ard + c * 32 + 4608);
    } else {
      static_assert(k2_slot(16 % L2_NS) == k2_slot(15 % L2_NS) + L2_SLOT && k2_slot(17 % L2_NS) == k2_slot(15 % L2_NS) + 2 * L2_SLOT, "ring");
      if constexpr (H == 0 && M == K2_SEQ) { S.ard = C.lanec + k2_slot(15 % L2_NS); PIN(S.ard); S.ardm = C.lanec + L2_MT; PIN(S.ardm); }
      const unsigned a = T <= 17 ? S.ard + (T - 15) * L2_SLOT + c * 32 : S.ardm + (T - 18) * L2_SLOT + c * 32;
      if constexpr (H == 0) S.wlo[M % 6] = trread(a);
      else S.whi[M % 6] = trread(a + 4608);
    }
  }
}
template <int c> DI void k2_send(K2S& S, const K2C& C) {
  if (C.w != C.sp) {
    const u32x4 v = {pack2(S.acc[c][0][0], S.acc[c][0][1]), pack2(S.acc[c][0][2], S.acc[c][0][3]),
                     pack2(S.acc[c][1][0], S.acc[c][1][1]), pack2(S.acc[c][1][2], S.acc[c][1][3])};
    bstore16_sc1<0>(v, C.hvoff, C.rsB, C.xso + c * 1024u);
  } else {
    *LDSP(f32x4, C.lb + L2_OWN + (c * 2) * 1024u + C.hvoff) = S.acc[c][0];
    *LDSP(f32x4, C.lb + L2_OWN + (c * 2 + 1) * 1024u + C.hvoff) = S.acc[c][1];
  }
}
template <int T, int N> DI void k2_wait(K2S& S) {
  asm volatile("s_waitcnt vmcnt(%2)\n\ts_barrier" : "+v"(S.hr[T % (K2D + 1)][0]), "+v"(S.hr[T % (K2D + 1)][1]) : "i"(N) : "memory");
}
template <int M> DI void k2_step(K2S& S, const K2C& C) {
  constexpr int T = k2_T(M), c = k2_c(M);
  constexpr bool seq = M < K2_SEQ;
  const s16x8 wf = __builtin_shufflevector(S.wlo[M % 6], S.whi[M % 6], 0, 1, 2, 3, 4, 5, 6, 7);
  const s16x8 h0 = __builtin_bit_cast(s16x8, k2_att(T) ? S.ha[k2_att(T) ? T - 18 : 0][0] : S.hr[T % (K2D + 1)][0]);
  const s16x8 h1 = __builtin_bit_cast(s16x8, k2_att(T) ? S.ha[k2_att(T) ? T - 18 : 0][1] : S.hr[T % (K2D + 1)][1]);
  const f32x4 z4 = {0.f, 0.f, 0.f, 0.f};
  S.acc[c][0] = mfma16(wf, h0, T == 0 ? z4 : S.acc[c][0]);
  SB();
  k2_read<M + 5, 0>(S, C);
  SB();
  S.acc[c][1] = mfma16(wf, h1, T == 0 ? z4 : S.acc[c][1]);
  SB();
  k2_read<M + 5, 1>(S, C);
  if constexpr (seq) {
    if constexpr (T + K2D <= 17) {
      if constexpr (c == 0) { if (!STAGGER || C.w < 4) k2_issue<T + K2D, 0, STAGGER>(S, C); }
      if constexpr (STAGGER && c == 1) { if (C.w >= 4) k2_issue<T + K2D, 0, 2>(S, C); }
      if constexpr (c == 2) k2_issue<T + K2D, 1>(S, C);
      if constexpr (c == (STAGGER ? 4 : 5)) { if (!STAGGER || C.w < 4) k2_issue<T + K2D, 2, STAGGER>(S, C); }
      if constexpr (STAGGER && c == 5) { if (C.w >= 4) k2_issue<T + K2D, 2, 2>(S, C); }
      if constexpr (c == (STAGGER ? 6 : 7)) { if (!STAGGER || C.w < 4) k2_issue<T + K2D, 3, STAGGER>(S, C); }
      if constexpr (STAGGER && c == 7) { if (C.w >= 4) k2_issue<T + K2D, 3, 2>(S, C); }
    }
    if constexpr (T >= 6 && T <= 11 && c == 8)
      bload16_sc1<((T - 6) & 1) * 1024>(S.ha[(T - 6) >> 1][(T - 6) & 1], C.hvoff, C.rsH, C.hsoA + ((T - 6) >> 1) * 131072u);
    if constexpr (T == 3 && c == 4) {
      if (C.w == 0) asm volatile("global_load_dword %0, %1, off sc1 ; role 3" : "=v"(S.flag) : "v"(C.chf));
    }
    if constexpr (T == 5 && c == 3) {
      if (C.w == 0) {
        asm volatile("s_waitcnt vmcnt(%1)" : "+v"(S.flag) : "i"(k2_younger(16 * 3 + 4, 5) - (STAGGER ? 1 : 0)));
        if (rfl(S.flag) < 4u) ctr_wait(C.chf, 4u);
      }
    }
    if constexpr (c == 3 && T < 14) k2_wait<T + 1, k2_count(T)>(S);
    if constexpr (c == 3 && T == 1) { if (threadIdx.x == 0) ctr_add(C.chf); }
    if constexpr (c == 2 && T == 14)
      asm volatile("s_waitcnt vmcnt(0)"
                   : "+v"(S.ha[0][0]), "+v"(S.ha[0][1]), "+v"(S.ha[1][0]), "+v"(S.ha[1][1]), "+v"(S.ha[2][0]), "+v"(S.ha[2][1]),
                     "+v"(S.hr[15 % (K2D + 1)][0]), "+v"(S.hr[15 % (K2D + 1)][1]), "+v"(S.hr[16 % (K2D + 1)][0]), "+v"(S.hr[16 % (K2D + 1)][1]),
                     "+v"(S.hr[17 % (K2D + 1)][0]), "+v"(S.hr[17 % (K2D + 1)][1]));
    if constexpr (c == 3 && T == 14) asm volatile("s_barrier" ::: "memory");
  } else if constexpr (T == 20 && c > 0) {
    k2_send<c - 1>(S, C);
  }
  SB();
  if constexpr (seq && c == 8 && T % 5 == 4) { STAMP(S.dg, 18 + T / 5) }
  if constexpr (M + 1 < 189) k2_step<M + 1>(S, C);
}

__global__ __launch_bounds__(512, 2) void kf_main(KFP p) {
  __shared__ __attribute__((aligned(128))) char smem[L1_TOTAL];
  const unsigned lb = (unsigned)(size_t)(__attribute__((address_space(3))) char*)smem;
  const int tid = threadIdx.x, l = tid & 63;
  const int w = rfl(tid >> 6);
  const int b = blockIdx.x, xc = b & 7, r = b >> 3;
  const int cr = r & 3, j = 4 * xc + cr, ib = r >> 2;
  const int g = l >> 4, q = (l & 15) >> 2, pp = l & 3, c16 = l & 15;

  STAMPR(p.dg, 30) STAMP(p.dg, 0)
  const rsrc_t rsX = mk_rsrc(p.xf, (unsigned)SZ_XF);
  const rsrc_t rsQ = mk_rsrc(p.wq, (unsigned)SZ_WQ);
  const rsrc_t rsG = mk_rsrc(p.wg, D_ * F_ * 2);
  const rsrc_t rsU = mk_rsrc(p.wu, D_ * F_ * 2);
  const rsrc_t rsH = mk_rsrc(p.hf, (unsigned)SZ_HF);
  gu32* const ctr = (gu32*)p.ctr;
  K1S S;

#pragma unroll
  for (int i = 0; i < 9; ++i) glds16(lb + L1_BUFA + (9 * w + i) * 1024u, l * 16u, rsQ, (j * 72u + 9 * w + i) * 1024u);
  const int T0 = 16 * ib + 2 * w;
  const unsigned hvoff = l * 16u, xsb = T0 * 18432u;
#define XL(s) bload16<((s) & 3) * 1024>(S.xf[0][s], hvoff, rsX, xsb + ((s) >> 2) * 4096u); \
              bload16<((s) & 3) * 1024>(S.xf[1][s], hvoff, rsX, xsb + 18432u + ((s) >> 2) * 4096u);
  XL(0) XL(1) XL(2) XL(3) XL(4) XL(5)
#undef XL
  K1G G;
  G.w = w;
  const unsigned wcol = (576u * xc + 128u * cr) * 2u;
  {
    const unsigned rowv = (l >> 3) * 9216u, c8 = l & 7, s0 = (l >> 4) & 1;
    const unsigned vA = rowv + ((c8 ^ (s0 << 1)) << 4), vB = rowv + ((c8 ^ ((s0 | 2) << 1)) << 4);
    G.vo0 = (w & 1) ? vB : vA;
    G.vo1 = (w & 1) ? vA : vB;
  }
  G.sstep = 8u * 9216u;
  const unsigned wsoff = 72u * w * 9216u, wdst = lb + 72u * w * 128u;
  G.rs = rsG; G.soff = wsoff + wcol; G.dst = wdst + L1_BUFB;

  STAMP(p.dg, 1)
  wait_bar<12>();
  STAMP(p.dg, 2)

  {
    const unsigned qa = lb + L1_BUFA + l * 16u;
    const f32x4 z4 = {0.f, 0.f, 0.f, 0.f};
    f32x4 Q[4][2];
#pragma unroll
    for (int X = 0; X < 4; ++X) Q[X][0] = Q[X][1] = z4;
    s16x8 wq[2][4];
#pragma unroll
    for (int X = 0; X < 4; ++X) wq[0][X] = *LDSP(s16x8, qa + X * 1024u);
    k1_qkv_step<0>(S, wq, Q, qa, hvoff, rsX, xsb, G);
    STAMP(p.dg, 3)
    unsigned fKA[4], fKB[4], fVA[4], fVB[4], fQ2[4];
#pragma unroll
    for (int T = 0; T < 2; ++T)
#pragma unroll
      for (int h2 = 0; h2 < 2; ++h2) {
        fKA[2 * T + h2] = pack2(phi_f(Q[0][T][2 * h2]), phi_f(Q[0][T][2 * h2 + 1]));
        fKB[2 * T + h2] = pack2(phi_f(Q[1][T][2 * h2]), phi_f(Q[1][T][2 * h2 + 1]));
        fVA[2 * T + h2] = pack2(Q[2][T][2 * h2], Q[2][T][2 * h2 + 1]);
        const unsigned raw = pack2(Q[3][T][2 * h2], Q[3][T][2 * h2 + 1]);
        fVB[2 * T + h2] = c16 < 2 ? 0x3f803f80u : raw;
        fQ2[2 * T + h2] = pack2(phi_f(Q[3][T][2 * h2]), phi_f(Q[3][T][2 * h2 + 1]));
      }
    const s16x8 aKA = mk_frag(fKA[0], fKA[1], fKA[2], fKA[3]), aKB = mk_frag(fKB[0], fKB[1], fKB[2], fKB[3]);
    const s16x8 bVA = mk_frag(fVA[0], fVA[1], fVA[2], fVA[3]), bVB = mk_frag(fVB[0], fVB[1], fVB[2], fVB[3]);
    const s16x8 aQ2 = mk_frag(fQ2[0], fQ2[1], fQ2[2], fQ2[3]);
    const f32x4 R0 = mfma16(aKA, bVA, z4), R1 = mfma16(aKA, bVB, z4);
    const f32x4 R2 = mfma16(aKB, bVA, z4), R3 = mfma16(aKB, bVB, z4);
    {
      const unsigned one = (c16 >> 2) == g ? (0x3f80u << (16 * (c16 & 1))) : 0u;
      const unsigned i0 = (c16 & 2) ? 0u : one, i1 = (c16 & 2) ? one : 0u;
      u32x4* pq = (u32x4*)p.pq + ((b * 8 + w) * 2) * 64 + l;
#pragma unroll
      for (int T = 0; T < 2; ++T) {
        const s16x8 id = T == 0 ? mk_frag(i0, i1, 0u, 0u) : mk_frag(0u, 0u, i0, i1);
        const f32x4 qa4 = mfma16(aKB, id, z4), qb4 = mfma16(aQ2, id, z4);
        const unsigned b01 = pack2(qb4[0], qb4[1]), b23 = pack2(qb4[2], qb4[3]);
        pq[T * 64] = u32x4{pack2(qa4[0], qa4[1]), pack2(qa4[2], qa4[3]), g == 1 ? b01 : (g == 2 ? b23 : 0u), g == 1 ? b23 : 0u};
      }
    }
    {
      const unsigned sb = lb + L1_SCR + w * 1408u;
      const int eB = c16 == 0 ? 18 : c16 + 14;
      const bool okB = c16 == 0 || c16 == 2 || c16 == 3;
      const unsigned dummy = lb + L1_SCR + 8 * 1408u + l * 4u;
#pragma unroll
      for (int rr = 0; rr < 4; ++rr) {
        const int d = 4 * g + rr;
        *LDSP(float, sb + (d * 19 + c16) * 4) = R0[rr];
        *LDSP(float, okB ? sb + (d * 19 + eB) * 4 : dummy) = R1[rr];
        if (rr >= 2) {
          *LDSP(float, g == 0 ? sb + ((14 + rr) * 19 + c16) * 4 : dummy) = R2[rr];
          *LDSP(float, g == 0 && okB ? sb + ((14 + rr) * 19 + eB) * 4 : dummy) = R3[rr];
        }
      }
    }
  }
  STAMP(p.dg, 4)
  wait_bar<2>();
  STAMP(p.dg, 5)

  const unsigned lane0 = lb + (8 * g + q) * 128u + 8u * pp + ((((q >> 1) & 1) | ((g & 1) << 1)) << 5);
  const unsigned abA = lane0 + L1_BUFA, abB = lane0 + L1_BUFB;
  const unsigned ks0 = 18u * xc + 4u * cr;
  const unsigned hs0 = (ks0 * 128u + T0) * 1024u;

  k1_first_reads<4096>(S, abB);
  const rsrc_t rsKV = mk_rsrc(p.kv, (unsigned)SZ_KV);
  if (tid < 88) {
    f32x4 a = *LDSP(f32x4, lb + L1_SCR + tid * 16);
#pragma unroll
    for (int ww = 1; ww < 8; ++ww) a += *LDSP(f32x4, lb + L1_SCR + ww * 1408u + tid * 16);
    bstore16_sc1<0>(__builtin_bit_cast(u32x4, a), tid * 16u, rsKV, (j * 8 + ib) * 1408u);
  }
  SB();
  G.wm0 = lb + L1_SCR + 15872u;
  G.wvo = l < 9 ? (72u * ib + 9u * w + l) * 9216u + wcol + 128u : 0xf0000000u;
  G.wrs = rsG;
  G.wvo2 = w < 3 ? (l < 54 ? (576u * xc * 9u + r * 162u + w * 54u + l) * 128u : 0xf0000000u)
         : w == 3 ? (l < 21 && r * 21 + l < 648 ? (72u * xc * 9u + r * 21u + l) * 128u : 0xf0000000u)
                  : (l < 18 ? (72u * ib + 18u * (w - 4) + l) * 9216u + wcol + 128u : 0xf0000000u);
  G.wrs2 = w < 3 ? mk_rsrc(p.wd, F_ * D_ * 2) : w == 3 ? mk_rsrc(p.wo, D_ * D_ * 2) : rsU;
  gu32* const ckv = ctr + (CT_KV + xc) * 32;
  const unsigned gvo0 = G.vo0, gvo1 = G.vo1;
  G.rs = rsU; G.soff = wsoff + wcol; G.dst = wdst + L1_BUFA;
  gu32* const chA = ctr + (CT_H + xc * 8 + ib) * 32;
  k1_step<1, 0, 4, 4096, 4096, 1, 1, 0>(S, abB, abA, G, rsH, hvoff, 0, 0, ckv, chA);
  STAMP(p.dg, 6)
  G.rs = (w >> 2) ? rsU : rsG;
  G.vo0 = G.vo1 = (l >> 2) * 9216u + (((l & 3) ^ ((l >> 5) << 1)) * 16u);
  G.sstep = 16u * 9216u;
  G.soff = 144u * (w & 3) * 9216u + (576u * xc + 512u + 32u * (cr >> 1)) * 2u;
  G.dst = lb + L1_BUFB + (w >> 2) * 36864u + 144u * (w & 3) * 64u;
  const unsigned abS = lb + L1_BUFB + (8 * g + q) * 64u + 8u * pp + (((cr & 1) ^ (g & 1)) << 5);
  k1_step<2, 1, 4, 4096, 2048, 1, 0, 0>(S, abA, abS, G, rsH, hvoff, hs0, 0, ckv, chA);
  STAMP(p.dg, 8)
  G.rs = rsG; G.vo0 = gvo0; G.vo1 = gvo1; G.sstep = 8u * 9216u; G.soff = wsoff + wcol + 128u; G.dst = wdst + L1_BUFA;
  k1_step<0, 2, 2, 2048, 4096, 1, 0, 0>(S, abS, abA, G, rsH, hvoff, 0, hs0 + 131072u, ckv, chA);
  STAMP(p.dg, 10)
  G.rs = rsU; G.soff = wsoff + wcol + 128u; G.dst = wdst + L1_BUFB;
  k1_step<1, 3, 4, 4096, 4096, 1, 2, 0>(S, abA, abB, G, rsH, hvoff, 0,
                                        ((18u * xc + 16u + (cr >> 1)) * 128u + T0) * 1024u + (cr & 1) * 8u, ckv, chA);
  STAMP(p.dg, 12)
  const rsrc_t rsW = mk_rsrc(p.wd, F_ * D_ * 2);
  const unsigned wsoF = 18u * xc * 36864u + cr * 288u;
  G.rs = w < 4 ? rsW : mk_rsrc(p.wo, D_ * D_ * 2);
  G.soff = w < 4 ? wsoF + (4 * (w >> 1) + (w & 1)) * 36864u : 18u * (4 * xc + w - 4) * 1152u + cr * 288u;
  G.dst = lb + L1_BUFA + w * L2_SLOT;
  G.rsk = rsKV;
  { const unsigned hh = tid / 88, q4 = tid - 88 * hh; G.vok = tid < 352 ? ((4 * xc + hh) * 8 * 352 + q4 * 4) * 4u : 0xf0000000u; }
  k1_step<2, 1, 4, 4096, 0, 2, 0, 0>(S, abB, 0, G, rsH, hvoff, hs0 + 2 * 131072u, 0, ckv, chA);
  u32x4 pqv[2];
  {
    const rsrc_t rsP = mk_rsrc(p.pq, (unsigned)SZ_PQ);
    bload16<0>(pqv[0], hvoff, rsP, (b * 8 + w) * 2048u);
    bload16<1024>(pqv[1], hvoff, rsP, (b * 8 + w) * 2048u);
  }
  asm volatile("s_waitcnt vmcnt(4)" : "+v"(S.kp[0]), "+v"(S.kp[1]), "+v"(S.kp[2]), "+v"(S.kp[3]), "+v"(S.kp[4]), "+v"(S.kp[5]), "+v"(S.kp[6]), "+v"(S.kp[7]));
  {
    f32x4 a = __builtin_bit_cast(f32x4, S.kp[0]);
#pragma unroll
    for (int i = 1; i < 8; ++i) a += __builtin_bit_cast(f32x4, S.kp[i]);
    if (tid < 352) *LDSP(f32x4, lb + L2_KV + tid * 16u) = a;
  }
  if (w == 0) ctr_wait(chA, 4u);
  k1_epi_all<2, 3, 1>(S);
  k1_store_pair(S, rsH, hvoff, hs0 + 3 * 131072u);
  STAMP(p.dg, 14) STAMPR(p.dg, 27)
  asm volatile("s_waitcnt lgkmcnt(0)\n\ts_barrier" ::: "memory");
  STAMP(p.dg, 15)

  K2S S2;
  K2C C;
  C.rsH = rsH; C.rsW = rsW; C.w = w; C.sp = xc; C.lb = lb;
  C.rsB = mk_rsrc(p.xb, (unsigned)SZ_XB);
  C.xso = (((ib * 4 + cr) * 8 + w) * 8 + xc) * 9216u;
  { const int ci = 64 * w + l, rho = ci / 18, c = ci - 18 * rho; C.vo0 = rho * 1152u + c * 16u; }
  { const int ci = 512 + l, rho = ci / 18, c = ci - 18 * rho; C.vo1 = rho * 1152u + c * 16u; }
  C.hvoff = hvoff;
  const unsigned hT = T0 * 1024u;
  C.hsoF = hT + 18u * xc * 131072u;
  C.wsoF = wsoF;
  C.lanec = lb + (4 * g + q) * 288u + 8u * pp;

  k2_issue<0, 2>(S2, C); k2_issue<0, 3>(S2, C); k2_issue<1, 2>(S2, C); k2_issue<1, 3>(S2, C);
  k2_issue<4, 0>(S2, C); k2_issue<4, 1>(S2, C);
  STAMP(p.dg, 16)
  {
    float za[4], zb[4];
#pragma unroll
    for (int e = 0; e < 4; ++e) {
      const int da = 4 * (g - 1) + e, db = da + 12;
      const float ta = *LDSP(float, lb + L2_KV + (cr * 352 + (g > 0 ? da * 19 + 18 : 0)) * 4);
      const float tb = *LDSP(float, lb + L2_KV + (cr * 352 + (g > 0 && db < 18 ? db * 19 + 18 : 0)) * 4);
      za[e] = g > 0 ? ta : 0.f;
      zb[e] = g > 0 && db < 18 ? tb : 0.f;
    }
    const int piA = 6 * cr + g - 1, piB = 6 * cr + g + 2;
    const unsigned base = (KS_FFN + 3u * xc) * 131072u + T0 * 1024u + c16 * 16u;
    const unsigned oA = g == 0 ? 0xf0000000u : base + (piA >> 3) * 131072u + (piA & 3) * 256u + ((piA >> 2) & 1) * 8u;
    const unsigned oB = g == 0 ? 0xf0000000u : base + (piB >> 3) * 131072u + (piB & 3) * 256u + ((piB >> 2) & 1) * 8u;
    asm volatile("s_waitcnt vmcnt(7)" : "+v"(pqv[0]), "+v"(pqv[1]));
#pragma unroll
    for (int T = 0; T < 2; ++T) {
      const u32x4 u = pqv[T];
      const float v[8] = {bf_lo(u[0]), bf_hi(u[0]), bf_lo(u[1]), bf_hi(u[1]), bf_lo(u[2]), bf_hi(u[2]), bf_lo(u[3]), bf_hi(u[3])};
      float den = 0.f;
#pragma unroll
      for (int e = 0; e < 4; ++e) den += v[e] * za[e] + v[4 + e] * zb[e];
      {
        const unsigned d0 = __float_as_uint(den);
        const auto r16 = __builtin_amdgcn_permlane16_swap(d0, d0, false, false);
        const unsigned r0 = r16[0], r1 = r16[1];
        const unsigned d1 = __float_as_uint(__uint_as_float(r0) + __uint_as_float(r1));
        const auto r32 = __builtin_amdgcn_permlane32_swap(d1, d1, false, false);
        const unsigned s0 = r32[0], s1 = r32[1];
        den = __uint_as_float(s0) + __uint_as_float(s1);
      }
      const float inv = frcp(den + 1e-6f);
      const u32x2 va = {pack2(v[0] * inv, v[1] * inv), pack2(v[2] * inv, v[3] * inv)};
      const u32x2 vb = {pack2(v[4] * inv, v[5] * inv), pack2(v[6] * inv, v[7] * inv)};
      __builtin_amdgcn_raw_buffer_store_b64(va, rsH, oA + T * 1024u, 0, AUX_SC1);
      __builtin_amdgcn_raw_buffer_store_b64(vb, rsH, oB + T * 1024u, 0, AUX_SC1);
    }
  }
  SB();
  STAMP(p.dg, 7)
  const f32x4 z4 = {0.f, 0.f, 0.f, 0.f};
  {
    const int hh = w >> 1, d = 16 * (w & 1) + c16;
    const unsigned ao = lb + L2_WO + hh * L2_SLOT + (8 * g + q) * 288u + 8u * pp;
    float v[8];
    const unsigned ka0 = lb + L2_KV + (hh * 352 + (d < 18 ? d : 0) * 19 + 8 * (g < 2 ? g : 2)) * 4;
#pragma unroll
    for (int e = 0; e < 8; ++e) v[e] = *LDSP(float, ka0 + e * 4);
    s16x8 A[9];
#pragma unroll
    for (int c = 0; c < 9; ++c) A[c] = trfrag(ao + c * 32, ao + c * 32 + 4 * 288);
    unsigned bh[4], bl[4];
#pragma unroll
    for (int e2 = 0; e2 < 4; ++e2) {
      const float v0 = (d < 18 && 8 * g + 2 * e2 < 18) ? v[2 * e2] : 0.f, v1 = (d < 18 && 8 * g + 2 * e2 + 1 < 18) ? v[2 * e2 + 1] : 0.f;
      bh[e2] = pack2(v0, v1);
      bl[e2] = pack2(v0 - bf_lo(bh[e2]), v1 - bf_hi(bh[e2]));
    }
    const s16x8 Bh = mk_frag(bh[0], bh[1], bh[2], bh[3]), Bl = mk_frag(bl[0], bl[1], bl[2], bl[3]);
    const int ka = 24 * hh + d;
    const unsigned mo = d < 24 ? lb + L2_MT + (ka >> 5) * L2_SLOT + (ka & 31) * 288u + 8u * g : lb + k2_slot(L2_NS - 1) + l * 8u;
    f32x4 D[9];
#pragma unroll
    for (int c = 0; c < 9; ++c) D[c] = mfma16(A[c], Bh, z4);
#pragma unroll
    for (int c = 0; c < 9; ++c) D[c] = mfma16(A[c], Bl, D[c]);
#pragma unroll
    for (int c = 0; c < 9; ++c) *LDSP(u32x2, mo + c * 32) = u32x2{pack2(D[c][0], D[c][1]), pack2(D[c][2], D[c][3])};
  }
  SB();
  STAMP(p.dg, 9)
  k2_issue<2, 2>(S2, C); k2_issue<2, 3>(S2, C); k2_issue<3, 2>(S2, C); k2_issue<3, 3>(S2, C);
  k2_issue<4, 2>(S2, C); k2_issue<4, 3>(S2, C);
  STAMP(p.dg, 11)
  asm volatile("s_waitcnt lgkmcnt(0)\n\ts_barrier" ::: "memory");
  C.chf = ctr + (CT_H2 + xc * 8 + ib) * 32;
  STAMP(p.dg, 17)
  C.hsoA = hT + (KS_FFN + 3u * xc) * 131072u;
  asm volatile("s_waitcnt vmcnt(13)" : "+v"(S2.hr[0][0]), "+v"(S2.hr[0][1]));
  k2_read<0, 0>(S2, C); k2_read<0, 1>(S2, C); k2_read<1, 0>(S2, C); k2_read<1, 1>(S2, C); k2_read<2, 0>(S2, C);
  k2_read<2, 1>(S2, C); k2_read<3, 0>(S2, C); k2_read<3, 1>(S2, C); k2_read<4, 0>(S2, C); k2_read<4, 1>(S2, C);
  SB();
  k2_step<0>(S2, C);
  STAMP(p.dg, 23) STAMPR(p.dg, 28)

  const int sp = xc, ni = cr, mi = ib;
  const int grp = mi * 4 + ni;
  const rsrc_t rsB = C.rsB;
  k2_send<8>(S2, C);
  const int tok0 = 256 * mi + 32 * sp;
  u32x4 xr[2];
  size_t xo[2];
#pragma unroll
  for (int i = 0; i < 2; ++i) {
    const int it = tid + 512 * i, itc = it < 576 ? it : 0, tk = itc / 18, ch = itc - tk * 18;
    xo[i] = (size_t)(tok0 + tk) * D_ + 144 * ni + 8 * ch;
    xr[i] = *(const u32x4*)((const uint16_t*)p.x + xo[i]);
  }
  asm volatile("s_waitcnt vmcnt(0) lgkmcnt(0)\n\ts_barrier" ::: "memory");
  STAMP(p.dg, 24)
  if (w == 0) {
    gu32* cx = ctr + (CT_X + grp * 8) * 32;
    if (l < 8) ctr_add(cx + l * 32);
    ctr_wait(cx + sp * 32, 8u);
  }
  asm volatile("s_waitcnt vmcnt(0)\n\ts_barrier" ::: "memory");
  STAMP(p.dg, 25) STAMPR(p.dg, 29)
#pragma unroll
  for (int i = 0; i < 2; ++i) {
    const int c = w + 8 * i;
    if (c < 9) {
      u32x4 v[8];
#pragma unroll
      for (int src = 0; src < 8; ++src)
        v[src] = __builtin_amdgcn_raw_buffer_load_b128(rsB, src == sp ? 0xf0000000u : hvoff + c * 1024u, ((grp * 8 + sp) * 8 + src) * 9216u, AUX_SC1);
      f32x4 a0 = *LDSP(f32x4, lb + L2_OWN + (2 * c) * 1024u + l * 16u), a1 = *LDSP(f32x4, lb + L2_OWN + (2 * c + 1) * 1024u + l * 16u);
#pragma unroll
      for (int src = 0; src < 8; ++src) {
        a0 += f32x4{bf_lo(v[src][0]), bf_hi(v[src][0]), bf_lo(v[src][1]), bf_hi(v[src][1])};
        a1 += f32x4{bf_lo(v[src][2]), bf_hi(v[src][2]), bf_lo(v[src][3]), bf_hi(v[src][3])};
      }
      *LDSP(f32x4, lb + L2_OUT + (c16 * 148 + 16 * c + 4 * g) * 4) = a0;
      *LDSP(f32x4, lb + L2_OUT + ((16 + c16) * 148 + 16 * c + 4 * g) * 4) = a1;
    }
  }
  __syncthreads();
#pragma unroll
  for (int i = 0; i < 2; ++i) {
    const int it = tid + 512 * i;
    if (it < 576) {
      const int tk = it / 18, ch = it - tk * 18;
      const f32x4 a = *LDSP(f32x4, lb + L2_OUT + (tk * 148 + 8 * ch) * 4), c = *LDSP(f32x4, lb + L2_OUT + (tk * 148 + 8 * ch + 4) * 4);
      u32x4 ov;
      ov[0] = pack2(a[0] + bf_lo(xr[i][0]), a[1] + bf_hi(xr[i][0]));
      ov[1] = pack2(a[2] + bf_lo(xr[i][1]), a[3] + bf_hi(xr[i][1]));
      ov[2] = pack2(c[0] + bf_lo(xr[i][2]), c[1] + bf_hi(xr[i][2]));
      ov[3] = pack2(c[2] + bf_lo(xr[i][3]), c[3] + bf_hi(xr[i][3]));
      *(u32x4*)((uint16_t*)p.out + xo[i]) = ov;
    }
  }
  STAMP(p.dg, 26) STAMPR(p.dg, 31) STAMP_DUMP(p.dg)
}

extern "C" void kernel_launch(void* const* d_in, const int* in_sizes, int n_in, void* d_out, int out_size,
                              void* d_ws, size_t ws_size, hipStream_t stream) {
  const uint16_t* x = (const uint16_t*)d_in[0];
  const void* wqkv = d_in[1];
  const uint16_t* wo = (const uint16_t*)d_in[2];
  const void* wg = d_in[4];
  const void* wu = d_in[5];
  const void* wd = d_in[6];
  char* ws = (char*)d_ws;

  k0_ln<<<256, K0_WARM ? 768 : 512, 0, stream>>>(x, (u32x4*)(ws + OFF_XF), (const uint16_t*)wqkv, (u32x4*)(ws + OFF_WQ),
                                                 (unsigned*)(ws + OFF_CT), (const unsigned*)wg, (const unsigned*)wu,
                                                 (const unsigned*)wd, (const unsigned*)wo);
  KFP p;
  p.xf = ws + OFF_XF; p.wq = ws + OFF_WQ; p.wg = wg; p.wu = wu; p.wd = wd; p.wo = wo; p.x = x;
  p.hf = ws + OFF_HF; p.kv = ws + OFF_KV; p.xb = ws + OFF_XB; p.pq = ws + OFF_PQ; p.ctr = ws + OFF_CT; p.out = d_out;
  kf_main<<<256, 512, 0, stream>>>(p);
}
```

```cpp
#include <hip/hip_runtime.h>
#include <hip/hip_bf16.h>
#include <stdint.h>

#define DI __device__ __forceinline__
typedef __attribute__((ext_vector_type(8))) short s16x8;
typedef __attribute__((ext_vector_type(4))) short s16x4;
typedef __attribute__((ext_vector_type(4))) float f32x4;
typedef __attribute__((ext_vector_type(4))) unsigned u32x4;
typedef __attribute__((ext_vector_type(2))) unsigned u32x2;
typedef __amdgpu_buffer_rsrc_t rsrc_t;
#define LDSP(T, a) ((__attribute__((address_space(3))) T*)(size_t)(a))

constexpr int S_ = 2048, D_ = 576, F_ = 4608, NH = 32, DH = 18;
constexpr int KS_FFN = 144, KS_ALL = 168;
constexpr size_t OFF_XF = 0;
constexpr size_t SZ_XF = (size_t)128 * 18 * 1024;
constexpr size_t OFF_WQ = OFF_XF + SZ_XF;
constexpr size_t SZ_WQ = (size_t)32 * 72 * 1024;
constexpr size_t OFF_HF = OFF_WQ + SZ_WQ;
constexpr size_t SZ_HF = (size_t)KS_ALL * 128 * 1024;
constexpr size_t OFF_KV = OFF_HF + SZ_HF;
constexpr size_t SZ_KV = (size_t)32 * 8 * 352 * 4;
constexpr size_t OFF_XB = OFF_KV + SZ_KV;
constexpr size_t SZ_XB = (size_t)32 * 8 * 8 * 9 * 1024;
constexpr size_t OFF_PQ = OFF_XB + SZ_XB;
constexpr size_t SZ_PQ = (size_t)256 * 8 * 2 * 1024;
constexpr size_t OFF_CT = OFF_PQ + SZ_PQ;
constexpr int CT_X = 0, CT_X2 = 256, CT_KV = 512, CT_H = 520, CT_H2 = 584, CT_N = 648;
constexpr size_t SZ_CT = (size_t)CT_N * 128;

DI rsrc_t mk_rsrc(const void* p, unsigned bytes) {
  return __builtin_amdgcn_make_buffer_rsrc((void*)p, (short)0, (int)bytes, 0x00020000);
}
DI unsigned rfl(unsigned v) { return __builtin_amdgcn_readfirstlane(v); }
DI float bf_lo(unsigned u) { return __uint_as_float(u << 16); }
DI float bf_hi(unsigned u) { return __uint_as_float(u & 0xffff0000u); }
DI unsigned pack2(float lo, float hi) {
  typedef __attribute__((ext_vector_type(2))) __bf16 bf2;
  typedef __attribute__((ext_vector_type(2))) float f2;
  f2 v = {lo, hi};
  bf2 r = __builtin_convertvector(v, bf2);
  return __builtin_bit_cast(unsigned, r);
}
DI s16x8 mk_frag(unsigned a, unsigned b, unsigned c, unsigned d) {
  u32x4 v = {a, b, c, d};
  return __builtin_bit_cast(s16x8, v);
}
DI f32x4 mfma16(s16x8 a, s16x8 b, f32x4 c) { return __builtin_amdgcn_mfma_f32_16x16x32_bf16(a, b, c, 0, 0, 0); }
DI s16x4 trread(unsigned addr) { return __builtin_amdgcn_ds_read_tr16_b64_v4i16(LDSP(s16x4, addr)); }
DI s16x8 trfrag(unsigned a0, unsigned a1) {
  s16x4 lo = trread(a0), hi = trread(a1);
  return __builtin_shufflevector(lo, hi, 0, 1, 2, 3, 4, 5, 6, 7);
}
template <int ROLE = 0> DI void glds16(unsigned m0v, unsigned voff, rsrc_t rs, unsigned soff) {
  asm volatile("s_mov_b32 m0, %0\n\ts_nop 0\n\tbuffer_load_dwordx4 %1, %2, %3 offen lds ; role %4"
               :: "s"(m0v), "v"(voff), "s"(rs), "s"(soff), "i"(ROLE) : "memory");
}
DI void warm_lines(unsigned m0v, unsigned voff, rsrc_t rs) {
  asm volatile("s_mov_b32 m0, %0\n\ts_nop 0\n\tbuffer_load_dword %1, %2, 0 offen lds" :: "s"(m0v), "v"(voff), "s"(rs) : "memory");
}
template <int IMM> DI void bload16(u32x4& d, unsigned voff, rsrc_t rs, unsigned soff) {
  asm volatile("buffer_load_dwordx4 %0, %1, %2, %3 offen offset:%4"
               : "=v"(d) : "v"(voff), "s"(rs), "s"(soff), "i"(IMM));
}
template <int IMM, int ROLE = 0> DI void bload16_sc1(u32x4& d, unsigned voff, rsrc_t rs, unsigned soff) {
  asm volatile("buffer_load_dwordx4 %0, %1, %2, %3 offen offset:%4 sc1 ; role %5"
               : "=v"(d) : "v"(voff), "s"(rs), "s"(soff), "i"(IMM), "i"(ROLE));
}
template <int IMM> DI void bstore16_sc1(u32x4 v, unsigned voff, rsrc_t rs, unsigned soff) {
  asm volatile("buffer_store_dwordx4 %0, %1, %2, %3 offen offset:%4 sc1\n\ts_nop 1"
               :: "v"(v), "v"(voff), "s"(rs), "s"(soff), "i"(IMM) : "memory");
}
typedef __attribute__((address_space(1))) unsigned gu32;
DI void ctr_add(gu32* c) { __hip_atomic_fetch_add(c, 1u, __ATOMIC_RELAXED, __HIP_MEMORY_SCOPE_AGENT); }
DI void ctr_wait(gu32* c, unsigned n) {
  for (unsigned spins = 0; spins < (1u << 20); ++spins) {
    if ((unsigned)__builtin_amdgcn_readfirstlane(__hip_atomic_load(c, __ATOMIC_RELAXED, __HIP_MEMORY_SCOPE_AGENT)) >= n) break;
    __builtin_amdgcn_s_sleep(1);
  }
}
template <int N> DI void wait_vm2(u32x4& a, u32x4& b) {
  asm volatile("s_waitcnt vmcnt(%2)" : "+v"(a), "+v"(b) : "i"(N));
}
template <int N> DI void wait_bar_nolgkm() {
  asm volatile("s_waitcnt vmcnt(%0)\n\ts_barrier" :: "i"(N) : "memory");
}
template <int N> DI void wait_bar() {
  asm volatile("s_waitcnt vmcnt(%0) lgkmcnt(0)\n\ts_barrier" :: "i"(N) : "memory");
}
DI float fexp2(float x) { return __builtin_amdgcn_exp2f(x); }
DI float frcp(float x) { return __builtin_amdgcn_rcpf(x); }
DI float phi_f(float t) { return (t > 0.f ? t + 1.f : fexp2(t * 1.4426950408889634f)) + 1e-6f; }
DI float geglu(float g, float u) {
  float e = fexp2(g * __builtin_fmaf(g * g, -0.10294323970f, -2.30220819814f));
  return (g * u) * frcp(1.f + e);
}
template <int CTRL> DI float dpp_add(float v) {
  return v + __builtin_bit_cast(float, __builtin_amdgcn_update_dpp(0, __builtin_bit_cast(int, v), CTRL, 0xf, 0xf, true));
}
DI float reduce32(float v) {
  v = dpp_add<0xB1>(v);
  v = dpp_add<0x4E>(v);
  v = dpp_add<0x141>(v);
  v = dpp_add<0x140>(v);
  const unsigned u = __float_as_uint(v);
  const auto r = __builtin_amdgcn_permlane16_swap(u, u, false, false);
  const unsigned r0 = r[0], r1 = r[1];
  return __uint_as_float(r0) + __uint_as_float(r1);
}

#define ABL2_NOW 0
#define ABL2_NOH 0
#define K0_NORETILE 0
#define K0_NOLN 0
#define STAGGER 0
#define NO_CODEWARM 0
#define NO_WARM 0
#define ABL_NOW 0
#define ABL_NOH 0
#define ABL_NOCOMP 0
#define ABL1_NOCOMP 0
#define ABL1_NOEPI 0
#define ABL1_NOSTAGE 0
#define STAMP(buf, slot)
#define STAMPR(buf, slot)
#define STAMP_DUMP(buf)
__global__ __launch_bounds__(512, 2) void k0_ln(const uint16_t* __restrict__ x, u32x4* __restrict__ xf,
                                                const uint16_t* __restrict__ wqkv, u32x4* __restrict__ wq,
                                                unsigned* __restrict__ ctr) {
  __shared__ u32x4 stage[4][288];
  const int l = threadIdx.x & 63, w = threadIdx.x >> 6;
  if (threadIdx.x < CT_N / 8) ctr[blockIdx.x * (CT_N / 8) + threadIdx.x] = 0u;
  if (w >= 4) {
    const int item = blockIdx.x + 256 * (w - 4);
    if (item >= 576 || K0_NORETILE) return;
    const int h = item / 18, s = item - 18 * h, g = l >> 4, c = l & 15;
    typedef u32x4 __attribute__((aligned(4))) u32x4_a4;
    u32x4* st = stage[w - 4];
    u32x4 t[5];
#pragma unroll
    for (int i = 0; i < 5; ++i) {
      const int ci = 64 * i + l < 288 ? 64 * i + l : 0, row = ci / 9, c9 = ci - 9 * row, sec = c9 / 3, cc = c9 - 3 * sec;
      const int col = (sec == 0 ? 576 : (sec == 1 ? 0 : 1152)) + 18 * h + (cc == 2 ? 10 : 8 * cc);
      t[i] = *(const u32x4_a4*)(wqkv + (size_t)(32 * s + row) * (3 * D_) + col);
    }
    asm volatile("" : "+v"(t[0]), "+v"(t[1]), "+v"(t[2]), "+v"(t[3]), "+v"(t[4]));
#pragma unroll
    for (int i = 0; i < 5; ++i)
      if (64 * i + l < 288) st[64 * i + l] = t[i];
    __builtin_amdgcn_fence(__ATOMIC_RELEASE, "wavefront");
    __builtin_amdgcn_wave_barrier();
    const int sec[4] = {0, c < 4 ? 0 : 48, 96, c < 4 ? 96 : 48};
    const int dim[4] = {c, c < 4 ? 14 + c : c - 4, c, c < 4 ? 14 + c : (c < 8 ? c + 8 : 14 + (c & 3))};
    unsigned short v[4][8];
#pragma unroll
    for (int X = 0; X < 4; ++X) {
      const uint16_t* src = (const uint16_t*)((const char*)st + 8 * g * 144 + sec[X] + 2 * dim[X] + (dim[X] >= 16 ? 12 : 0));
#pragma unroll
      for (int e = 0; e < 8; ++e) v[X][e] = src[e * 72];
    }
#pragma unroll
    for (int X = 0; X < 4; ++X) {
      u32x4 o;
#pragma unroll
      for (int e = 0; e < 4; ++e) o[e] = (unsigned)v[X][2 * e] | ((unsigned)v[X][2 * e + 1] << 16);
      wq[((size_t)item * 4 + X) * 64 + l] = o;
    }
    return;
  }
  if (K0_NOLN) return;
  const int tok = blockIdx.x * 8 + w * 2 + (l >> 5), p = l & 31;
  const u32x4* row = (const u32x4*)(x + (size_t)tok * D_);
  u32x4 c[3];
  c[0] = row[p];
  c[1] = row[p + 32];
  c[2] = row[p < 8 ? p + 64 : p];
  const bool has2 = p < 8;
  float v[24];
#pragma unroll
  for (int i = 0; i < 3; ++i)
#pragma unroll
    for (int j = 0; j < 4; ++j) { v[i * 8 + 2 * j] = bf_lo(c[i][j]); v[i * 8 + 2 * j + 1] = bf_hi(c[i][j]); }
  float s = 0.f, s2 = 0.f;
#pragma unroll
  for (int j = 0; j < 16; ++j) s += v[j];
#pragma unroll
  for (int j = 16; j < 24; ++j) s2 += v[j];
  s += has2 ? s2 : 0.f;
  const float mean = reduce32(s) * (1.f / 576.f);
  float q = 0.f, q2 = 0.f;
#pragma unroll
  for (int j = 0; j < 24; ++j) v[j] -= mean;
#pragma unroll
  for (int j = 0; j < 16; ++j) q += v[j] * v[j];
#pragma unroll
  for (int j = 16; j < 24; ++j) q2 += v[j] * v[j];
  q += has2 ? q2 : 0.f;
  const float var = reduce32(q) * (1.f / 576.f);
  const float rstd = 1.f / sqrtf(var + 1e-5f);
  const int T = tok >> 4, t16 = tok & 15;
#pragma unroll
  for (int i = 0; i < 3; ++i) {
    u32x4 o;
#pragma unroll
    for (int j = 0; j < 4; ++j) o[j] = pack2(v[i * 8 + 2 * j] * rstd, v[i * 8 + 2 * j + 1] * rstd);
    const int ch = p + 32 * i;
    if (i < 2 || has2) xf[((size_t)T * 18 + (ch >> 2)) * 64 + (ch & 3) * 16 + t16] = o;
  }
}

constexpr unsigned L1_BUFA = 0, L1_BUFB = 73728, L1_SCR = 147456, L1_TOTAL = 163840;
constexpr int AUX_SC1 = 16;
#define SB() __builtin_amdgcn_sched_barrier(0)
#define PIN(x) asm volatile("" : "+v"(x))

struct KFP {
  const void* xf; const void* wq; const void* wg; const void* wu; const void* wd; const void* wo; const void* x;
  void* hf; void* kv; void* xb; void* pq; void* ctr; void* out;
};

struct K1S {
  u32x4 xf[2][18];
  s16x4 wlo[6], whi[6];
  f32x4 acc[2][2];
  unsigned gel[4][2][2];
  u32x4 hq[2];
  float t0, t1;
  unsigned ard;
  u32x4 kp[8];
};
struct K1G { unsigned dst, soff, sstep, vo0, vo1, vok, wm0, wvo, wvo2; rsrc_t rs, rsk, wrs, wrs2; int w; };
template <int I, int ROLE = 0> DI void k1_piece(const K1G& G) {
  if (!ABL1_NOSTAGE) glds16<ROLE>(G.dst + I * 1024u, (I & 1) ? G.vo1 : G.vo0, G.rs, G.soff + I * G.sstep);
}
template <int I, int ROLE = 0> DI void k1_piece2(const K1G& G, int l) {
  const int ci = 64 * I + l, rho = ci / 18, c = ci - 18 * rho;
  glds16<ROLE>(G.dst + I * 1024u, rho * 1152u + c * 16u, G.rs, G.soff);
}
template <int STRIDE, int N, int H> DI void k1_read(K1S& S, unsigned a0, int slot_unused = 0) {
  constexpr int P = N / 18, KS = N % 18;
  if constexpr (H == 0 && KS == 0) { S.ard = STRIDE == 4096 ? (a0 ^ (P << 5)) : a0 + P * 36864u; PIN(S.ard); }
  if constexpr (H == 0 && KS == 9) { S.ard += 9 * STRIDE; PIN(S.ard); }
  if constexpr (H == 0) S.wlo[N % 6] = trread(S.ard + (KS % 9) * STRIDE);
  else S.whi[N % 6] = trread(S.ard + (KS % 9) * STRIDE + STRIDE / 8);
}
template <int KIND, int U, int A, int PR, int PH, int SL> DI void k1_epi(K1S& S) {
  constexpr int T = PR >> 1, R = 2 * (PR & 1);
  const float a = S.acc[A][T][R], b = S.acc[A][T][R + 1];
  if (ABL1_NOEPI) {
    if (PH == 3 && SL == 1) { if (KIND == 1) S.gel[U][T][PR & 1] = pack2(a, b); else S.hq[T][2 * (U & 1) + (PR & 1)] = pack2(a, b) ^ S.gel[U][T][PR & 1]; }
    return;
  }
  if constexpr (KIND == 1) {
    if constexpr (PH == 0 && SL == 0) { S.t0 = a * a; S.t1 = b * b; }
    if constexpr (PH == 0 && SL == 1) {
      S.t0 = __builtin_fmaf(S.t0, -0.10294323970f, -2.30220819814f);
      S.t1 = __builtin_fmaf(S.t1, -0.10294323970f, -2.30220819814f);
    }
    if constexpr (PH == 1 && SL == 0) { S.t0 *= a; S.t1 *= b; }
    if constexpr (PH == 1 && SL == 1) { S.t0 = fexp2(S.t0); S.t1 = fexp2(S.t1); }
    if constexpr (PH == 2 && SL == 0) { S.t0 += 1.f; S.t1 += 1.f; }
    if constexpr (PH == 2 && SL == 1) { S.t0 = frcp(S.t0); S.t1 = frcp(S.t1); }
    if constexpr (PH == 3 && SL == 0) { S.t0 *= a; S.t1 *= b; }
    if constexpr (PH == 3 && SL == 1) { unsigned v = pack2(S.t0, S.t1); PIN(v); S.gel[U][T][PR & 1] = v; }
    else { PIN(S.t0); PIN(S.t1); }
  } else {
    if constexpr (PH == 1 && SL == 0) { const unsigned u = S.gel[U][T][PR & 1]; S.t0 = bf_lo(u); S.t1 = bf_hi(u); PIN(S.t0); PIN(S.t1); }
    if constexpr (PH == 2 && SL == 0) { S.t0 *= a; S.t1 *= b; PIN(S.t0); PIN(S.t1); }
    if constexpr (PH == 3 && SL == 0) { unsigned v = pack2(S.t0, S.t1); PIN(v); S.hq[T][2 * (U & 1) + (PR & 1)] = v; }
  }
}
template <int KIND, int U, int A, int I = 0> DI void k1_epi_all(K1S& S) {
  if constexpr (I < 32) { k1_epi<KIND, U, A, I / 8, (I / 2) % 4, I % 2>(S); k1_epi_all<KIND, U, A, I + 1>(S); }
}
DI void k1_store_pair(K1S& S, rsrc_t rsH, unsigned voff, unsigned soff) {
  bstore16_sc1<0>(S.hq[0], voff, rsH, soff);
  bstore16_sc1<1024>(S.hq[1], voff, rsH, soff);
}
template <int KIND, int PREV, int NP, int STRIDE, int NSTRIDE, int STG, int SIG, int N>
DI void k1_step(K1S& S, unsigned ab, unsigned abn, const K1G& G, rsrc_t rsH, unsigned hvoff, unsigned hso, unsigned hsop,
                gu32* sig, gu32* sig2) {
  constexpr int P = N / 18, KS = N % 18, A = P & 1, NT = NP * 18;
  constexpr int EK = P == 0 ? (PREV == 3 ? 2 : PREV) : (NP == 2 ? 1 : KIND);
  constexpr int EU = P == 0 ? (PREV == 3 ? 0 : 3) : (NP == 2 ? 0 : P - 1);
  constexpr int EA = P == 0 ? 1 : (A ^ 1);
  constexpr bool EPI = EK != 0 && KS >= 1 && KS <= 16;
  const f32x4 z4 = {0.f, 0.f, 0.f, 0.f};
  const s16x8 wf = __builtin_shufflevector(S.wlo[N % 6], S.whi[N % 6], 0, 1, 2, 3, 4, 5, 6, 7);
  const s16x8 x0 = __builtin_bit_cast(s16x8, S.xf[0][KS]), x1 = __builtin_bit_cast(s16x8, S.xf[1][KS]);
  if (!ABL1_NOCOMP) S.acc[A][0] = mfma16(wf, x0, KS == 0 ? z4 : S.acc[A][0]);
  else if (KS == 0) { S.acc[A][0] = z4; S.acc[A][0][0] = (float)wf[0]; }
  SB();
  if constexpr (N + 5 < NT) k1_read<STRIDE, N + 5, 0>(S, ab);
  else if constexpr (NSTRIDE != 0) k1_read<NSTRIDE == 0 ? 4096 : NSTRIDE, N + 5 - NT, 0>(S, abn);
  if constexpr (EPI) k1_epi<EK, EU, EA, (KS - 1) / 4, (KS - 1) % 4, 0>(S);
  SB();
  if (!ABL1_NOCOMP) S.acc[A][1] = mfma16(wf, x1, KS == 0 ? z4 : S.acc[A][1]);
  else if (KS == 0) { S.acc[A][1] = z4; }
  SB();
  if constexpr (N + 5 < NT) k1_read<STRIDE, N + 5, 1>(S, ab);
  else if constexpr (NSTRIDE != 0) k1_read<NSTRIDE == 0 ? 4096 : NSTRIDE, N + 5 - NT, 1>(S, abn);
  if constexpr (EPI) k1_epi<EK, EU, EA, (KS - 1) / 4, (KS - 1) % 4, 1>(S);
  if constexpr (STG == 1 && NP == 4 && N % 6 == 2 && N / 6 < 9) { if (!STAGGER || G.w < 4) k1_piece<N / 6, STAGGER>(G); }
  if constexpr (STAGGER && STG == 1 && NP == 4 && N % 6 == 5 && N / 6 < 9) { if (G.w >= 4) k1_piece<N / 6, 2>(G); }
  if constexpr (STG == 1 && NP == 2 && N % 2 == 0 && N >= 2 && N / 2 <= 9) { if (!STAGGER || G.w < 4) k1_piece<N / 2 - 1, STAGGER>(G); }
  if constexpr (STAGGER && STG == 1 && NP == 2 && N % 2 == 1 && N >= 3 && N / 2 <= 9) { if (G.w >= 4) k1_piece<N / 2 - 1, 2>(G); }
  if constexpr (SIG == 1 && !NO_WARM && N == 56) warm_lines(G.wm0, G.wvo, G.wrs);
  if constexpr (SIG == 1 && !NO_WARM && N == 59) warm_lines(G.wm0, G.wvo2, G.wrs2);
  if constexpr (STG == 2 && N % 6 == 2 && N / 6 < 9) { if (!STAGGER || G.w < 4) k1_piece2<N / 6, STAGGER>(G, hvoff >> 4); }
  if constexpr (STAGGER && STG == 2 && N % 6 == 4 && N / 6 < 9) { if (G.w >= 4) k1_piece2<N / 6, 2>(G, hvoff >> 4); }
  if constexpr (STG == 2 && N % 6 == (STAGGER ? 0 : 5) && (N - (STAGGER ? 6 : 0)) / 6 < 8 && N >= 5)
    bload16_sc1<0>(S.kp[(N - (STAGGER ? 6 : 0)) / 6], G.vok, G.rsk, ((N - (STAGGER ? 6 : 0)) / 6) * 1408u);
  if constexpr (PREV == 2 && N == 17) k1_store_pair(S, rsH, hvoff, hsop);
  if constexpr (PREV == 3 && N == 17) {
#pragma unroll
    for (int T = 0; T < 2; ++T) {
      const u32x2 v = {S.hq[T][0], S.hq[T][1]};
      __builtin_amdgcn_raw_buffer_store_b64(v, rsH, hvoff + T * 1024u, hsop, AUX_SC1);
    }
  }
  if constexpr (KIND == 2 && NP == 4 && N == 2 * 18 + 17) k1_store_pair(S, rsH, hvoff, hso);
  if constexpr (N == 1) asm volatile("s_barrier" ::: "memory");
  if constexpr (SIG == 2 && N == NT - 8) { if (rfl(threadIdx.x >> 6) == 0) ctr_wait(sig, 32u); }
  if constexpr (NSTRIDE != 0 && N == NT - 8) wait_bar_nolgkm<KIND == 2 ? 2 : (SIG == 1 && !NO_WARM ? 2 : 0)>();
  if constexpr (SIG == 1 && N == NT - 8) { if (threadIdx.x == 0) ctr_add(sig); }
  if constexpr (SIG == 2 && N == NT - 8) { if (threadIdx.x == 0) ctr_add(sig2); }
  SB();
  if constexpr (N + 1 < NT) k1_step<KIND, PREV, NP, STRIDE, NSTRIDE, STG, SIG, N + 1>(S, ab, abn, G, rsH, hvoff, hso, hsop, sig, sig2);
}
template <int STRIDE> DI void k1_first_reads(K1S& S, unsigned ab) {
  k1_read<STRIDE, 0, 0>(S, ab); k1_read<STRIDE, 0, 1>(S, ab); k1_read<STRIDE, 1, 0>(S, ab); k1_read<STRIDE, 1, 1>(S, ab);
  k1_read<STRIDE, 2, 0>(S, ab); k1_read<STRIDE, 2, 1>(S, ab); k1_read<STRIDE, 3, 0>(S, ab); k1_read<STRIDE, 3, 1>(S, ab);
  k1_read<STRIDE, 4, 0>(S, ab); k1_read<STRIDE, 4, 1>(S, ab);
  SB();
}
template <int KS> DI void k1_qkv_step(K1S& S, s16x8 (&wq)[2][4], f32x4 (&Q)[4][2], unsigned qa, unsigned xv, rsrc_t rsX,
                                      unsigned xsb, const K1G& G) {
  if constexpr (KS + 6 < 18) {
    bload16<((KS + 6) & 3) * 1024>(S.xf[0][KS + 6], xv, rsX, xsb + ((KS + 6) >> 2) * 4096u);
    bload16<((KS + 6) & 3) * 1024>(S.xf[1][KS + 6], xv, rsX, xsb + 18432u + ((KS + 6) >> 2) * 4096u);
  } else if constexpr (KS < 15) { k1_piece<2 * (KS - 12)>(G); k1_piece<2 * (KS - 12) + 1>(G); }
  else k1_piece<KS - 9>(G);
  constexpr int YOUNGER = ABL1_NOSTAGE ? (KS < 12 ? 12 : 2 * (17 - KS)) : (KS < 15 ? 12 : 26 - KS);
  wait_vm2<YOUNGER>(S.xf[0][KS], S.xf[1][KS]);
  const s16x8 x0 = __builtin_bit_cast(s16x8, S.xf[0][KS]), x1 = __builtin_bit_cast(s16x8, S.xf[1][KS]);
  SB();
#pragma unroll
  for (int X = 0; X < 4; ++X) {
    Q[X][0] = mfma16(x0, wq[KS & 1][X], Q[X][0]);
    SB();
    if constexpr (KS + 1 < 18) wq[(KS + 1) & 1][X] = *LDSP(s16x8, qa + ((KS + 1) * 4 + X) * 1024u);
    SB();
    Q[X][1] = mfma16(x1, wq[KS & 1][X], Q[X][1]);
    SB();
  }
  if constexpr (KS + 1 < 18) k1_qkv_step<KS + 1>(S, wq, Q, qa, xv, rsX, xsb, G);
}

constexpr int K2D = 5;
constexpr unsigned L2_SLOT = 9216, L2_NS = K2D + 2;
constexpr unsigned L2_WO = L1_BUFA + 4 * L2_SLOT;
constexpr unsigned L2_MT = L1_BUFB + 3 * L2_SLOT, L2_KV = L1_SCR;
constexpr unsigned L2_OWN = L1_BUFB, L2_OUT = L1_BUFA;

struct K2S {
  u32x4 hr[K2D + 1][2];
  u32x4 ha[3][2];
  s16x4 wlo[6], whi[6];
  f32x4 acc[9][2];
  unsigned flag, ardm;
  unsigned ard;
};
struct K2C {
  rsrc_t rsH, rsW, rsB;
  unsigned vo0, vo1, hvoff, hsoF, hsoA, wsoF, xso, lb, lanec;
  int w, sp;
  gu32* chf;
  gu32* cxa;
};
constexpr bool k2_att(int n) { return n >= 18; }
constexpr unsigned k2_loc(int n) {
  return n < 8 ? 4 * (n >> 1) + (n & 1) : n < 10 ? 8 + n : 4 * ((n - 10) >> 1) + 2 + (n & 1);
}
constexpr unsigned k2_slot(int s) { return s < 4 ? L1_BUFA + s * L2_SLOT : L1_BUFB + (s - 4) * L2_SLOT; }
constexpr int k2_tW(int m) { return m == 4 ? -30 : 16 * (m - 5); }
constexpr int k2_tH(int m, int half) {
  return m <= 1 ? -40 + 2 * m + half : m <= 4 ? -24 + 2 * m + half : 16 * (m - 5) + 5 + 2 * half;
}
constexpr int k2_tA(int i) { return 16 * (6 + i) + 8; }
constexpr int k2_younger(int t0, int n) {
  int cnt = 0;
  const int tw = 16 * n + 3;
  for (int m = 4; m <= 17; ++m) if (k2_tW(m) > t0 && k2_tW(m) < tw) ++cnt;
  for (int m = 0; m <= 17; ++m)
    for (int h = 0; h < 2; ++h) if (k2_tH(m, h) > t0 && k2_tH(m, h) < tw) ++cnt;
  for (int i = 0; i < 6; ++i) if (k2_tA(i) > t0 && k2_tA(i) < tw) ++cnt;
  for (int i = 0; i < 4; ++i) if (-29 + i > t0) ++cnt;
  return cnt;
}
constexpr int k2_count(int n) {
  const int m = n + 1, th = k2_tH(m, 1), tw = m >= 4 ? k2_tW(m) : -100;
  return k2_younger(th > tw ? th : tw, n);
}
static_assert(k2_count(0) == 12 && k2_count(1) == 8 && k2_count(3) == 10 && k2_count(8) == 12 && k2_count(12) == 14 && k2_count(13) == 12, "schedule");
static_assert(k2_younger(16 * 3 + 4, 5) == 6, "schedule");
template <int T, int I, int ROLE = 0> DI void k2_issue(K2S& S, const K2C& C) {
  if constexpr (T <= 17) {
    constexpr unsigned ks = k2_loc(T);
    const unsigned wso = ABL2_NOW ? 0xf0000000u : C.wsoF + ks * 36864u, hso = ABL2_NOH ? 0xf0000000u : C.hsoF + ks * 131072u;
    if constexpr (I == 0) glds16<ROLE>(C.lb + k2_slot(T % L2_NS) + C.w * 1024u, C.vo0, C.rsW, wso);
    if constexpr (I == 1) { if ((T & 7) == C.w) glds16<3>(C.lb + k2_slot(T % L2_NS) + 8192u, C.vo1, C.rsW, wso); }
    if constexpr (I == 2) bload16_sc1<0, ROLE>(S.hr[T % (K2D + 1)][0], C.hvoff, C.rsH, hso);
    if constexpr (I == 3) bload16_sc1<1024, ROLE>(S.hr[T % (K2D + 1)][1], C.hvoff, C.rsH, hso);
  }
}
constexpr int K2_SEQ = 135;
constexpr int k2_T(int M) { return M < K2_SEQ ? M / 9 : 15 + (M - K2_SEQ) % 6; }
constexpr int k2_c(int M) { return M < K2_SEQ ? M % 9 : (M - K2_SEQ) / 6; }
template <int M, int H> DI void k2_read(K2S& S, const K2C& C) {
  if constexpr (M < 189) {
    constexpr int T = k2_T(M), c = k2_c(M);
    if constexpr (M < K2_SEQ) {
      if constexpr (H == 0 && c == 0) { S.ard = C.lanec + k2_slot(T % L2_NS); PIN(S.ard); }
      if constexpr (H == 0) S.wlo[M % 6] = trread(S.ard + c * 32);
      else S.whi[M % 6] = trread(S.ard + c * 32 + 4608);
    } else {
      static_assert(k2_slot(16 % L2_NS) == k2_slot(15 % L2_NS) + L2_SLOT && k2_slot(17 % L2_NS) == k2_slot(15 % L2_NS) + 2 * L2_SLOT, "ring");
      if constexpr (H == 0 && M == K2_SEQ) { S.ard = C.lanec + k2_slot(15 % L2_NS); PIN(S.ard); S.ardm = C.lanec + L2_MT; PIN(S.ardm); }
      const unsigned a = T <= 17 ? S.ard + (T - 15) * L2_SLOT + c * 32 : S.ardm + (T - 18) * L2_SLOT + c * 32;
      if constexpr (H == 0) S.wlo[M % 6] = trread(a);
      else S.whi[M % 6] = trread(a + 4608);
    }
  }
}
template <int c> DI void k2_send(K2S& S, const K2C& C) {
  if (C.w != C.sp) {
    const u32x4 v = {pack2(S.acc[c][0][0], S.acc[c][0][1]), pack2(S.acc[c][0][2], S.acc[c][0][3]),
                     pack2(S.acc[c][1][0], S.acc[c][1][1]), pack2(S.acc[c][1][2], S.acc[c][1][3])};
    bstore16_sc1<0>(v, C.hvoff, C.rsB, C.xso + c * 1024u);
  } else {
    *LDSP(f32x4, C.lb + L2_OWN + (c * 2) * 1024u + C.hvoff) = S.acc[c][0];
    *LDSP(f32x4, C.lb + L2_OWN + (c * 2 + 1) * 1024u + C.hvoff) = S.acc[c][1];
  }
}
template <int T, int N> DI void k2_wait(K2S& S) {
  asm volatile("s_waitcnt vmcnt(%2)\n\ts_barrier" : "+v"(S.hr[T % (K2D + 1)][0]), "+v"(S.hr[T % (K2D + 1)][1]) : "i"(N) : "memory");
}
template <int M> DI void k2_step(K2S& S, const K2C& C) {
  constexpr int T = k2_T(M), c = k2_c(M);
  constexpr bool seq = M < K2_SEQ;
  const s16x8 wf = __builtin_shufflevector(S.wlo[M % 6], S.whi[M % 6], 0, 1, 2, 3, 4, 5, 6, 7);
  const s16x8 h0 = __builtin_bit_cast(s16x8, k2_att(T) ? S.ha[k2_att(T) ? T - 18 : 0][0] : S.hr[T % (K2D + 1)][0]);
  const s16x8 h1 = __builtin_bit_cast(s16x8, k2_att(T) ? S.ha[k2_att(T) ? T - 18 : 0][1] : S.hr[T % (K2D + 1)][1]);
  const f32x4 z4 = {0.f, 0.f, 0.f, 0.f};
  S.acc[c][0] = mfma16(wf, h0, T == 0 ? z4 : S.acc[c][0]);
  SB();
  k2_read<M + 5, 0>(S, C);
  SB();
  S.acc[c][1] = mfma16(wf, h1, T == 0 ? z4 : S.acc[c][1]);
  SB();
  k2_read<M + 5, 1>(S, C);
  if constexpr (seq) {
    if constexpr (T + K2D <= 17) {
      if constexpr (c == 0) { if (!STAGGER || C.w < 4) k2_issue<T + K2D, 0, STAGGER>(S, C); }
      if constexpr (STAGGER && c == 1) { if (C.w >= 4) k2_issue<T + K2D, 0, 2>(S, C); }
      if constexpr (c == 2) k2_issue<T + K2D, 1>(S, C);
      if constexpr (c == (STAGGER ? 4 : 5)) { if (!STAGGER || C.w < 4) k2_issue<T + K2D, 2, STAGGER>(S, C); }
      if constexpr (STAGGER && c == 5) { if (C.w >= 4) k2_issue<T + K2D, 2, 2>(S, C); }
      if constexpr (c == (STAGGER ? 6 : 7)) { if (!STAGGER || C.w < 4) k2_issue<T + K2D, 3, STAGGER>(S, C); }
      if constexpr (STAGGER && c == 7) { if (C.w >= 4) k2_issue<T + K2D, 3, 2>(S, C); }
    }
    if constexpr (T >= 6 && T <= 11 && c == 8)
      bload16_sc1<((T - 6) & 1) * 1024>(S.ha[(T - 6) >> 1][(T - 6) & 1], C.hvoff, C.rsH, C.hsoA + ((T - 6) >> 1) * 131072u);
    if constexpr (T == 3 && c == 4) {
      if (C.w == 0) asm volatile("global_load_dword %0, %1, off sc1 ; role 3" : "=v"(S.flag) : "v"(C.chf));
    }
    if constexpr (T == 5 && c == 3) {
      if (C.w == 0) {
        asm volatile("s_waitcnt vmcnt(%1)" : "+v"(S.flag) : "i"(k2_younger(16 * 3 + 4, 5) - (STAGGER ? 1 : 0)));
        if (rfl(S.flag) < 4u) ctr_wait(C.chf, 4u);
      }
    }
    if constexpr (c == 3 && T < 14) k2_wait<T + 1, k2_count(T)>(S);
    if constexpr (c == 3 && T == 1) { if (threadIdx.x == 0) ctr_add(C.chf); }
    if constexpr (c == 2 && T == 14)
      asm volatile("s_waitcnt vmcnt(0)"
                   : "+v"(S.ha[0][0]), "+v"(S.ha[0][1]), "+v"(S.ha[1][0]), "+v"(S.ha[1][1]), "+v"(S.ha[2][0]), "+v"(S.ha[2][1]),
                     "+v"(S.hr[15 % (K2D + 1)][0]), "+v"(S.hr[15 % (K2D + 1)][1]), "+v"(S.hr[16 % (K2D + 1)][0]), "+v"(S.hr[16 % (K2D + 1)][1]),
                     "+v"(S.hr[17 % (K2D + 1)][0]), "+v"(S.hr[17 % (K2D + 1)][1]));
    if constexpr (c == 3 && T == 14) asm volatile("s_barrier" ::: "memory");
  } else {
    if constexpr (T == 20 && c > 0) k2_send<c - 1>(S, C);
    if constexpr (T == 15 && c == 7) {
      asm volatile("s_waitcnt vmcnt(1)\n\ts_barrier" ::: "memory");
      if (C.w == 0 && (C.hvoff >> 4) < 8) ctr_add(C.cxa + (C.hvoff >> 4) * 32);
    }
  }
  SB();
  if constexpr (seq && c == 8 && T % 5 == 4) { STAMP(S.dg, 18 + T / 5) }
  if constexpr (M + 1 < 189) k2_step<M + 1>(S, C);
}

__global__ __launch_bounds__(512, 2) void kf_main(KFP p) {
  __shared__ __attribute__((aligned(128))) char smem[L1_TOTAL];
  const unsigned lb = (unsigned)(size_t)(__attribute__((address_space(3))) char*)smem;
  const int tid = threadIdx.x, l = tid & 63;
  const int w = rfl(tid >> 6);
  const int b = blockIdx.x, xc = b & 7, r = b >> 3;
  const int cr = r & 3, j = 4 * xc + cr, ib = r >> 2;
  const int g = l >> 4, q = (l & 15) >> 2, pp = l & 3, c16 = l & 15;

  STAMPR(p.dg, 30) STAMP(p.dg, 0)
  const rsrc_t rsX = mk_rsrc(p.xf, (unsigned)SZ_XF);
  const rsrc_t rsQ = mk_rsrc(p.wq, (unsigned)SZ_WQ);
  const rsrc_t rsG = mk_rsrc(p.wg, D_ * F_ * 2);
  const rsrc_t rsU = mk_rsrc(p.wu, D_ * F_ * 2);
  const rsrc_t rsH = mk_rsrc(p.hf, (unsigned)SZ_HF);
  gu32* const ctr = (gu32*)p.ctr;
  const unsigned long long t_start = __builtin_amdgcn_s_memrealtime();
  K1S S;

  if (!NO_CODEWARM) {
    unsigned long long pc;
    unsigned len;
    asm volatile("s_getpc_b64 %0\n98764:\n\ts_mov_b32 %1, 98765f-98764b" : "=s"(pc), "=s"(len));
    warm_lines(lb + L1_SCR + 15872u, l < 2 ? ((r * 8 + w) * 2 + l) * 128u : 0xf0000000u, mk_rsrc((const void*)pc, len));
  }

#pragma unroll
  for (int i = 0; i < 9; ++i) glds16(lb + L1_BUFA + (9 * w + i) * 1024u, l * 16u, rsQ, (j * 72u + 9 * w + i) * 1024u);
  const int T0 = 16 * ib + 2 * w;
  const unsigned hvoff = l * 16u, xsb = T0 * 18432u;
#define XL(s) bload16<((s) & 3) * 1024>(S.xf[0][s], hvoff, rsX, xsb + ((s) >> 2) * 4096u); \
              bload16<((s) & 3) * 1024>(S.xf[1][s], hvoff, rsX, xsb + 18432u + ((s) >> 2) * 4096u);
  XL(0) XL(1) XL(2) XL(3) XL(4) XL(5)
#undef XL
  K1G G;
  G.w = w;
  const unsigned wcol = (576u * xc + 128u * cr) * 2u;
  {
    const unsigned rowv = (l >> 3) * 9216u, c8 = l & 7, s0 = (l >> 4) & 1;
    const unsigned vA = rowv + ((c8 ^ (s0 << 1)) << 4), vB = rowv + ((c8 ^ ((s0 | 2) << 1)) << 4);
    G.vo0 = (w & 1) ? vB : vA;
    G.vo1 = (w & 1) ? vA : vB;
  }
  G.sstep = 8u * 9216u;
  const unsigned wsoff = 72u * w * 9216u, wdst = lb + 72u * w * 128u;
  G.rs = rsG; G.soff = wsoff + wcol; G.dst = wdst + L1_BUFB;

  STAMP(p.dg, 1)
  wait_bar<12>();
  STAMP(p.dg, 2)

  {
    const unsigned qa = lb + L1_BUFA + l * 16u;
    const f32x4 z4 = {0.f, 0.f, 0.f, 0.f};
    f32x4 Q[4][2];
#pragma unroll
    for (int X = 0; X < 4; ++X) Q[X][0] = Q[X][1] = z4;
    s16x8 wq[2][4];
#pragma unroll
    for (int X = 0; X < 4; ++X) wq[0][X] = *LDSP(s16x8, qa + X * 1024u);
    k1_qkv_step<0>(S, wq, Q, qa, hvoff, rsX, xsb, G);
    STAMP(p.dg, 3)
    unsigned fKA[4], fKB[4], fVA[4], fVB[4], fQ2[4];
#pragma unroll
    for (int T = 0; T < 2; ++T)
#pragma unroll
      for (int h2 = 0; h2 < 2; ++h2) {
        fKA[2 * T + h2] = pack2(phi_f(Q[0][T][2 * h2]), phi_f(Q[0][T][2 * h2 + 1]));
        fKB[2 * T + h2] = pack2(phi_f(Q[1][T][2 * h2]), phi_f(Q[1][T][2 * h2 + 1]));
        fVA[2 * T + h2] = pack2(Q[2][T][2 * h2], Q[2][T][2 * h2 + 1]);
        const unsigned raw = pack2(Q[3][T][2 * h2], Q[3][T][2 * h2 + 1]);
        fVB[2 * T + h2] = c16 < 2 ? 0x3f803f80u : raw;
        fQ2[2 * T + h2] = pack2(phi_f(Q[3][T][2 * h2]), phi_f(Q[3][T][2 * h2 + 1]));
      }
    const s16x8 aKA = mk_frag(fKA[0], fKA[1], fKA[2], fKA[3]), aKB = mk_frag(fKB[0], fKB[1], fKB[2], fKB[3]);
    const s16x8 bVA = mk_frag(fVA[0], fVA[1], fVA[2], fVA[3]), bVB = mk_frag(fVB[0], fVB[1], fVB[2], fVB[3]);
    const s16x8 aQ2 = mk_frag(fQ2[0], fQ2[1], fQ2[2], fQ2[3]);
    const f32x4 R0 = mfma16(aKA, bVA, z4), R1 = mfma16(aKA, bVB, z4);
    const f32x4 R2 = mfma16(aKB, bVA, z4), R3 = mfma16(aKB, bVB, z4);
    {
      const unsigned one = (c16 >> 2) == g ? (0x3f80u << (16 * (c16 & 1))) : 0u;
      const unsigned i0 = (c16 & 2) ? 0u : one, i1 = (c16 & 2) ? one : 0u;
      const rsrc_t rsP = mk_rsrc(p.pq, (unsigned)SZ_PQ);
#pragma unroll
      for (int T = 0; T < 2; ++T) {
        const s16x8 id = T == 0 ? mk_frag(i0, i1, 0u, 0u) : mk_frag(0u, 0u, i0, i1);
        const f32x4 qa4 = mfma16(aKB, id, z4), qb4 = mfma16(aQ2, id, z4);
        const unsigned b01 = pack2(qb4[0], qb4[1]), b23 = pack2(qb4[2], qb4[3]);
        bstore16_sc1<0>(u32x4{pack2(qa4[0], qa4[1]), pack2(qa4[2], qa4[3]), g == 1 ? b01 : (g == 2 ? b23 : 0u), g == 1 ? b23 : 0u},
                        hvoff + T * 1024u, rsP, (b * 8 + w) * 2048u);
      }
    }
    {
      const unsigned sb = lb + L1_SCR + w * 1408u;
      const int eB = c16 == 0 ? 18 : c16 + 14;
      const bool okB = c16 == 0 || c16 == 2 || c16 == 3;
      const unsigned dummy = lb + L1_SCR + 8 * 1408u + l * 4u;
#pragma unroll
      for (int rr = 0; rr < 4; ++rr) {
        const int d = 4 * g + rr;
        *LDSP(float, sb + (d * 19 + c16) * 4) = R0[rr];
        *LDSP(float, okB ? sb + (d * 19 + eB) * 4 : dummy) = R1[rr];
        if (rr >= 2) {
          *LDSP(float, g == 0 ? sb + ((14 + rr) * 19 + c16) * 4 : dummy) = R2[rr];
          *LDSP(float, g == 0 && okB ? sb + ((14 + rr) * 19 + eB) * 4 : dummy) = R3[rr];
        }
      }
    }
  }
  STAMP(p.dg, 4)
  wait_bar<2>();
  STAMP(p.dg, 5)

  const unsigned lane0 = lb + (8 * g + q) * 128u + 8u * pp + ((((q >> 1) & 1) | ((g & 1) << 1)) << 5);
  const unsigned abA = lane0 + L1_BUFA, abB = lane0 + L1_BUFB;
  const unsigned ks0 = 18u * xc + 4u * cr;
  const unsigned hs0 = (ks0 * 128u + T0) * 1024u;

  k1_first_reads<4096>(S, abB);
  const rsrc_t rsKV = mk_rsrc(p.kv, (unsigned)SZ_KV);
  if (tid < 88) {
    f32x4 a = *LDSP(f32x4, lb + L1_SCR + tid * 16);
#pragma unroll
    for (int ww = 1; ww < 8; ++ww) a += *LDSP(f32x4, lb + L1_SCR + ww * 1408u + tid * 16);
    bstore16_sc1<0>(__builtin_bit_cast(u32x4, a), tid * 16u, rsKV, (j * 8 + ib) * 1408u);
  }
  SB();
  G.wm0 = lb + L1_SCR + 15872u;
  G.wvo = l < 9 ? (72u * ib + 9u * w + l) * 9216u + wcol + 128u : 0xf0000000u;
  G.wrs = rsG;
  G.wvo2 = w < 3 ? (l < 54 ? (576u * xc * 9u + r * 162u + w * 54u + l) * 128u : 0xf0000000u)
         : w == 3 ? (l < 21 && r * 21 + l < 648 ? (72u * xc * 9u + r * 21u + l) * 128u : 0xf0000000u)
                  : (l < 18 ? (72u * ib + 18u * (w - 4) + l) * 9216u + wcol + 128u : 0xf0000000u);
  G.wrs2 = w < 3 ? mk_rsrc(p.wd, F_ * D_ * 2) : w == 3 ? mk_rsrc(p.wo, D_ * D_ * 2) : rsU;
  gu32* const ckv = ctr + (CT_KV + xc) * 32;
  const unsigned gvo0 = G.vo0, gvo1 = G.vo1;
  G.rs = rsU; G.soff = wsoff + wcol; G.dst = wdst + L1_BUFA;
  gu32* const chA = ctr + (CT_H + xc * 8 + ib) * 32;
  k1_step<1, 0, 4, 4096, 4096, 1, 1, 0>(S, abB, abA, G, rsH, hvoff, 0, 0, ckv, chA);
  STAMP(p.dg, 6)
  G.rs = (w >> 2) ? rsU : rsG;
  G.vo0 = G.vo1 = (l >> 2) * 9216u + (((l & 3) ^ ((l >> 5) << 1)) * 16u);
  G.sstep = 16u * 9216u;
  G.soff = 144u * (w & 3) * 9216u + (576u * xc + 512u + 32u * (cr >> 1)) * 2u;
  G.dst = lb + L1_BUFB + (w >> 2) * 36864u + 144u * (w & 3) * 64u;
  const unsigned abS = lb + L1_BUFB + (8 * g + q) * 64u + 8u * pp + (((cr & 1) ^ (g & 1)) << 5);
  k1_step<2, 1, 4, 4096, 2048, 1, 0, 0>(S, abA, abS, G, rsH, hvoff, hs0, 0, ckv, chA);
  STAMP(p.dg, 8)
  G.rs = rsG; G.vo0 = gvo0; G.vo1 = gvo1; G.sstep = 8u * 9216u; G.soff = wsoff + wcol + 128u; G.dst = wdst + L1_BUFA;
  k1_step<0, 2, 2, 2048, 4096, 1, 0, 0>(S, abS, abA, G, rsH, hvoff, 0, hs0 + 131072u, ckv, chA);
  STAMP(p.dg, 10)
  G.rs = rsU; G.soff = wsoff + wcol + 128u; G.dst = wdst + L1_BUFB;
  k1_step<1, 3, 4, 4096, 4096, 1, 2, 0>(S, abA, abB, G, rsH, hvoff, 0,
                                        ((18u * xc + 16u + (cr >> 1)) * 128u + T0) * 1024u + (cr & 1) * 8u, ckv, chA);
  STAMP(p.dg, 12)
  const rsrc_t rsW = mk_rsrc(p.wd, F_ * D_ * 2);
  const unsigned wsoF = 18u * xc * 36864u + cr * 288u;
  G.rs = w < 4 ? rsW : mk_rsrc(p.wo, D_ * D_ * 2);
  G.soff = w < 4 ? wsoF + (4 * (w >> 1) + (w & 1)) * 36864u : 18u * (4 * xc + w - 4) * 1152u + cr * 288u;
  G.dst = lb + L1_BUFA + w * L2_SLOT;
  G.rsk = rsKV;
  { const unsigned hh = tid / 88, q4 = tid - 88 * hh; G.vok = tid < 352 ? ((4 * xc + hh) * 8 * 352 + q4 * 4) * 4u : 0xf0000000u; }
  k1_step<2, 1, 4, 4096, 0, 2, 0, 0>(S, abB, 0, G, rsH, hvoff, hs0 + 2 * 131072u, 0, ckv, chA);
  u32x4 pqv[2];
  {
    const rsrc_t rsP = mk_rsrc(p.pq, (unsigned)SZ_PQ);
    bload16<0>(pqv[0], hvoff, rsP, (b * 8 + w) * 2048u);
    bload16<1024>(pqv[1], hvoff, rsP, (b * 8 + w) * 2048u);
  }
  asm volatile("s_waitcnt vmcnt(4)" : "+v"(S.kp[0]), "+v"(S.kp[1]), "+v"(S.kp[2]), "+v"(S.kp[3]), "+v"(S.kp[4]), "+v"(S.kp[5]), "+v"(S.kp[6]), "+v"(S.kp[7]));
  {
    f32x4 a = __builtin_bit_cast(f32x4, S.kp[0]);
#pragma unroll
    for (int i = 1; i < 8; ++i) a += __builtin_bit_cast(f32x4, S.kp[i]);
    if (tid < 352) *LDSP(f32x4, lb + L2_KV + tid * 16u) = a;
  }
  if (w == 0) ctr_wait(chA, 4u);
  k1_epi_all<2, 3, 1>(S);
  k1_store_pair(S, rsH, hvoff, hs0 + 3 * 131072u);
  while (__builtin_amdgcn_s_memrealtime() - t_start < 2200ull) __builtin_amdgcn_s_sleep(1);
  STAMP(p.dg, 14) STAMPR(p.dg, 27)
  asm volatile("s_waitcnt lgkmcnt(0)\n\ts_barrier" ::: "memory");
  STAMP(p.dg, 15)

  K2S S2;
  K2C C;
  C.rsH = rsH; C.rsW = rsW; C.w = w; C.sp = xc; C.lb = lb;
  C.rsB = mk_rsrc(p.xb, (unsigned)SZ_XB);
  C.xso = (((ib * 4 + cr) * 8 + w) * 8 + xc) * 9216u;
  C.cxa = ctr + (CT_X + (ib * 4 + cr) * 8) * 32;
  { const int ci = 64 * w + l, rho = ci / 18, c = ci - 18 * rho; C.vo0 = rho * 1152u + c * 16u; }
  { const int ci = 512 + l, rho = ci / 18, c = ci - 18 * rho; C.vo1 = rho * 1152u + c * 16u; }
  C.hvoff = hvoff;
  const unsigned hT = T0 * 1024u;
  C.hsoF = hT + 18u * xc * 131072u;
  C.wsoF = wsoF;
  C.lanec = lb + (4 * g + q) * 288u + 8u * pp;

  k2_issue<0, 2>(S2, C); k2_issue<0, 3>(S2, C); k2_issue<1, 2>(S2, C); k2_issue<1, 3>(S2, C);
  k2_issue<4, 0>(S2, C); k2_issue<4, 1>(S2, C);
  STAMP(p.dg, 16)
  {
    float za[4], zb[4];
#pragma unroll
    for (int e = 0; e < 4; ++e) {
      const int da = 4 * (g - 1) + e, db = da + 12;
      const float ta = *LDSP(float, lb + L2_KV + (cr * 352 + (g > 0 ? da * 19 + 18 : 0)) * 4);
      const float tb = *LDSP(float, lb + L2_KV + (cr * 352 + (g > 0 && db < 18 ? db * 19 + 18 : 0)) * 4);
      za[e] = g > 0 ? ta : 0.f;
      zb[e] = g > 0 && db < 18 ? tb : 0.f;
    }
    const int piA = 6 * cr + g - 1, piB = 6 * cr + g + 2;
    const unsigned base = (KS_FFN + 3u * xc) * 131072u + T0 * 1024u + c16 * 16u;
    const unsigned oA = g == 0 ? 0xf0000000u : base + (piA >> 3) * 131072u + (piA & 3) * 256u + ((piA >> 2) & 1) * 8u;
    const unsigned oB = g == 0 ? 0xf0000000u : base + (piB >> 3) * 131072u + (piB & 3) * 256u + ((piB >> 2) & 1) * 8u;
    asm volatile("s_waitcnt vmcnt(7)" : "+v"(pqv[0]), "+v"(pqv[1]));
#pragma unroll
    for (int T = 0; T < 2; ++T) {
      const u32x4 u = pqv[T];
      const float v[8] = {bf_lo(u[0]), bf_hi(u[0]), bf_lo(u[1]), bf_hi(u[1]), bf_lo(u[2]), bf_hi(u[2]), bf_lo(u[3]), bf_hi(u[3])};
      float den = 0.f;
#pragma unroll
      for (int e = 0; e < 4; ++e) den += v[e] * za[e] + v[4 + e] * zb[e];
      {
        const unsigned d0 = __float_as_uint(den);
        const auto r16 = __builtin_amdgcn_permlane16_swap(d0, d0, false, false);
        const unsigned r0 = r16[0], r1 = r16[1];
        const unsigned d1 = __float_as_uint(__uint_as_float(r0) + __uint_as_float(r1));
        const auto r32 = __builtin_amdgcn_permlane32_swap(d1, d1, false, false);
        const unsigned s0 = r32[0], s1 = r32[1];
        den = __uint_as_float(s0) + __uint_as_float(s1);
      }
      const float inv = frcp(den + 1e-6f);
      const u32x2 va = {pack2(v[0] * inv, v[1] * inv), pack2(v[2] * inv, v[3] * inv)};
      const u32x2 vb = {pack2(v[4] * inv, v[5] * inv), pack2(v[6] * inv, v[7] * inv)};
      __builtin_amdgcn_raw_buffer_store_b64(va, rsH, oA + T * 1024u, 0, AUX_SC1);
      __builtin_amdgcn_raw_buffer_store_b64(vb, rsH, oB + T * 1024u, 0, AUX_SC1);
    }
  }
  SB();
  STAMP(p.dg, 7)
  const f32x4 z4 = {0.f, 0.f, 0.f, 0.f};
  {
    const int hh = w >> 1, d = 16 * (w & 1) + c16;
    const unsigned ao = lb + L2_WO + hh * L2_SLOT + (8 * g + q) * 288u + 8u * pp;
    float v[8];
    const unsigned ka0 = lb + L2_KV + (hh * 352 + (d < 18 ? d : 0) * 19 + 8 * (g < 2 ? g : 2)) * 4;
#pragma unroll
    for (int e = 0; e < 8; ++e) v[e] = *LDSP(float, ka0 + e * 4);
    s16x8 A[9];
#pragma unroll
    for (int c = 0; c < 9; ++c) A[c] = trfrag(ao + c * 32, ao + c * 32 + 4 * 288);
    unsigned bh[4], bl[4];
#pragma unroll
    for (int e2 = 0; e2 < 4; ++e2) {
      const float v0 = (d < 18 && 8 * g + 2 * e2 < 18) ? v[2 * e2] : 0.f, v1 = (d < 18 && 8 * g + 2 * e2 + 1 < 18) ? v[2 * e2 + 1] : 0.f;
      bh[e2] = pack2(v0, v1);
      bl[e2] = pack2(v0 - bf_lo(bh[e2]), v1 - bf_hi(bh[e2]));
    }
    const s16x8 Bh = mk_frag(bh[0], bh[1], bh[2], bh[3]), Bl = mk_frag(bl[0], bl[1], bl[2], bl[3]);
    const int ka = 24 * hh + d;
    const unsigned mo = d < 24 ? lb + L2_MT + (ka >> 5) * L2_SLOT + (ka & 31) * 288u + 8u * g : lb + k2_slot(L2_NS - 1) + l * 8u;
    f32x4 D[9];
#pragma unroll
    for (int c = 0; c < 9; ++c) D[c] = mfma16(A[c], Bh, z4);
#pragma unroll
    for (int c = 0; c < 9; ++c) D[c] = mfma16(A[c], Bl, D[c]);
#pragma unroll
    for (int c = 0; c < 9; ++c) *LDSP(u32x2, mo + c * 32) = u32x2{pack2(D[c][0], D[c][1]), pack2(D[c][2], D[c][3])};
  }
  SB();
  STAMP(p.dg, 9)
  k2_issue<2, 2>(S2, C); k2_issue<2, 3>(S2, C); k2_issue<3, 2>(S2, C); k2_issue<3, 3>(S2, C);
  k2_issue<4, 2>(S2, C); k2_issue<4, 3>(S2, C);
  STAMP(p.dg, 11)
  asm volatile("s_waitcnt lgkmcnt(0)\n\ts_barrier" ::: "memory");
  C.chf = ctr + (CT_H2 + xc * 8 + ib) * 32;
  STAMP(p.dg, 17)
  C.hsoA = hT + (KS_FFN + 3u * xc) * 131072u;
  asm volatile("s_waitcnt vmcnt(13)" : "+v"(S2.hr[0][0]), "+v"(S2.hr[0][1]));
  k2_read<0, 0>(S2, C); k2_read<0, 1>(S2, C); k2_read<1, 0>(S2, C); k2_read<1, 1>(S2, C); k2_read<2, 0>(S2, C);
  k2_read<2, 1>(S2, C); k2_read<3, 0>(S2, C); k2_read<3, 1>(S2, C); k2_read<4, 0>(S2, C); k2_read<4, 1>(S2, C);
  SB();
  k2_step<0>(S2, C);
  STAMP(p.dg, 23) STAMPR(p.dg, 28)

  const int sp = xc, ni = cr, mi = ib;
  const int grp = mi * 4 + ni;
  const rsrc_t rsB = C.rsB;
  k2_send<8>(S2, C);
  const int tok0 = 256 * mi + 32 * sp;
  const rsrc_t rsXr = mk_rsrc(p.x, S_ * D_ * 2);
  u32x4 xr[2];
  unsigned xo[2];
#pragma unroll
  for (int i = 0; i < 2; ++i) {
    const int it = tid + 512 * i, tk = it / 18, ch = it - tk * 18;
    xo[i] = it < 576 ? ((tok0 + tk) * D_ + 144 * ni + 8 * ch) * 2u : 0xf0000000u;
  }
  bload16<0>(xr[0], xo[0], rsXr, 0);
  bload16<0>(xr[1], xo[1], rsXr, 0);
  if (w == sp) ctr_wait(C.cxa + sp * 32, 8u);
  asm volatile("s_waitcnt lgkmcnt(0)\n\ts_barrier" ::: "memory");
  STAMP(p.dg, 24)
  u32x4 v[8];
  auto fetch = [&](int c) {
#pragma unroll
    for (int src = 0; src < 8; ++src)
      bload16_sc1<0>(v[src], src == sp ? 0xf0000000u : hvoff + c * 1024u, rsB, ((grp * 8 + sp) * 8 + src) * 9216u);
  };
  auto reduce = [&](int c) {
    asm volatile("s_waitcnt vmcnt(0)" : "+v"(v[0]), "+v"(v[1]), "+v"(v[2]), "+v"(v[3]), "+v"(v[4]), "+v"(v[5]), "+v"(v[6]), "+v"(v[7]));
    f32x4 a0 = *LDSP(f32x4, lb + L2_OWN + (2 * c) * 1024u + l * 16u), a1 = *LDSP(f32x4, lb + L2_OWN + (2 * c + 1) * 1024u + l * 16u);
#pragma unroll
    for (int src = 0; src < 8; ++src) {
      a0 += f32x4{bf_lo(v[src][0]), bf_hi(v[src][0]), bf_lo(v[src][1]), bf_hi(v[src][1])};
      a1 += f32x4{bf_lo(v[src][2]), bf_hi(v[src][2]), bf_lo(v[src][3]), bf_hi(v[src][3])};
    }
    *LDSP(f32x4, lb + L2_OUT + (c16 * 148 + 16 * c + 4 * g) * 4) = a0;
    *LDSP(f32x4, lb + L2_OUT + ((16 + c16) * 148 + 16 * c + 4 * g) * 4) = a1;
  };
  if (w < 5) {
    fetch(w);
    asm volatile("s_waitcnt vmcnt(8)\n\ts_barrier" ::: "memory");
  } else {
    asm volatile("s_waitcnt vmcnt(0)\n\ts_barrier" ::: "memory");
  }
  gu32* const cxb = ctr + (CT_X2 + grp * 8) * 32;
  if (w == 7) {
    if (l < 8) ctr_add(cxb + l * 32);
    ctr_wait(cxb + sp * 32, 8u);
  }
  if (w < 5) reduce(w);
  asm volatile("s_waitcnt vmcnt(0) lgkmcnt(0)\n\ts_barrier" ::: "memory");
  STAMP(p.dg, 25) STAMPR(p.dg, 29)
  if (w >= 4) { fetch(w + 1); reduce(w + 1); }
  asm volatile("s_waitcnt vmcnt(0)" : "+v"(xr[0]), "+v"(xr[1]));
  __syncthreads();
#pragma unroll
  for (int i = 0; i < 2; ++i) {
    const int it = tid + 512 * i;
    if (it < 576) {
      const int tk = it / 18, ch = it - tk * 18;
      const f32x4 a = *LDSP(f32x4, lb + L2_OUT + (tk * 148 + 8 * ch) * 4), c = *LDSP(f32x4, lb + L2_OUT + (tk * 148 + 8 * ch + 4) * 4);
      u32x4 ov;
      ov[0] = pack2(a[0] + bf_lo(xr[i][0]), a[1] + bf_hi(xr[i][0]));
      ov[1] = pack2(a[2] + bf_lo(xr[i][1]), a[3] + bf_hi(xr[i][1]));
      ov[2] = pack2(c[0] + bf_lo(xr[i][2]), c[1] + bf_hi(xr[i][2]));
      ov[3] = pack2(c[2] + bf_lo(xr[i][3]), c[3] + bf_hi(xr[i][3]));
      *(u32x4*)((char*)p.out + xo[i]) = ov;
    }
  }
  STAMP(p.dg, 26) STAMPR(p.dg, 31) STAMP_DUMP(p.dg)
  asm volatile("98765:");
}

extern "C" void kernel_launch(void* const* d_in, const int* in_sizes, int n_in, void* d_out, int out_size,
                              void* d_ws, size_t ws_size, hipStream_t stream) {
  const uint16_t* x = (const uint16_t*)d_in[0];
  const void* wqkv = d_in[1];
  const uint16_t* wo = (const uint16_t*)d_in[2];
  const void* wg = d_in[4];
  const void* wu = d_in[5];
  const void* wd = d_in[6];
  char* ws = (char*)d_ws;

  k0_ln<<<256, 512, 0, stream>>>(x, (u32x4*)(ws + OFF_XF), (const uint16_t*)wqkv, (u32x4*)(ws + OFF_WQ),
                                 (unsigned*)(ws + OFF_CT));
  KFP p;
  p.xf = ws + OFF_XF; p.wq = ws + OFF_WQ; p.wg = wg; p.wu = wu; p.wd = wd; p.wo = wo; p.x = x;
  p.hf = ws + OFF_HF; p.kv = ws + OFF_KV; p.xb = ws + OFF_XB; p.pq = ws + OFF_PQ; p.ctr = ws + OFF_CT; p.out = d_out;
  kf_main<<<256, 512, 0, stream>>>(p);
}
```

```cpp
#include <hip/hip_runtime.h>
#include <hip/hip_bf16.h>
#include <stdint.h>

#define DI __device__ __forceinline__
typedef __attribute__((ext_vector_type(8))) short s16x8;
typedef __attribute__((ext_vector_type(4))) short s16x4;
typedef __attribute__((ext_vector_type(4))) float f32x4;
typedef __attribute__((ext_vector_type(4))) unsigned u32x4;
typedef __attribute__((ext_vector_type(2))) unsigned u32x2;
typedef __amdgpu_buffer_rsrc_t rsrc_t;
#define LDSP(T, a) ((__attribute__((address_space(3))) T*)(size_t)(a))

constexpr int S_ = 2048, D_ = 576, F_ = 4608, NH = 32, DH = 18;
constexpr int KS_FFN = 144, KS_ALL = 168;
constexpr size_t OFF_XF = 0;
constexpr size_t SZ_XF = (size_t)128 * 18 * 1024;
constexpr size_t OFF_WQ = OFF_XF + SZ_XF;
constexpr size_t SZ_WQ = (size_t)32 * 72 * 1024;
constexpr size_t OFF_HF = OFF_WQ + SZ_WQ;
constexpr size_t SZ_HF = (size_t)KS_ALL * 128 * 1024;
constexpr size_t OFF_KV = OFF_HF + SZ_HF;
constexpr size_t SZ_KV = (size_t)32 * 8 * 352 * 4;
constexpr size_t OFF_XB = OFF_KV + SZ_KV;
constexpr size_t SZ_XB = (size_t)32 * 8 * 8 * 9 * 1024;
constexpr size_t OFF_CT = OFF_XB + SZ_XB;
constexpr int CT_X = 0, CT_X2 = 256, CT_KV = 512, CT_H = 520, CT_H2 = 584, CT_N = 648;
constexpr size_t SZ_CT = (size_t)CT_N * 128;

DI rsrc_t mk_rsrc(const void* p, unsigned bytes) {
  return __builtin_amdgcn_make_buffer_rsrc((void*)p, (short)0, (int)bytes, 0x00020000);
}
DI unsigned rfl(unsigned v) { return __builtin_amdgcn_readfirstlane(v); }
DI float bf_lo(unsigned u) { return __uint_as_float(u << 16); }
DI float bf_hi(unsigned u) { return __uint_as_float(u & 0xffff0000u); }
DI unsigned pack2(float lo, float hi) {
  typedef __attribute__((ext_vector_type(2))) __bf16 bf2;
  typedef __attribute__((ext_vector_type(2))) float f2;
  f2 v = {lo, hi};
  bf2 r = __builtin_convertvector(v, bf2);
  return __builtin_bit_cast(unsigned, r);
}
DI s16x8 mk_frag(unsigned a, unsigned b, unsigned c, unsigned d) {
  u32x4 v = {a, b, c, d};
  return __builtin_bit_cast(s16x8, v);
}
DI f32x4 mfma16(s16x8 a, s16x8 b, f32x4 c) { return __builtin_amdgcn_mfma_f32_16x16x32_bf16(a, b, c, 0, 0, 0); }
DI s16x4 trread(unsigned addr) { return __builtin_amdgcn_ds_read_tr16_b64_v4i16(LDSP(s16x4, addr)); }
DI s16x8 trfrag(unsigned a0, unsigned a1) {
  s16x4 lo = trread(a0), hi = trread(a1);
  return __builtin_shufflevector(lo, hi, 0, 1, 2, 3, 4, 5, 6, 7);
}
template <int ROLE = 0> DI void glds16(unsigned m0v, unsigned voff, rsrc_t rs, unsigned soff) {
  asm volatile("s_mov_b32 m0, %0\n\ts_nop 0\n\tbuffer_load_dwordx4 %1, %2, %3 offen lds ; role %4"
               :: "s"(m0v), "v"(voff), "s"(rs), "s"(soff), "i"(ROLE) : "memory");
}
DI void warm_lines(unsigned m0v, unsigned voff, rsrc_t rs) {
  asm volatile("s_mov_b32 m0, %0\n\ts_nop 0\n\tbuffer_load_dword %1, %2, 0 offen lds" :: "s"(m0v), "v"(voff), "s"(rs) : "memory");
}
template <int IMM> DI void bload16(u32x4& d, unsigned voff, rsrc_t rs, unsigned soff) {
  asm volatile("buffer_load_dwordx4 %0, %1, %2, %3 offen offset:%4"
               : "=v"(d) : "v"(voff), "s"(rs), "s"(soff), "i"(IMM));
}
template <int IMM, int ROLE = 0> DI void bload16_sc1(u32x4& d, unsigned voff, rsrc_t rs, unsigned soff) {
  asm volatile("buffer_load_dwordx4 %0, %1, %2, %3 offen offset:%4 sc1 ; role %5"
               : "=v"(d) : "v"(voff), "s"(rs), "s"(soff), "i"(IMM), "i"(ROLE));
}
template <int IMM> DI void bstore16_sc1(u32x4 v, unsigned voff, rsrc_t rs, unsigned soff) {
  asm volatile("buffer_store_dwordx4 %0, %1, %2, %3 offen offset:%4 sc1\n\ts_nop 1"
               :: "v"(v), "v"(voff), "s"(rs), "s"(soff), "i"(IMM) : "memory");
}
typedef __attribute__((address_space(1))) unsigned gu32;
DI void ctr_add(gu32* c) { __hip_atomic_fetch_add(c, 1u, __ATOMIC_RELAXED, __HIP_MEMORY_SCOPE_AGENT); }
DI void ctr_wait(gu32* c, unsigned n) {
  for (unsigned spins = 0; spins < (1u << 20); ++spins) {
    if ((unsigned)__builtin_amdgcn_readfirstlane(__hip_atomic_load(c, __ATOMIC_RELAXED, __HIP_MEMORY_SCOPE_AGENT)) >= n) break;
    __builtin_amdgcn_s_sleep(1);
  }
}
template <int N> DI void wait_vm2(u32x4& a, u32x4& b) {
  asm volatile("s_waitcnt vmcnt(%2)" : "+v"(a), "+v"(b) : "i"(N));
}
template <int N> DI void wait_bar_nolgkm() {
  asm volatile("s_waitcnt vmcnt(%0)\n\ts_barrier" :: "i"(N) : "memory");
}
template <int N> DI void wait_bar() {
  asm volatile("s_waitcnt vmcnt(%0) lgkmcnt(0)\n\ts_barrier" :: "i"(N) : "memory");
}
DI float fexp2(float x) { return __builtin_amdgcn_exp2f(x); }
DI float frcp(float x) { return __builtin_amdgcn_rcpf(x); }
DI float phi_f(float t) { return (t > 0.f ? t + 1.f : fexp2(t * 1.4426950408889634f)) + 1e-6f; }
DI float geglu(float g, float u) {
  float e = fexp2(g * __builtin_fmaf(g * g, -0.10294323970f, -2.30220819814f));
  return (g * u) * frcp(1.f + e);
}
template <int CTRL> DI float dpp_add(float v) {
  return v + __builtin_bit_cast(float, __builtin_amdgcn_update_dpp(0, __builtin_bit_cast(int, v), CTRL, 0xf, 0xf, true));
}
DI float reduce32(float v) {
  v = dpp_add<0xB1>(v);
  v = dpp_add<0x4E>(v);
  v = dpp_add<0x141>(v);
  v = dpp_add<0x140>(v);
  const unsigned u = __float_as_uint(v);
  const auto r = __builtin_amdgcn_permlane16_swap(u, u, false, false);
  const unsigned r0 = r[0], r1 = r[1];
  return __uint_as_float(r0) + __uint_as_float(r1);
}

#define ABL2_NOW 0
#define ABL2_NOH 0
#define K0_NORETILE 0
#define K0_NOLN 0
#define STAGGER 0
#define NO_CODEWARM 0
#define NO_WARM0 1
#define WARM_MODE 2
#define ABL_NOW 0
#define ABL_NOH 0
#define ABL_NOCOMP 0
#define ABL1_NOCOMP 0
#define ABL1_NOEPI 0
#define ABL1_NOSTAGE 0
#define STAMP(buf, slot)
#define STAMPR(buf, slot)
#define STAMP_DUMP(buf)
__global__ __launch_bounds__(512, 2) void k0_ln(const uint16_t* __restrict__ x, u32x4* __restrict__ xf,
                                                const uint16_t* __restrict__ wqkv, u32x4* __restrict__ wq,
                                                unsigned* __restrict__ ctr) {
  __shared__ u32x4 stage[4][288];
  const int l = threadIdx.x & 63, w = threadIdx.x >> 6;
  if (threadIdx.x < CT_N / 8) ctr[blockIdx.x * (CT_N / 8) + threadIdx.x] = 0u;
  if (w >= 4) {
    const int item = blockIdx.x + 256 * (w - 4);
    if (item >= 576 || K0_NORETILE) return;
    const int h = item / 18, s = item - 18 * h, g = l >> 4, c = l & 15;
    typedef u32x4 __attribute__((aligned(4))) u32x4_a4;
    u32x4* st = stage[w - 4];
    u32x4 t[5];
#pragma unroll
    for (int i = 0; i < 5; ++i) {
      const int ci = 64 * i + l < 288 ? 64 * i + l : 0, row = ci / 9, c9 = ci - 9 * row, sec = c9 / 3, cc = c9 - 3 * sec;
      const int col = (sec == 0 ? 576 : (sec == 1 ? 0 : 1152)) + 18 * h + (cc == 2 ? 10 : 8 * cc);
      t[i] = *(const u32x4_a4*)(wqkv + (size_t)(32 * s + row) * (3 * D_) + col);
    }
    asm volatile("" : "+v"(t[0]), "+v"(t[1]), "+v"(t[2]), "+v"(t[3]), "+v"(t[4]));
#pragma unroll
    for (int i = 0; i < 5; ++i)
      if (64 * i + l < 288) st[64 * i + l] = t[i];
    __builtin_amdgcn_fence(__ATOMIC_RELEASE, "wavefront");
    __builtin_amdgcn_wave_barrier();
    const int sec[4] = {0, c < 4 ? 0 : 48, 96, c < 4 ? 96 : 48};
    const int dim[4] = {c, c < 4 ? 14 + c : c - 4, c, c < 4 ? 14 + c : (c < 8 ? c + 8 : 14 + (c & 3))};
    unsigned short v[4][8];
#pragma unroll
    for (int X = 0; X < 4; ++X) {
      const uint16_t* src = (const uint16_t*)((const char*)st + 8 * g * 144 + sec[X] + 2 * dim[X] + (dim[X] >= 16 ? 12 : 0));
#pragma unroll
      for (int e = 0; e < 8; ++e) v[X][e] = src[e * 72];
    }
#pragma unroll
    for (int X = 0; X < 4; ++X) {
      u32x4 o;
#pragma unroll
      for (int e = 0; e < 4; ++e) o[e] = (unsigned)v[X][2 * e] | ((unsigned)v[X][2 * e + 1] << 16);
      wq[((size_t)item * 4 + X) * 64 + l] = o;
    }
    return;
  }
  if (K0_NOLN) return;
  const int tok = blockIdx.x * 8 + w * 2 + (l >> 5), p = l & 31;
  const u32x4* row = (const u32x4*)(x + (size_t)tok * D_);
  u32x4 c[3];
  c[0] = row[p];
  c[1] = row[p + 32];
  c[2] = row[p < 8 ? p + 64 : p];
  const bool has2 = p < 8;
  float v[24];
#pragma unroll
  for (int i = 0; i < 3; ++i)
#pragma unroll
    for (int j = 0; j < 4; ++j) { v[i * 8 + 2 * j] = bf_lo(c[i][j]); v[i * 8 + 2 * j + 1] = bf_hi(c[i][j]); }
  float s = 0.f, s2 = 0.f;
#pragma unroll
  for (int j = 0; j < 16; ++j) s += v[j];
#pragma unroll
  for (int j = 16; j < 24; ++j) s2 += v[j];
  s += has2 ? s2 : 0.f;
  const float mean = reduce32(s) * (1.f / 576.f);
  float q = 0.f, q2 = 0.f;
#pragma unroll
  for (int j = 0; j < 24; ++j) v[j] -= mean;
#pragma unroll
  for (int j = 0; j < 16; ++j) q += v[j] * v[j];
#pragma unroll
  for (int j = 16; j < 24; ++j) q2 += v[j] * v[j];
  q += has2 ? q2 : 0.f;
  const float var = reduce32(q) * (1.f / 576.f);
  const float rstd = 1.f / sqrtf(var + 1e-5f);
  const int T = tok >> 4, t16 = tok & 15;
#pragma unroll
  for (int i = 0; i < 3; ++i) {
    u32x4 o;
#pragma unroll
    for (int j = 0; j < 4; ++j) o[j] = pack2(v[i * 8 + 2 * j] * rstd, v[i * 8 + 2 * j + 1] * rstd);
    const int ch = p + 32 * i;
    if (i < 2 || has2) xf[((size_t)T * 18 + (ch >> 2)) * 64 + (ch & 3) * 16 + t16] = o;
  }
}

constexpr unsigned L1_BUFA = 0, L1_BUFB = 73728, L1_SCR = 147456, L1_TOTAL = 163840;
constexpr int AUX_SC1 = 16;
#define SB() __builtin_amdgcn_sched_barrier(0)
#define PIN(x) asm volatile("" : "+v"(x))

struct KFP {
  const void* xf; const void* wq; const void* wg; const void* wu; const void* wd; const void* wo; const void* x;
  void* hf; void* kv; void* xb; void* ctr; void* out;
};

struct K1S {
  u32x4 xf[2][18];
  s16x4 wlo[6], whi[6];
  f32x4 acc[2][2];
  unsigned gel[4][2][2];
  u32x4 hq[2];
  float t0, t1;
  unsigned ard;
  u32x4 kp[8];
};
struct K1G { unsigned dst, soff, sstep, vo0, vo1, vok, wm0, wvo, wvo2; rsrc_t rs, rsk, wrs, wrs2; int w; };
template <int I, int ROLE = 0> DI void k1_piece(const K1G& G) {
  if (!ABL1_NOSTAGE) glds16<ROLE>(G.dst + I * 1024u, (I & 1) ? G.vo1 : G.vo0, G.rs, G.soff + I * G.sstep);
}
template <int I, int ROLE = 0> DI void k1_piece2(const K1G& G, int l) {
  const int ci = 64 * I + l, rho = ci / 18, c = ci - 18 * rho;
  glds16<ROLE>(G.dst + I * 1024u, rho * 1152u + c * 16u, G.rs, G.soff);
}
template <int STRIDE, int N, int H> DI void k1_read(K1S& S, unsigned a0, int slot_unused = 0) {
  constexpr int P = N / 18, KS = N % 18;
  if constexpr (H == 0 && KS == 0) { S.ard = STRIDE == 4096 ? (a0 ^ (P << 5)) : a0 + P * 36864u; PIN(S.ard); }
  if constexpr (H == 0 && KS == 9) { S.ard += 9 * STRIDE; PIN(S.ard); }
  if constexpr (H == 0) S.wlo[N % 6] = trread(S.ard + (KS % 9) * STRIDE);
  else S.whi[N % 6] = trread(S.ard + (KS % 9) * STRIDE + STRIDE / 8);
}
template <int KIND, int U, int A, int PR, int PH, int SL> DI void k1_epi(K1S& S) {
  constexpr int T = PR >> 1, R = 2 * (PR & 1);
  const float a = S.acc[A][T][R], b = S.acc[A][T][R + 1];
  if (ABL1_NOEPI) {
    if (PH == 3 && SL == 1) { if (KIND == 1) S.gel[U][T][PR & 1] = pack2(a, b); else S.hq[T][2 * (U & 1) + (PR & 1)] = pack2(a, b) ^ S.gel[U][T][PR & 1]; }
    return;
  }
  if constexpr (KIND == 1) {
    if constexpr (PH == 0 && SL == 0) { S.t0 = a * a; S.t1 = b * b; }
    if constexpr (PH == 0 && SL == 1) {
      S.t0 = __builtin_fmaf(S.t0, -0.10294323970f, -2.30220819814f);
      S.t1 = __builtin_fmaf(S.t1, -0.10294323970f, -2.30220819814f);
    }
    if constexpr (PH == 1 && SL == 0) { S.t0 *= a; S.t1 *= b; }
    if constexpr (PH == 1 && SL == 1) { S.t0 = fexp2(S.t0); S.t1 = fexp2(S.t1); }
    if constexpr (PH == 2 && SL == 0) { S.t0 += 1.f; S.t1 += 1.f; }
    if constexpr (PH == 2 && SL == 1) { S.t0 = frcp(S.t0); S.t1 = frcp(S.t1); }
    if constexpr (PH == 3 && SL == 0) { S.t0 *= a; S.t1 *= b; }
    if constexpr (PH == 3 && SL == 1) { unsigned v = pack2(S.t0, S.t1); PIN(v); S.gel[U][T][PR & 1] = v; }
    else { PIN(S.t0); PIN(S.t1); }
  } else {
    if constexpr (PH == 1 && SL == 0) { const unsigned u = S.gel[U][T][PR & 1]; S.t0 = bf_lo(u); S.t1 = bf_hi(u); PIN(S.t0); PIN(S.t1); }
    if constexpr (PH == 2 && SL == 0) { S.t0 *= a; S.t1 *= b; PIN(S.t0); PIN(S.t1); }
    if constexpr (PH == 3 && SL == 0) { unsigned v = pack2(S.t0, S.t1); PIN(v); S.hq[T][2 * (U & 1) + (PR & 1)] = v; }
  }
}
template <int KIND, int U, int A, int I = 0> DI void k1_epi_all(K1S& S) {
  if constexpr (I < 32) { k1_epi<KIND, U, A, I / 8, (I / 2) % 4, I % 2>(S); k1_epi_all<KIND, U, A, I + 1>(S); }
}
DI void k1_store_pair(K1S& S, rsrc_t rsH, unsigned voff, unsigned soff) {
  bstore16_sc1<0>(S.hq[0], voff, rsH, soff);
  bstore16_sc1<1024>(S.hq[1], voff, rsH, soff);
}
template <int KIND, int PREV, int NP, int STRIDE, int NSTRIDE, int STG, int SIG, int N>
DI void k1_step(K1S& S, unsigned ab, unsigned abn, const K1G& G, rsrc_t rsH, unsigned hvoff, unsigned hso, unsigned hsop,
                gu32* sig, gu32* sig2) {
  constexpr int P = N / 18, KS = N % 18, A = P & 1, NT = NP * 18;
  constexpr int EK = P == 0 ? (PREV == 3 ? 2 : PREV) : (NP == 2 ? 1 : KIND);
  constexpr int EU = P == 0 ? (PREV == 3 ? 0 : 3) : (NP == 2 ? 0 : P - 1);
  constexpr int EA = P == 0 ? 1 : (A ^ 1);
  constexpr bool EPI = EK != 0 && KS >= 1 && KS <= 16;
  const f32x4 z4 = {0.f, 0.f, 0.f, 0.f};
  const s16x8 wf = __builtin_shufflevector(S.wlo[N % 6], S.whi[N % 6], 0, 1, 2, 3, 4, 5, 6, 7);
  const s16x8 x0 = __builtin_bit_cast(s16x8, S.xf[0][KS]), x1 = __builtin_bit_cast(s16x8, S.xf[1][KS]);
  if (!ABL1_NOCOMP) S.acc[A][0] = mfma16(wf, x0, KS == 0 ? z4 : S.acc[A][0]);
  else if (KS == 0) { S.acc[A][0] = z4; S.acc[A][0][0] = (float)wf[0]; }
  SB();
  if constexpr (N + 5 < NT) k1_read<STRIDE, N + 5, 0>(S, ab);
  else if constexpr (NSTRIDE != 0) k1_read<NSTRIDE == 0 ? 4096 : NSTRIDE, N + 5 - NT, 0>(S, abn);
  if constexpr (EPI) k1_epi<EK, EU, EA, (KS - 1) / 4, (KS - 1) % 4, 0>(S);
  SB();
  if (!ABL1_NOCOMP) S.acc[A][1] = mfma16(wf, x1, KS == 0 ? z4 : S.acc[A][1]);
  else if (KS == 0) { S.acc[A][1] = z4; }
  SB();
  if constexpr (N + 5 < NT) k1_read<STRIDE, N + 5, 1>(S, ab);
  else if constexpr (NSTRIDE != 0) k1_read<NSTRIDE == 0 ? 4096 : NSTRIDE, N + 5 - NT, 1>(S, abn);
  if constexpr (EPI) k1_epi<EK, EU, EA, (KS - 1) / 4, (KS - 1) % 4, 1>(S);
  constexpr int PS = WARM_MODE == 2 ? 3 : 6;
  if constexpr (STG == 1 && NP == 4 && N % PS == 2 && N / PS < 9) k1_piece<N / PS>(G);
  if constexpr (STG == 1 && NP == 2 && N % 2 == 0 && N >= 2 && N / 2 <= 9) k1_piece<N / 2 - 1>(G);
  if constexpr (WARM_MODE == 2 && STG == 1 && N == (NP == 4 ? 45 : 22)) warm_lines(G.wm0, G.wvo, G.wrs);
  if constexpr (WARM_MODE == 1 && SIG == 1 && N == NT - 7) warm_lines(G.wm0, G.wvo, G.wrs);
  if constexpr (WARM_MODE == 1 && SIG == 1 && N == NT - 5) warm_lines(G.wm0, G.wvo2, G.wrs2);
  if constexpr (STG == 2 && N % 6 == 2 && N / 6 < 9) k1_piece2<N / 6>(G, hvoff >> 4);
  if constexpr (STG == 2 && N % 6 == 5 && N / 6 < 8) bload16_sc1<0>(S.kp[N / 6], G.vok, G.rsk, (N / 6) * 1408u);
  if constexpr (PREV == 2 && N == 17) k1_store_pair(S, rsH, hvoff, hsop);
  if constexpr (PREV == 3 && N == 17) {
#pragma unroll
    for (int T = 0; T < 2; ++T) {
      const u32x2 v = {S.hq[T][0], S.hq[T][1]};
      __builtin_amdgcn_raw_buffer_store_b64(v, rsH, hvoff + T * 1024u, hsop, AUX_SC1);
    }
  }
  if constexpr (KIND == 2 && NP == 4 && N == 2 * 18 + 17) k1_store_pair(S, rsH, hvoff, hso);
  if constexpr (N == 1) asm volatile("s_barrier" ::: "memory");
  if constexpr (SIG == 2 && N == NT - 8) { if (rfl(threadIdx.x >> 6) == 0) ctr_wait(sig, 32u); }
  if constexpr (NSTRIDE != 0 && N == NT - 8) wait_bar_nolgkm<(KIND == 2 ? 2 : 0) + (WARM_MODE == 2 ? 1 : 0)>();
  if constexpr (SIG == 1 && N == NT - 8) { if (threadIdx.x == 0) ctr_add(sig); }
  if constexpr (SIG == 2 && N == NT - 8) { if (threadIdx.x == 0) ctr_add(sig2); }
  SB();
  if constexpr (N + 1 < NT) k1_step<KIND, PREV, NP, STRIDE, NSTRIDE, STG, SIG, N + 1>(S, ab, abn, G, rsH, hvoff, hso, hsop, sig, sig2);
}
template <int STRIDE> DI void k1_first_reads(K1S& S, unsigned ab) {
  k1_read<STRIDE, 0, 0>(S, ab); k1_read<STRIDE, 0, 1>(S, ab); k1_read<STRIDE, 1, 0>(S, ab); k1_read<STRIDE, 1, 1>(S, ab);
  k1_read<STRIDE, 2, 0>(S, ab); k1_read<STRIDE, 2, 1>(S, ab); k1_read<STRIDE, 3, 0>(S, ab); k1_read<STRIDE, 3, 1>(S, ab);
  k1_read<STRIDE, 4, 0>(S, ab); k1_read<STRIDE, 4, 1>(S, ab);
  SB();
}
template <int KS> DI void k1_qkv_step(K1S& S, s16x8 (&wq)[2][4], f32x4 (&Q)[4][2], unsigned qa, unsigned xv, rsrc_t rsX,
                                      unsigned xsb, const K1G& G) {
  if constexpr (KS + 6 < 18) {
    bload16<((KS + 6) & 3) * 1024>(S.xf[0][KS + 6], xv, rsX, xsb + ((KS + 6) >> 2) * 4096u);
    bload16<((KS + 6) & 3) * 1024>(S.xf[1][KS + 6], xv, rsX, xsb + 18432u + ((KS + 6) >> 2) * 4096u);
  } else if constexpr (KS < 15) { k1_piece<2 * (KS - 12)>(G); k1_piece<2 * (KS - 12) + 1>(G); }
  else k1_piece<KS - 9>(G);
  constexpr int YOUNGER = (ABL1_NOSTAGE ? (KS < 12 ? 12 : 2 * (17 - KS)) : (KS < 15 ? 12 : 26 - KS)) + (KS < 6 && !NO_WARM0 ? 2 : 0);
  wait_vm2<YOUNGER>(S.xf[0][KS], S.xf[1][KS]);
  const s16x8 x0 = __builtin_bit_cast(s16x8, S.xf[0][KS]), x1 = __builtin_bit_cast(s16x8, S.xf[1][KS]);
  SB();
#pragma unroll
  for (int X = 0; X < 4; ++X) {
    Q[X][0] = mfma16(x0, wq[KS & 1][X], Q[X][0]);
    SB();
    if constexpr (KS + 1 < 18) wq[(KS + 1) & 1][X] = *LDSP(s16x8, qa + ((KS + 1) * 4 + X) * 1024u);
    SB();
    Q[X][1] = mfma16(x1, wq[KS & 1][X], Q[X][1]);
    SB();
  }
  if constexpr (KS + 1 < 18) k1_qkv_step<KS + 1>(S, wq, Q, qa, xv, rsX, xsb, G);
}

constexpr int K2D = 5;
constexpr unsigned L2_SLOT = 9216, L2_NS = K2D + 2;
constexpr unsigned L2_WO = L1_BUFA + 4 * L2_SLOT;
constexpr unsigned L2_MT = L1_BUFB + 3 * L2_SLOT, L2_KV = L1_SCR;
constexpr unsigned L2_OWN = L1_BUFB, L2_OUT = L1_BUFA;

struct K2S {
  u32x4 hr[K2D + 1][2];
  u32x4 ha[3][2];
  s16x4 wlo[6], whi[6];
  f32x4 acc[9][2];
  unsigned flag, ardm;
  unsigned ard;
};
struct K2C {
  rsrc_t rsH, rsW, rsB;
  unsigned vo0, vo1, hvoff, hsoF, hsoA, wsoF, xso, lb, lanec;
  int w, sp;
  gu32* chf;
  gu32* cxa;
};
constexpr bool k2_att(int n) { return n >= 18; }
constexpr unsigned k2_loc(int n) {
  return n < 8 ? 4 * (n >> 1) + (n & 1) : n < 10 ? 8 + n : 4 * ((n - 10) >> 1) + 2 + (n & 1);
}
constexpr unsigned k2_slot(int s) { return s < 4 ? L1_BUFA + s * L2_SLOT : L1_BUFB + (s - 4) * L2_SLOT; }
constexpr int k2_tW(int m) { return m == 4 ? -30 : 16 * (m - 5); }
constexpr int k2_tH(int m, int half) {
  return m <= 1 ? -40 + 2 * m + half : m <= 4 ? -24 + 2 * m + half : 16 * (m - 5) + 5 + 2 * half;
}
constexpr int k2_tA(int i) { return 16 * (6 + i) + 8; }
constexpr int k2_younger(int t0, int n) {
  int cnt = 0;
  const int tw = 16 * n + 3;
  for (int m = 4; m <= 17; ++m) if (k2_tW(m) > t0 && k2_tW(m) < tw) ++cnt;
  for (int m = 0; m <= 17; ++m)
    for (int h = 0; h < 2; ++h) if (k2_tH(m, h) > t0 && k2_tH(m, h) < tw) ++cnt;
  for (int i = 0; i < 6; ++i) if (k2_tA(i) > t0 && k2_tA(i) < tw) ++cnt;
  for (int i = 0; i < 4; ++i) if (-29 + i > t0) ++cnt;
  return cnt;
}
constexpr int k2_count(int n) {
  const int m = n + 1, th = k2_tH(m, 1), tw = m >= 4 ? k2_tW(m) : -100;
  return k2_younger(th > tw ? th : tw, n);
}
static_assert(k2_count(0) == 12 && k2_count(1) == 8 && k2_count(3) == 10 && k2_count(8) == 12 && k2_count(12) == 14 && k2_count(13) == 12, "schedule");
static_assert(k2_younger(16 * 3 + 4, 5) == 6, "schedule");
template <int T, int I, int ROLE = 0> DI void k2_issue(K2S& S, const K2C& C) {
  if constexpr (T <= 17) {
    constexpr unsigned ks = k2_loc(T);
    const unsigned wso = ABL2_NOW ? 0xf0000000u : C.wsoF + ks * 36864u, hso = ABL2_NOH ? 0xf0000000u : C.hsoF + ks * 131072u;
    if constexpr (I == 0) glds16<ROLE>(C.lb + k2_slot(T % L2_NS) + C.w * 1024u, C.vo0, C.rsW, wso);
    if constexpr (I == 1) { if ((T & 7) == C.w) glds16<3>(C.lb + k2_slot(T % L2_NS) + 8192u, C.vo1, C.rsW, wso); }
    if constexpr (I == 2) bload16_sc1<0, ROLE>(S.hr[T % (K2D + 1)][0], C.hvoff, C.rsH, hso);
    if constexpr (I == 3) bload16_sc1<1024, ROLE>(S.hr[T % (K2D + 1)][1], C.hvoff, C.rsH, hso);
  }
}
constexpr int K2_SEQ = 135;
constexpr int k2_T(int M) { return M < K2_SEQ ? M / 9 : 15 + (M - K2_SEQ) % 6; }
constexpr int k2_c(int M) { return M < K2_SEQ ? M % 9 : (M - K2_SEQ) / 6; }
template <int M, int H> DI void k2_read(K2S& S, const K2C& C) {
  if constexpr (M < 189) {
    constexpr int T = k2_T(M), c = k2_c(M);
    if constexpr (M < K2_SEQ) {
      if constexpr (H == 0 && c == 0) { S.ard = C.lanec + k2_slot(T % L2_NS); PIN(S.ard); }
      if constexpr (H == 0) S.wlo[M % 6] = trread(S.ard + c * 32);
      else S.whi[M % 6] = trread(S.ard + c * 32 + 4608);
    } else {
      static_assert(k2_slot(16 % L2_NS) == k2_slot(15 % L2_NS) + L2_SLOT && k2_slot(17 % L2_NS) == k2_slot(15 % L2_NS) + 2 * L2_SLOT, "ring");
      if constexpr (H == 0 && M == K2_SEQ) { S.ard = C.lanec + k2_slot(15 % L2_NS); PIN(S.ard); S.ardm = C.lanec + L2_MT; PIN(S.ardm); }
      const unsigned a = T <= 17 ? S.ard + (T - 15) * L2_SLOT + c * 32 : S.ardm + (T - 18) * L2_SLOT + c * 32;
      if constexpr (H == 0) S.wlo[M % 6] = trread(a);
      else S.whi[M % 6] = trread(a + 4608);
    }
  }
}
template <int c> DI void k2_send(K2S& S, const K2C& C) {
  if (C.w != C.sp) {
    const u32x4 v = {pack2(S.acc[c][0][0], S.acc[c][0][1]), pack2(S.acc[c][0][2], S.acc[c][0][3]),
                     pack2(S.acc[c][1][0], S.acc[c][1][1]), pack2(S.acc[c][1][2], S.acc[c][1][3])};
    bstore16_sc1<0>(v, C.hvoff, C.rsB, C.xso + c * 1024u);
  } else {
    *LDSP(f32x4, C.lb + L2_OWN + (c * 2) * 1024u + C.hvoff) = S.acc[c][0];
    *LDSP(f32x4, C.lb + L2_OWN + (c * 2 + 1) * 1024u + C.hvoff) = S.acc[c][1];
  }
}
template <int T, int N> DI void k2_wait(K2S& S) {
  asm volatile("s_waitcnt vmcnt(%2)\n\ts_barrier" : "+v"(S.hr[T % (K2D + 1)][0]), "+v"(S.hr[T % (K2D + 1)][1]) : "i"(N) : "memory");
}
template <int M> DI void k2_step(K2S& S, const K2C& C) {
  constexpr int T = k2_T(M), c = k2_c(M);
  constexpr bool seq = M < K2_SEQ;
  const s16x8 wf = __builtin_shufflevector(S.wlo[M % 6], S.whi[M % 6], 0, 1, 2, 3, 4, 5, 6, 7);
  const s16x8 h0 = __builtin_bit_cast(s16x8, k2_att(T) ? S.ha[k2_att(T) ? T - 18 : 0][0] : S.hr[T % (K2D + 1)][0]);
  const s16x8 h1 = __builtin_bit_cast(s16x8, k2_att(T) ? S.ha[k2_att(T) ? T - 18 : 0][1] : S.hr[T % (K2D + 1)][1]);
  const f32x4 z4 = {0.f, 0.f, 0.f, 0.f};
  S.acc[c][0] = mfma16(wf, h0, T == 0 ? z4 : S.acc[c][0]);
  SB();
  k2_read<M + 5, 0>(S, C);
  SB();
  S.acc[c][1] = mfma16(wf, h1, T == 0 ? z4 : S.acc[c][1]);
  SB();
  k2_read<M + 5, 1>(S, C);
  if constexpr (seq) {
    if constexpr (T + K2D <= 17) {
      if constexpr (c == 0) { if (!STAGGER || C.w < 4) k2_issue<T + K2D, 0, STAGGER>(S, C); }
      if constexpr (STAGGER && c == 1) { if (C.w >= 4) k2_issue<T + K2D, 0, 2>(S, C); }
      if constexpr (c == 2) k2_issue<T + K2D, 1>(S, C);
      if constexpr (c == (STAGGER ? 4 : 5)) { if (!STAGGER || C.w < 4) k2_issue<T + K2D, 2, STAGGER>(S, C); }
      if constexpr (STAGGER && c == 5) { if (C.w >= 4) k2_issue<T + K2D, 2, 2>(S, C); }
      if constexpr (c == (STAGGER ? 6 : 7)) { if (!STAGGER || C.w < 4) k2_issue<T + K2D, 3, STAGGER>(S, C); }
      if constexpr (STAGGER && c == 7) { if (C.w >= 4) k2_issue<T + K2D, 3, 2>(S, C); }
    }
    if constexpr (T >= 6 && T <= 11 && c == 8)
      bload16_sc1<((T - 6) & 1) * 1024>(S.ha[(T - 6) >> 1][(T - 6) & 1], C.hvoff, C.rsH, C.hsoA + ((T - 6) >> 1) * 131072u);
    if constexpr (T == 3 && c == 4) {
      if (C.w == 0) asm volatile("global_load_dword %0, %1, off sc1 ; role 3" : "=v"(S.flag) : "v"(C.chf));
    }
    if constexpr (T == 5 && c == 3) {
      if (C.w == 0) {
        asm volatile("s_waitcnt vmcnt(%1)" : "+v"(S.flag) : "i"(k2_younger(16 * 3 + 4, 5) - (STAGGER ? 1 : 0)));
        if (rfl(S.flag) < 4u) ctr_wait(C.chf, 4u);
      }
    }
    if constexpr (c == 3 && T < 14) k2_wait<T + 1, k2_count(T)>(S);
    if constexpr (c == 3 && T == 1) { if (threadIdx.x == 0) ctr_add(C.chf); }
    if constexpr (c == 2 && T == 14)
      asm volatile("s_waitcnt vmcnt(0)"
                   : "+v"(S.ha[0][0]), "+v"(S.ha[0][1]), "+v"(S.ha[1][0]), "+v"(S.ha[1][1]), "+v"(S.ha[2][0]), "+v"(S.ha[2][1]),
                     "+v"(S.hr[15 % (K2D + 1)][0]), "+v"(S.hr[15 % (K2D + 1)][1]), "+v"(S.hr[16 % (K2D + 1)][0]), "+v"(S.hr[16 % (K2D + 1)][1]),
                     "+v"(S.hr[17 % (K2D + 1)][0]), "+v"(S.hr[17 % (K2D + 1)][1]));
    if constexpr (c == 3 && T == 14) asm volatile("s_barrier" ::: "memory");
  } else {
    if constexpr (T == 20 && c > 0) k2_send<c - 1>(S, C);
    if constexpr (T == 15 && c == 7) {
      asm volatile("s_waitcnt vmcnt(1)\n\ts_barrier" ::: "memory");
      if (C.w == 0 && (C.hvoff >> 4) < 8) ctr_add(C.cxa + (C.hvoff >> 4) * 32);
    }
  }
  SB();
  if constexpr (seq && c == 8 && T % 5 == 4) { STAMP(S.dg, 18 + T / 5) }
  if constexpr (M + 1 < 189) k2_step<M + 1>(S, C);
}

__global__ __launch_bounds__(512, 2) void kf_main(KFP p) {
  __shared__ __attribute__((aligned(128))) char smem[L1_TOTAL];
  const unsigned lb = (unsigned)(size_t)(__attribute__((address_space(3))) char*)smem;
  const int tid = threadIdx.x, l = tid & 63;
  const int w = rfl(tid >> 6);
  const int b = blockIdx.x, xc = b & 7, r = b >> 3;
  const int cr = r & 3, j = 4 * xc + cr, ib = r >> 2;
  const int g = l >> 4, q = (l & 15) >> 2, pp = l & 3, c16 = l & 15;

  STAMPR(p.dg, 30) STAMP(p.dg, 0)
  const rsrc_t rsX = mk_rsrc(p.xf, (unsigned)SZ_XF);
  const rsrc_t rsQ = mk_rsrc(p.wq, (unsigned)SZ_WQ);
  const rsrc_t rsG = mk_rsrc(p.wg, D_ * F_ * 2);
  const rsrc_t rsU = mk_rsrc(p.wu, D_ * F_ * 2);
  const rsrc_t rsH = mk_rsrc(p.hf, (unsigned)SZ_HF);
  const rsrc_t rsB = mk_rsrc(p.xb, (unsigned)SZ_XB);
  const unsigned xso = (((ib * 4 + cr) * 8 + w) * 8 + xc) * 9216u;
  gu32* const ctr = (gu32*)p.ctr;
  const unsigned long long t_start = __builtin_amdgcn_s_memrealtime();
  K1S S;

  if (!NO_CODEWARM) {
    unsigned long long pc;
    unsigned len;
    asm volatile("s_getpc_b64 %0\n98764:\n\ts_mov_b32 %1, 98765f-98764b" : "=s"(pc), "=s"(len));
    warm_lines(lb + L1_SCR + 15872u, l < 2 ? ((r * 8 + w) * 2 + l) * 128u : 0xf0000000u, mk_rsrc((const void*)pc, len));
  }

#pragma unroll
  for (int i = 0; i < 9; ++i) glds16(lb + L1_BUFA + (9 * w + i) * 1024u, l * 16u, rsQ, (j * 72u + 9 * w + i) * 1024u);
  const int T0 = 16 * ib + 2 * w;
  const unsigned hvoff = l * 16u, xsb = T0 * 18432u;
#define XL(s) bload16<((s) & 3) * 1024>(S.xf[0][s], hvoff, rsX, xsb + ((s) >> 2) * 4096u); \
              bload16<((s) & 3) * 1024>(S.xf[1][s], hvoff, rsX, xsb + 18432u + ((s) >> 2) * 4096u);
  XL(0) XL(1) XL(2) XL(3) XL(4) XL(5)
#undef XL
  const unsigned wcol = (576u * xc + 128u * cr) * 2u;
  if (!NO_WARM0) {
    const unsigned si = 3 * w + (l - 9);
    const unsigned vo = l < 9 ? (72u * ib + 9u * w + l) * 9216u + wcol
                      : (l < 12 && si < 18 ? (18u * r + si) * 9216u + (576u * xc + 512u) * 2u : 0xf0000000u);
    warm_lines(lb + L1_SCR + 15872u, vo, rsG);
    warm_lines(lb + L1_SCR + 15872u, vo, rsU);
  }
  K1G G;
  G.w = w;
  {
    const unsigned rowv = (l >> 3) * 9216u, c8 = l & 7, s0 = (l >> 4) & 1;
    const unsigned vA = rowv + ((c8 ^ (s0 << 1)) << 4), vB = rowv + ((c8 ^ ((s0 | 2) << 1)) << 4);
    G.vo0 = (w & 1) ? vB : vA;
    G.vo1 = (w & 1) ? vA : vB;
  }
  G.sstep = 8u * 9216u;
  const unsigned wsoff = 72u * w * 9216u, wdst = lb + 72u * w * 128u;
  G.rs = rsG; G.soff = wsoff + wcol; G.dst = wdst + L1_BUFB;

  STAMP(p.dg, 1)
  wait_bar<NO_WARM0 ? 12 : 14>();
  STAMP(p.dg, 2)

  {
    const unsigned qa = lb + L1_BUFA + l * 16u;
    const f32x4 z4 = {0.f, 0.f, 0.f, 0.f};
    f32x4 Q[4][2];
#pragma unroll
    for (int X = 0; X < 4; ++X) Q[X][0] = Q[X][1] = z4;
    s16x8 wq[2][4];
#pragma unroll
    for (int X = 0; X < 4; ++X) wq[0][X] = *LDSP(s16x8, qa + X * 1024u);
    k1_qkv_step<0>(S, wq, Q, qa, hvoff, rsX, xsb, G);
    STAMP(p.dg, 3)
    unsigned fKA[4], fKB[4], fVA[4], fVB[4], fQ2[4];
#pragma unroll
    for (int T = 0; T < 2; ++T)
#pragma unroll
      for (int h2 = 0; h2 < 2; ++h2) {
        fKA[2 * T + h2] = pack2(phi_f(Q[0][T][2 * h2]), phi_f(Q[0][T][2 * h2 + 1]));
        fKB[2 * T + h2] = pack2(phi_f(Q[1][T][2 * h2]), phi_f(Q[1][T][2 * h2 + 1]));
        fVA[2 * T + h2] = pack2(Q[2][T][2 * h2], Q[2][T][2 * h2 + 1]);
        const unsigned raw = pack2(Q[3][T][2 * h2], Q[3][T][2 * h2 + 1]);
        fVB[2 * T + h2] = c16 < 2 ? 0x3f803f80u : raw;
        fQ2[2 * T + h2] = pack2(phi_f(Q[3][T][2 * h2]), phi_f(Q[3][T][2 * h2 + 1]));
      }
    const s16x8 aKA = mk_frag(fKA[0], fKA[1], fKA[2], fKA[3]), aKB = mk_frag(fKB[0], fKB[1], fKB[2], fKB[3]);
    const s16x8 bVA = mk_frag(fVA[0], fVA[1], fVA[2], fVA[3]), bVB = mk_frag(fVB[0], fVB[1], fVB[2], fVB[3]);
    const s16x8 aQ2 = mk_frag(fQ2[0], fQ2[1], fQ2[2], fQ2[3]);
    const f32x4 R0 = mfma16(aKA, bVA, z4), R1 = mfma16(aKA, bVB, z4);
    const f32x4 R2 = mfma16(aKB, bVA, z4), R3 = mfma16(aKB, bVB, z4);
    {
      const unsigned one = (c16 >> 2) == g ? (0x3f80u << (16 * (c16 & 1))) : 0u;
      const unsigned i0 = (c16 & 2) ? 0u : one, i1 = (c16 & 2) ? one : 0u;
#pragma unroll
      for (int T = 0; T < 2; ++T) {
        const s16x8 id = T == 0 ? mk_frag(i0, i1, 0u, 0u) : mk_frag(0u, 0u, i0, i1);
        const f32x4 qa4 = mfma16(aKB, id, z4), qb4 = mfma16(aQ2, id, z4);
        const unsigned b01 = pack2(qb4[0], qb4[1]), b23 = pack2(qb4[2], qb4[3]);
        bstore16_sc1<0>(u32x4{pack2(qa4[0], qa4[1]), pack2(qa4[2], qa4[3]), g == 1 ? b01 : (g == 2 ? b23 : 0u), g == 1 ? b23 : 0u},
                        hvoff + T * 1024u, rsB, xso);
      }
    }
    {
      const unsigned sb = lb + L1_SCR + w * 1408u;
      const int eB = c16 == 0 ? 18 : c16 + 14;
      const bool okB = c16 == 0 || c16 == 2 || c16 == 3;
      const unsigned dummy = lb + L1_SCR + 8 * 1408u + l * 4u;
#pragma unroll
      for (int rr = 0; rr < 4; ++rr) {
        const int d = 4 * g + rr;
        *LDSP(float, sb + (d * 19 + c16) * 4) = R0[rr];
        *LDSP(float, okB ? sb + (d * 19 + eB) * 4 : dummy) = R1[rr];
        if (rr >= 2) {
          *LDSP(float, g == 0 ? sb + ((14 + rr) * 19 + c16) * 4 : dummy) = R2[rr];
          *LDSP(float, g == 0 && okB ? sb + ((14 + rr) * 19 + eB) * 4 : dummy) = R3[rr];
        }
      }
    }
  }
  STAMP(p.dg, 4)
  wait_bar<2>();
  STAMP(p.dg, 5)

  const unsigned lane0 = lb + (8 * g + q) * 128u + 8u * pp + ((((q >> 1) & 1) | ((g & 1) << 1)) << 5);
  const unsigned abA = lane0 + L1_BUFA, abB = lane0 + L1_BUFB;
  const unsigned ks0 = 18u * xc + 4u * cr;
  const unsigned hs0 = (ks0 * 128u + T0) * 1024u;

  k1_first_reads<4096>(S, abB);
  const rsrc_t rsKV = mk_rsrc(p.kv, (unsigned)SZ_KV);
  if (tid < 88) {
    f32x4 a = *LDSP(f32x4, lb + L1_SCR + tid * 16);
#pragma unroll
    for (int ww = 1; ww < 8; ++ww) a += *LDSP(f32x4, lb + L1_SCR + ww * 1408u + tid * 16);
    bstore16_sc1<0>(__builtin_bit_cast(u32x4, a), tid * 16u, rsKV, (j * 8 + ib) * 1408u);
  }
  SB();
  const rsrc_t rsW = mk_rsrc(p.wd, F_ * D_ * 2), rsO = mk_rsrc(p.wo, D_ * D_ * 2);
  constexpr unsigned OOB = 0xf0000000u;
  const unsigned wdl = (576u * xc * 9u + r * 162u) * 128u;
  const unsigned win1 = (72u * ib + 18u * (w & 3) + l) * 9216u + wcol + 128u;
  G.wm0 = lb + L1_SCR + 15872u;
  if (WARM_MODE == 1) {
    G.wvo = l < 9 ? (72u * ib + 9u * w + l) * 9216u + wcol + 128u : OOB;
    G.wrs = rsG;
    G.wvo2 = w < 3 ? (l < 54 ? wdl + (w * 54u + l) * 128u : OOB)
           : w == 3 ? (l < 21 && r * 21 + l < 648 ? (72u * xc * 9u + r * 21u + l) * 128u : OOB) : (l < 18 ? win1 : OOB);
    G.wrs2 = w < 3 ? rsW : w == 3 ? rsO : rsU;
  } else {
    const unsigned sl = (18u * r + 5u * (w & 3) + l) * 9216u + (576u * xc + 512u) * 2u, su = (18u * r + 9u * (w & 1) + l) * 9216u + (576u * xc + 512u) * 2u;
    G.wvo = w < 4 ? (l < 5 && 5 * w + l < 18 ? sl : OOB) : w < 6 ? (l < 9 ? su : OOB) : (l < 27 ? wdl + (27u * (w - 6) + l) * 128u : OOB);
    G.wrs = w < 4 ? rsG : w < 6 ? rsU : rsW;
  }
  gu32* const ckv = ctr + (CT_KV + xc) * 32;
  const unsigned gvo0 = G.vo0, gvo1 = G.vo1;
  G.rs = rsU; G.soff = wsoff + wcol; G.dst = wdst + L1_BUFA;
  gu32* const chA = ctr + (CT_H + xc * 8 + ib) * 32;
  k1_step<1, 0, 4, 4096, 4096, 1, 1, 0>(S, abB, abA, G, rsH, hvoff, 0, 0, ckv, chA);
  STAMP(p.dg, 6)
  G.rs = (w >> 2) ? rsU : rsG;
  G.vo0 = G.vo1 = (l >> 2) * 9216u + (((l & 3) ^ ((l >> 5) << 1)) * 16u);
  G.sstep = 16u * 9216u;
  G.soff = 144u * (w & 3) * 9216u + (576u * xc + 512u + 32u * (cr >> 1)) * 2u;
  G.dst = lb + L1_BUFB + (w >> 2) * 36864u + 144u * (w & 3) * 64u;
  const unsigned abS = lb + L1_BUFB + (8 * g + q) * 64u + 8u * pp + (((cr & 1) ^ (g & 1)) << 5);
  if (WARM_MODE == 2) {
    G.wvo = w < 4 ? (l < 18 ? win1 : OOB) : (l < 14 && 14 * (w - 4) + l < 54 ? wdl + (54u + 14u * (w - 4) + l) * 128u : OOB);
    G.wrs = w < 4 ? rsG : rsW;
  }
  k1_step<2, 1, 4, 4096, 2048, 1, 0, 0>(S, abA, abS, G, rsH, hvoff, hs0, 0, ckv, chA);
  STAMP(p.dg, 8)
  G.rs = rsG; G.vo0 = gvo0; G.vo1 = gvo1; G.sstep = 8u * 9216u; G.soff = wsoff + wcol + 128u; G.dst = wdst + L1_BUFA;
  if (WARM_MODE == 2) { G.wvo = w < 4 && l < 18 ? win1 : OOB; G.wrs = rsU; }
  k1_step<0, 2, 2, 2048, 4096, 1, 0, 0>(S, abS, abA, G, rsH, hvoff, 0, hs0 + 131072u, ckv, chA);
  STAMP(p.dg, 10)
  G.rs = rsU; G.soff = wsoff + wcol + 128u; G.dst = wdst + L1_BUFB;
  if (WARM_MODE == 2) {
    G.wvo = w < 2 ? (l < 27 ? wdl + (108u + 27u * w + l) * 128u : OOB)
                  : (w == 2 && l < 21 && r * 21 + l < 648 ? (72u * xc * 9u + r * 21u + l) * 128u : OOB);
    G.wrs = w < 2 ? rsW : rsO;
  }
  k1_step<1, 3, 4, 4096, 4096, 1, 2, 0>(S, abA, abB, G, rsH, hvoff, 0,
                                        ((18u * xc + 16u + (cr >> 1)) * 128u + T0) * 1024u + (cr & 1) * 8u, ckv, chA);
  STAMP(p.dg, 12)
  const unsigned wsoF = 18u * xc * 36864u + cr * 288u;
  G.rs = w < 4 ? rsW : rsO;
  G.soff = w < 4 ? wsoF + (4 * (w >> 1) + (w & 1)) * 36864u : 18u * (4 * xc + w - 4) * 1152u + cr * 288u;
  G.dst = lb + L1_BUFA + w * L2_SLOT;
  G.rsk = rsKV;
  { const unsigned hh = tid / 88, q4 = tid - 88 * hh; G.vok = tid < 352 ? ((4 * xc + hh) * 8 * 352 + q4 * 4) * 4u : 0xf0000000u; }
  k1_step<2, 1, 4, 4096, 0, 2, 0, 0>(S, abB, 0, G, rsH, hvoff, hs0 + 2 * 131072u, 0, ckv, chA);
  u32x4 pqv[2];
  {
    bload16_sc1<0>(pqv[0], hvoff, rsB, xso);
    bload16_sc1<1024>(pqv[1], hvoff, rsB, xso);
  }
  asm volatile("s_waitcnt vmcnt(4)" : "+v"(S.kp[0]), "+v"(S.kp[1]), "+v"(S.kp[2]), "+v"(S.kp[3]), "+v"(S.kp[4]), "+v"(S.kp[5]), "+v"(S.kp[6]), "+v"(S.kp[7]));
  {
    f32x4 a = __builtin_bit_cast(f32x4, S.kp[0]);
#pragma unroll
    for (int i = 1; i < 8; ++i) a += __builtin_bit_cast(f32x4, S.kp[i]);
    if (tid < 352) *LDSP(f32x4, lb + L2_KV + tid * 16u) = a;
  }
  if (w == 0) ctr_wait(chA, 4u);
  k1_epi_all<2, 3, 1>(S);
  k1_store_pair(S, rsH, hvoff, hs0 + 3 * 131072u);
  STAMP(p.dg, 14) STAMPR(p.dg, 27)
  asm volatile("s_waitcnt lgkmcnt(0)\n\ts_barrier" ::: "memory");
  STAMP(p.dg, 15)

  K2S S2;
  K2C C;
  C.rsH = rsH; C.rsW = rsW; C.w = w; C.sp = xc; C.lb = lb;
  C.rsB = rsB;
  C.xso = xso;
  C.cxa = ctr + (CT_X + (ib * 4 + cr) * 8) * 32;
  { const int ci = 64 * w + l, rho = ci / 18, c = ci - 18 * rho; C.vo0 = rho * 1152u + c * 16u; }
  { const int ci = 512 + l, rho = ci / 18, c = ci - 18 * rho; C.vo1 = rho * 1152u + c * 16u; }
  C.hvoff = hvoff;
  const unsigned hT = T0 * 1024u;
  C.hsoF = hT + 18u * xc * 131072u;
  C.wsoF = wsoF;
  C.lanec = lb + (4 * g + q) * 288u + 8u * pp;

  k2_issue<0, 2>(S2, C); k2_issue<0, 3>(S2, C); k2_issue<1, 2>(S2, C); k2_issue<1, 3>(S2, C);
  k2_issue<4, 0>(S2, C); k2_issue<4, 1>(S2, C);
  STAMP(p.dg, 16)
  {
    float za[4], zb[4];
#pragma unroll
    for (int e = 0; e < 4; ++e) {
      const int da = 4 * (g - 1) + e, db = da + 12;
      const float ta = *LDSP(float, lb + L2_KV + (cr * 352 + (g > 0 ? da * 19 + 18 : 0)) * 4);
      const float tb = *LDSP(float, lb + L2_KV + (cr * 352 + (g > 0 && db < 18 ? db * 19 + 18 : 0)) * 4);
      za[e] = g > 0 ? ta : 0.f;
      zb[e] = g > 0 && db < 18 ? tb : 0.f;
    }
    const int piA = 6 * cr + g - 1, piB = 6 * cr + g + 2;
    const unsigned base = (KS_FFN + 3u * xc) * 131072u + T0 * 1024u + c16 * 16u;
    const unsigned oA = g == 0 ? 0xf0000000u : base + (piA >> 3) * 131072u + (piA & 3) * 256u + ((piA >> 2) & 1) * 8u;
    const unsigned oB = g == 0 ? 0xf0000000u : base + (piB >> 3) * 131072u + (piB & 3) * 256u + ((piB >> 2) & 1) * 8u;
    asm volatile("s_waitcnt vmcnt(7)" : "+v"(pqv[0]), "+v"(pqv[1]));
#pragma unroll
    for (int T = 0; T < 2; ++T) {
      const u32x4 u = pqv[T];
      const float v[8] = {bf_lo(u[0]), bf_hi(u[0]), bf_lo(u[1]), bf_hi(u[1]), bf_lo(u[2]), bf_hi(u[2]), bf_lo(u[3]), bf_hi(u[3])};
      float den = 0.f;
#pragma unroll
      for (int e = 0; e < 4; ++e) den += v[e] * za[e] + v[4 + e] * zb[e];
      {
        const unsigned d0 = __float_as_uint(den);
        const auto r16 = __builtin_amdgcn_permlane16_swap(d0, d0, false, false);
        const unsigned r0 = r16[0], r1 = r16[1];
        const unsigned d1 = __float_as_uint(__uint_as_float(r0) + __uint_as_float(r1));
        const auto r32 = __builtin_amdgcn_permlane32_swap(d1, d1, false, false);
        const unsigned s0 = r32[0], s1 = r32[1];
        den = __uint_as_float(s0) + __uint_as_float(s1);
      }
      const float inv = frcp(den + 1e-6f);
      const u32x2 va = {pack2(v[0] * inv, v[1] * inv), pack2(v[2] * inv, v[3] * inv)};
      const u32x2 vb = {pack2(v[4] * inv, v[5] * inv), pack2(v[6] * inv, v[7] * inv)};
      __builtin_amdgcn_raw_buffer_store_b64(va, rsH, oA + T * 1024u, 0, AUX_SC1);
      __builtin_amdgcn_raw_buffer_store_b64(vb, rsH, oB + T * 1024u, 0, AUX_SC1);
    }
  }
  SB();
  STAMP(p.dg, 7)
  const f32x4 z4 = {0.f, 0.f, 0.f, 0.f};
  {
    const int hh = w >> 1, d = 16 * (w & 1) + c16;
    const unsigned ao = lb + L2_WO + hh * L2_SLOT + (8 * g + q) * 288u + 8u * pp;
    float v[8];
    const unsigned ka0 = lb + L2_KV + (hh * 352 + (d < 18 ? d : 0) * 19 + 8 * (g < 2 ? g : 2)) * 4;
#pragma unroll
    for (int e = 0; e < 8; ++e) v[e] = *LDSP(float, ka0 + e * 4);
    s16x8 A[9];
#pragma unroll
    for (int c = 0; c < 9; ++c) A[c] = trfrag(ao + c * 32, ao + c * 32 + 4 * 288);
    unsigned bh[4], bl[4];
#pragma unroll
    for (int e2 = 0; e2 < 4; ++e2) {
      const float v0 = (d < 18 && 8 * g + 2 * e2 < 18) ? v[2 * e2] : 0.f, v1 = (d < 18 && 8 * g + 2 * e2 + 1 < 18) ? v[2 * e2 + 1] : 0.f;
      bh[e2] = pack2(v0, v1);
      bl[e2] = pack2(v0 - bf_lo(bh[e2]), v1 - bf_hi(bh[e2]));
    }
    const s16x8 Bh = mk_frag(bh[0], bh[1], bh[2], bh[3]), Bl = mk_frag(bl[0], bl[1], bl[2], bl[3]);
    const int ka = 24 * hh + d;
    const unsigned mo = d < 24 ? lb + L2_MT + (ka >> 5) * L2_SLOT + (ka & 31) * 288u + 8u * g : lb + k2_slot(L2_NS - 1) + l * 8u;
    f32x4 D[9];
#pragma unroll
    for (int c = 0; c < 9; ++c) D[c] = mfma16(A[c], Bh, z4);
#pragma unroll
    for (int c = 0; c < 9; ++c) D[c] = mfma16(A[c], Bl, D[c]);
#pragma unroll
    for (int c = 0; c < 9; ++c) *LDSP(u32x2, mo + c * 32) = u32x2{pack2(D[c][0], D[c][1]), pack2(D[c][2], D[c][3])};
  }
  SB();
  STAMP(p.dg, 9)
  k2_issue<2, 2>(S2, C); k2_issue<2, 3>(S2, C); k2_issue<3, 2>(S2, C); k2_issue<3, 3>(S2, C);
  k2_issue<4, 2>(S2, C); k2_issue<4, 3>(S2, C);
  STAMP(p.dg, 11)
  asm volatile("s_waitcnt lgkmcnt(0)\n\ts_barrier" ::: "memory");
  C.chf = ctr + (CT_H2 + xc * 8 + ib) * 32;
  STAMP(p.dg, 17)
  C.hsoA = hT + (KS_FFN + 3u * xc) * 131072u;
  asm volatile("s_waitcnt vmcnt(13)" : "+v"(S2.hr[0][0]), "+v"(S2.hr[0][1]));
  k2_read<0, 0>(S2, C); k2_read<0, 1>(S2, C); k2_read<1, 0>(S2, C); k2_read<1, 1>(S2, C); k2_read<2, 0>(S2, C);
  k2_read<2, 1>(S2, C); k2_read<3, 0>(S2, C); k2_read<3, 1>(S2, C); k2_read<4, 0>(S2, C); k2_read<4, 1>(S2, C);
  SB();
  k2_step<0>(S2, C);
  STAMP(p.dg, 23) STAMPR(p.dg, 28)

  const int sp = xc, ni = cr, mi = ib;
  const int grp = mi * 4 + ni;
  k2_send<8>(S2, C);
  const int tok0 = 256 * mi + 32 * sp;
  const rsrc_t rsXr = mk_rsrc(p.x, S_ * D_ * 2);
  u32x4 xr[2];
  unsigned xo[2];
#pragma unroll
  for (int i = 0; i < 2; ++i) {
    const int it = tid + 512 * i, tk = it / 18, ch = it - tk * 18;
    xo[i] = it < 576 ? ((tok0 + tk) * D_ + 144 * ni + 8 * ch) * 2u : 0xf0000000u;
  }
  bload16<0>(xr[0], xo[0], rsXr, 0);
  bload16<0>(xr[1], xo[1], rsXr, 0);
  if (w == sp) ctr_wait(C.cxa + sp * 32, 8u);
  asm volatile("s_waitcnt lgkmcnt(0)\n\ts_barrier" ::: "memory");
  STAMP(p.dg, 24)
  u32x4 v[8];
  auto fetch = [&](int c) {
#pragma unroll
    for (int src = 0; src < 8; ++src)
      bload16_sc1<0>(v[src], src == sp ? 0xf0000000u : hvoff + c * 1024u, rsB, ((grp * 8 + sp) * 8 + src) * 9216u);
  };
  auto reduce = [&](int c) {
    asm volatile("s_waitcnt vmcnt(0)" : "+v"(v[0]), "+v"(v[1]), "+v"(v[2]), "+v"(v[3]), "+v"(v[4]), "+v"(v[5]), "+v"(v[6]), "+v"(v[7]));
    f32x4 a0 = *LDSP(f32x4, lb + L2_OWN + (2 * c) * 1024u + l * 16u), a1 = *LDSP(f32x4, lb + L2_OWN + (2 * c + 1) * 1024u + l * 16u);
#pragma unroll
    for (int src = 0; src < 8; ++src) {
      a0 += f32x4{bf_lo(v[src][0]), bf_hi(v[src][0]), bf_lo(v[src][1]), bf_hi(v[src][1])};
      a1 += f32x4{bf_lo(v[src][2]), bf_hi(v[src][2]), bf_lo(v[src][3]), bf_hi(v[src][3])};
    }
    *LDSP(f32x4, lb + L2_OUT + (c16 * 148 + 16 * c + 4 * g) * 4) = a0;
    *LDSP(f32x4, lb + L2_OUT + ((16 + c16) * 148 + 16 * c + 4 * g) * 4) = a1;
  };
  if (w < 5) {
    fetch(w);
    asm volatile("s_waitcnt vmcnt(8)\n\ts_barrier" ::: "memory");
  } else {
    asm volatile("s_waitcnt vmcnt(0)\n\ts_barrier" ::: "memory");
  }
  gu32* const cxb = ctr + (CT_X2 + grp * 8) * 32;
  if (w == 7) {
    if (l < 8) ctr_add(cxb + l * 32);
    ctr_wait(cxb + sp * 32, 8u);
  }
  if (w < 5) reduce(w);
  asm volatile("s_waitcnt vmcnt(0) lgkmcnt(0)\n\ts_barrier" ::: "memory");
  STAMP(p.dg, 25) STAMPR(p.dg, 29)
  if (w >= 4) { fetch(w + 1); reduce(w + 1); }
  asm volatile("s_waitcnt vmcnt(0)" : "+v"(xr[0]), "+v"(xr[1]));
  __syncthreads();
#pragma unroll
  for (int i = 0; i < 2; ++i) {
    const int it = tid + 512 * i;
    if (it < 576) {
      const int tk = it / 18, ch = it - tk * 18;
      const f32x4 a = *LDSP(f32x4, lb + L2_OUT + (tk * 148 + 8 * ch) * 4), c = *LDSP(f32x4, lb + L2_OUT + (tk * 148 + 8 * ch + 4) * 4);
      u32x4 ov;
      ov[0] = pack2(a[0] + bf_lo(xr[i][0]), a[1] + bf_hi(xr[i][0]));
      ov[1] = pack2(a[2] + bf_lo(xr[i][1]), a[3] + bf_hi(xr[i][1]));
      ov[2] = pack2(c[0] + bf_lo(xr[i][2]), c[1] + bf_hi(xr[i][2]));
      ov[3] = pack2(c[2] + bf_lo(xr[i][3]), c[3] + bf_hi(xr[i][3]));
      *(u32x4*)((char*)p.out + xo[i]) = ov;
    }
  }
  while (__builtin_amdgcn_s_memrealtime() - t_start < 3800ull) __builtin_amdgcn_s_sleep(1);
  STAMP(p.dg, 26) STAMPR(p.dg, 31) STAMP_DUMP(p.dg)
  asm volatile("98765:");
}

extern "C" void kernel_launch(void* const* d_in, const int* in_sizes, int n_in, void* d_out, int out_size,
                              void* d_ws, size_t ws_size, hipStream_t stream) {
  const uint16_t* x = (const uint16_t*)d_in[0];
  const void* wqkv = d_in[1];
  const uint16_t* wo = (const uint16_t*)d_in[2];
  const void* wg = d_in[4];
  const void* wu = d_in[5];
  const void* wd = d_in[6];
  char* ws = (char*)d_ws;

  k0_ln<<<256, 512, 0, stream>>>(x, (u32x4*)(ws + OFF_XF), (const uint16_t*)wqkv, (u32x4*)(ws + OFF_WQ),
                                 (unsigned*)(ws + OFF_CT));
  KFP p;
  p.xf = ws + OFF_XF; p.wq = ws + OFF_WQ; p.wg = wg; p.wu = wu; p.wd = wd; p.wo = wo; p.x = x;
  p.hf = ws + OFF_HF; p.kv = ws + OFF_KV; p.xb = ws + OFF_XB; p.ctr = ws + OFF_CT; p.out = d_out;
  kf_main<<<256, 512, 0, stream>>>(p);
}
```

```cpp
#include <hip/hip_runtime.h>
#include <hip/hip_bf16.h>
#include <stdint.h>

#define DI __device__ __forceinline__
typedef __attribute__((ext_vector_type(8))) short s16x8;
typedef __attribute__((ext_vector_type(4))) short s16x4;
typedef __attribute__((ext_vector_type(4))) float f32x4;
typedef __attribute__((ext_vector_type(4))) unsigned u32x4;
typedef __attribute__((ext_vector_type(2))) unsigned u32x2;
typedef __amdgpu_buffer_rsrc_t rsrc_t;
#define LDSP(T, a) ((__attribute__((address_space(3))) T*)(size_t)(a))

constexpr int S_ = 2048, D_ = 576, F_ = 4608, NH = 32, DH = 18;
constexpr int KS_FFN = 144, KS_ALL = 168;
constexpr size_t OFF_XF = 0;
constexpr size_t SZ_XF = (size_t)128 * 18 * 1024;
constexpr size_t OFF_WQ = OFF_XF + SZ_XF;
constexpr size_t SZ_WQ = (size_t)32 * 72 * 1024;
constexpr size_t OFF_HF = OFF_WQ + SZ_WQ;
constexpr size_t SZ_HF = (size_t)KS_ALL * 128 * 1024;
constexpr size_t OFF_KV = OFF_HF + SZ_HF;
constexpr size_t SZ_KV = (size_t)32 * 8 * 352 * 4;
constexpr size_t OFF_XB = OFF_KV + SZ_KV;
constexpr size_t SZ_XB = (size_t)32 * 8 * 8 * 9 * 1024;
constexpr size_t OFF_CT = OFF_XB + SZ_XB;
constexpr int CT_X = 0, CT_X2 = 256, CT_KV = 512, CT_H = 520, CT_H2 = 584, CT_N = 648;
constexpr size_t SZ_CT = (size_t)CT_N * 128;

DI rsrc_t mk_rsrc(const void* p, unsigned bytes) {
  return __builtin_amdgcn_make_buffer_rsrc((void*)p, (short)0, (int)bytes, 0x00020000);
}
DI unsigned rfl(unsigned v) { return __builtin_amdgcn_readfirstlane(v); }
DI float bf_lo(unsigned u) { return __uint_as_float(u << 16); }
DI float bf_hi(unsigned u) { return __uint_as_float(u & 0xffff0000u); }
DI unsigned pack2(float lo, float hi) {
  typedef __attribute__((ext_vector_type(2))) __bf16 bf2;
  typedef __attribute__((ext_vector_type(2))) float f2;
  f2 v = {lo, hi};
  bf2 r = __builtin_convertvector(v, bf2);
  return __builtin_bit_cast(unsigned, r);
}
DI s16x8 mk_frag(unsigned a, unsigned b, unsigned c, unsigned d) {
  u32x4 v = {a, b, c, d};
  return __builtin_bit_cast(s16x8, v);
}
DI f32x4 mfma16(s16x8 a, s16x8 b, f32x4 c) { return __builtin_amdgcn_mfma_f32_16x16x32_bf16(a, b, c, 0, 0, 0); }
DI s16x4 trread(unsigned addr) { return __builtin_amdgcn_ds_read_tr16_b64_v4i16(LDSP(s16x4, addr)); }
DI s16x8 trfrag(unsigned a0, unsigned a1) {
  s16x4 lo = trread(a0), hi = trread(a1);
  return __builtin_shufflevector(lo, hi, 0, 1, 2, 3, 4, 5, 6, 7);
}
template <int ROLE = 0> DI void glds16(unsigned m0v, unsigned voff, rsrc_t rs, unsigned soff) {
  asm volatile("s_mov_b32 m0, %0\n\ts_nop 0\n\tbuffer_load_dwordx4 %1, %2, %3 offen lds ; role %4"
               :: "s"(m0v), "v"(voff), "s"(rs), "s"(soff), "i"(ROLE) : "memory");
}
DI void warm_lines(unsigned m0v, unsigned voff, rsrc_t rs) {
  asm volatile("s_mov_b32 m0, %0\n\ts_nop 0\n\tbuffer_load_dword %1, %2, 0 offen lds" :: "s"(m0v), "v"(voff), "s"(rs) : "memory");
}
template <int IMM> DI void bload16(u32x4& d, unsigned voff, rsrc_t rs, unsigned soff) {
  asm volatile("buffer_load_dwordx4 %0, %1, %2, %3 offen offset:%4"
               : "=v"(d) : "v"(voff), "s"(rs), "s"(soff), "i"(IMM));
}
template <int IMM, int ROLE = 0> DI void bload16_sc1(u32x4& d, unsigned voff, rsrc_t rs, unsigned soff) {
  asm volatile("buffer_load_dwordx4 %0, %1, %2, %3 offen offset:%4 sc1 ; role %5"
               : "=v"(d) : "v"(voff), "s"(rs), "s"(soff), "i"(IMM), "i"(ROLE));
}
template <int IMM> DI void bstore16_sc1(u32x4 v, unsigned voff, rsrc_t rs, unsigned soff) {
  asm volatile("buffer_store_dwordx4 %0, %1, %2, %3 offen offset:%4 sc1\n\ts_nop 1"
               :: "v"(v), "v"(voff), "s"(rs), "s"(soff), "i"(IMM) : "memory");
}
typedef __attribute__((address_space(1))) unsigned gu32;
DI void ctr_add(gu32* c) { __hip_atomic_fetch_add(c, 1u, __ATOMIC_RELAXED, __HIP_MEMORY_SCOPE_AGENT); }
DI void ctr_wait(gu32* c, unsigned n) {
  for (unsigned spins = 0; spins < (1u << 20); ++spins) {
    if ((unsigned)__builtin_amdgcn_readfirstlane(__hip_atomic_load(c, __ATOMIC_RELAXED, __HIP_MEMORY_SCOPE_AGENT)) >= n) break;
    __builtin_amdgcn_s_sleep(1);
  }
}
template <int N> DI void wait_vm2(u32x4& a, u32x4& b) {
  asm volatile("s_waitcnt vmcnt(%2)" : "+v"(a), "+v"(b) : "i"(N));
}
template <int N> DI void wait_bar_nolgkm() {
  asm volatile("s_waitcnt vmcnt(%0)\n\ts_barrier" :: "i"(N) : "memory");
}
template <int N> DI void wait_bar() {
  asm volatile("s_waitcnt vmcnt(%0) lgkmcnt(0)\n\ts_barrier" :: "i"(N) : "memory");
}
DI float fexp2(float x) { return __builtin_amdgcn_exp2f(x); }
DI float frcp(float x) { return __builtin_amdgcn_rcpf(x); }
DI float phi_f(float t) { return (t > 0.f ? t + 1.f : fexp2(t * 1.4426950408889634f)) + 1e-6f; }
DI float geglu(float g, float u) {
  float e = fexp2(g * __builtin_fmaf(g * g, -0.10294323970f, -2.30220819814f));
  return (g * u) * frcp(1.f + e);
}
template <int CTRL> DI float dpp_add(float v) {
  return v + __builtin_bit_cast(float, __builtin_amdgcn_update_dpp(0, __builtin_bit_cast(int, v), CTRL, 0xf, 0xf, true));
}
DI float reduce32(float v) {
  v = dpp_add<0xB1>(v);
  v = dpp_add<0x4E>(v);
  v = dpp_add<0x141>(v);
  v = dpp_add<0x140>(v);
  const unsigned u = __float_as_uint(v);
  const auto r = __builtin_amdgcn_permlane16_swap(u, u, false, false);
  const unsigned r0 = r[0], r1 = r[1];
  return __uint_as_float(r0) + __uint_as_float(r1);
}

#define ABL2_NOW 0
#define ABL2_NOH 0
#define K0_NORETILE 0
#define K0_NOLN 0
#define STAGGER 0
#define WS_TOP 1
#define NO_CODEWARM0 0
#define NO_CODEWARM 0
#define NO_WARM0 1
#define WARM_MODE 2
#define ABL_NOW 0
#define ABL_NOH 0
#define ABL_NOCOMP 0
#define ABL1_NOCOMP 0
#define ABL1_NOEPI 0
#define ABL1_NOSTAGE 0
#define STAMP(buf, slot)
#define STAMPR(buf, slot)
#define STAMP_DUMP(buf)
__global__ __launch_bounds__(512, 2) void k0_ln(const uint16_t* __restrict__ x, u32x4* __restrict__ xf,
                                                const uint16_t* __restrict__ wqkv, u32x4* __restrict__ wq,
                                                unsigned* __restrict__ ctr) {
  __shared__ u32x4 stage[4][288];
  const int l = threadIdx.x & 63, w = threadIdx.x >> 6;
  if (threadIdx.x < CT_N / 8) ctr[blockIdx.x * (CT_N / 8) + threadIdx.x] = 0u;
  if (w == 7 && !NO_CODEWARM0) {
    unsigned long long pc;
    unsigned off, len;
    asm volatile("s_getpc_b64 %0\n98763:\n\ts_mov_b32 %1, 98764f-98763b\n\ts_mov_b32 %2, 98765f-98764f" : "=s"(pc), "=s"(off), "=s"(len));
    const unsigned v = __builtin_amdgcn_raw_buffer_load_b32(mk_rsrc((const char*)pc + off, len), l < 2 ? (blockIdx.x * 2 + l) * 128u : 0xf0000000u, 0, 0);
    asm volatile("" :: "v"(v));
    return;
  }
  if (w >= 4) {
    const int item = blockIdx.x + 256 * (w - 4);
    if (item >= 576 || K0_NORETILE) return;
    const int h = item / 18, s = item - 18 * h, g = l >> 4, c = l & 15;
    typedef u32x4 __attribute__((aligned(4))) u32x4_a4;
    u32x4* st = stage[w - 4];
    u32x4 t[5];
#pragma unroll
    for (int i = 0; i < 5; ++i) {
      const int ci = 64 * i + l < 288 ? 64 * i + l : 0, row = ci / 9, c9 = ci - 9 * row, sec = c9 / 3, cc = c9 - 3 * sec;
      const int col = (sec == 0 ? 576 : (sec == 1 ? 0 : 1152)) + 18 * h + (cc == 2 ? 10 : 8 * cc);
      t[i] = *(const u32x4_a4*)(wqkv + (size_t)(32 * s + row) * (3 * D_) + col);
    }
    asm volatile("" : "+v"(t[0]), "+v"(t[1]), "+v"(t[2]), "+v"(t[3]), "+v"(t[4]));
#pragma unroll
    for (int i = 0; i < 5; ++i)
      if (64 * i + l < 288) st[64 * i + l] = t[i];
    __builtin_amdgcn_fence(__ATOMIC_RELEASE, "wavefront");
    __builtin_amdgcn_wave_barrier();
    const int sec[4] = {0, c < 4 ? 0 : 48, 96, c < 4 ? 96 : 48};
    const int dim[4] = {c, c < 4 ? 14 + c : c - 4, c, c < 4 ? 14 + c : (c < 8 ? c + 8 : 14 + (c & 3))};
    unsigned short v[4][8];
#pragma unroll
    for (int X = 0; X < 4; ++X) {
      const uint16_t* src = (const uint16_t*)((const char*)st + 8 * g * 144 + sec[X] + 2 * dim[X] + (dim[X] >= 16 ? 12 : 0));
#pragma unroll
      for (int e = 0; e < 8; ++e) v[X][e] = src[e * 72];
    }
#pragma unroll
    for (int X = 0; X < 4; ++X) {
      u32x4 o;
#pragma unroll
      for (int e = 0; e < 4; ++e) o[e] = (unsigned)v[X][2 * e] | ((unsigned)v[X][2 * e + 1] << 16);
      wq[((size_t)item * 4 + X) * 64 + l] = o;
    }
    return;
  }
  if (K0_NOLN) return;
  const int tok = blockIdx.x * 8 + w * 2 + (l >> 5), p = l & 31;
  const u32x4* row = (const u32x4*)(x + (size_t)tok * D_);
  u32x4 c[3];
  c[0] = row[p];
  c[1] = row[p + 32];
  c[2] = row[p < 8 ? p + 64 : p];
  const bool has2 = p < 8;
  float v[24];
#pragma unroll
  for (int i = 0; i < 3; ++i)
#pragma unroll
    for (int j = 0; j < 4; ++j) { v[i * 8 + 2 * j] = bf_lo(c[i][j]); v[i * 8 + 2 * j + 1] = bf_hi(c[i][j]); }
  float s = 0.f, s2 = 0.f;
#pragma unroll
  for (int j = 0; j < 16; ++j) s += v[j];
#pragma unroll
  for (int j = 16; j < 24; ++j) s2 += v[j];
  s += has2 ? s2 : 0.f;
  const float mean = reduce32(s) * (1.f / 576.f);
  float q = 0.f, q2 = 0.f;
#pragma unroll
  for (int j = 0; j < 24; ++j) v[j] -= mean;
#pragma unroll
  for (int j = 0; j < 16; ++j) q += v[j] * v[j];
#pragma unroll
  for (int j = 16; j < 24; ++j) q2 += v[j] * v[j];
  q += has2 ? q2 : 0.f;
  const float var = reduce32(q) * (1.f / 576.f);
  const float rstd = 1.f / sqrtf(var + 1e-5f);
  const int T = tok >> 4, t16 = tok & 15;
#pragma unroll
  for (int i = 0; i < 3; ++i) {
    u32x4 o;
#pragma unroll
    for (int j = 0; j < 4; ++j) o[j] = pack2(v[i * 8 + 2 * j] * rstd, v[i * 8 + 2 * j + 1] * rstd);
    const int ch = p + 32 * i;
    if (i < 2 || has2) xf[((size_t)T * 18 + (ch >> 2)) * 64 + (ch & 3) * 16 + t16] = o;
  }
}

constexpr unsigned L1_BUFA = 0, L1_BUFB = 73728, L1_SCR = 147456, L1_TOTAL = 163840;
constexpr int AUX_SC1 = 16;
#define SB() __builtin_amdgcn_sched_barrier(0)
#define PIN(x) asm volatile("" : "+v"(x))

struct KFP {
  const void* xf; const void* wq; const void* wg; const void* wu; const void* wd; const void* wo; const void* x;
  void* hf; void* kv; void* xb; void* ctr; void* out;
};

struct K1S {
  u32x4 xf[2][18];
  s16x4 wlo[6], whi[6];
  f32x4 acc[2][2];
  unsigned gel[4][2][2];
  u32x4 hq[2];
  float t0, t1;
  unsigned ard;
  u32x4 kp[8];
  unsigned flag;
};
struct K1G { unsigned dst, soff, sstep, vo0, vo1, vok, wm0, wvo, wvo2; rsrc_t rs, rsk, wrs, wrs2; int w; };
template <int I, int ROLE = 0> DI void k1_piece(const K1G& G) {
  if (!ABL1_NOSTAGE) glds16<ROLE>(G.dst + I * 1024u, (I & 1) ? G.vo1 : G.vo0, G.rs, G.soff + I * G.sstep);
}
template <int I, int ROLE = 0> DI void k1_piece2(const K1G& G, int l) {
  const int ci = 64 * I + l, rho = ci / 18, c = ci - 18 * rho;
  glds16<ROLE>(G.dst + I * 1024u, rho * 1152u + c * 16u, G.rs, G.soff);
}
template <int STRIDE, int N, int H> DI void k1_read(K1S& S, unsigned a0, int slot_unused = 0) {
  constexpr int P = N / 18, KS = N % 18;
  if constexpr (H == 0 && KS == 0) { S.ard = STRIDE == 4096 ? (a0 ^ (P << 5)) : a0 + P * 36864u; PIN(S.ard); }
  if constexpr (H == 0 && KS == 9) { S.ard += 9 * STRIDE; PIN(S.ard); }
  if constexpr (H == 0) S.wlo[N % 6] = trread(S.ard + (KS % 9) * STRIDE);
  else S.whi[N % 6] = trread(S.ard + (KS % 9) * STRIDE + STRIDE / 8);
}
template <int KIND, int U, int A, int PR, int PH, int SL> DI void k1_epi(K1S& S) {
  constexpr int T = PR >> 1, R = 2 * (PR & 1);
  const float a = S.acc[A][T][R], b = S.acc[A][T][R + 1];
  if (ABL1_NOEPI) {
    if (PH == 3 && SL == 1) { if (KIND == 1) S.gel[U][T][PR & 1] = pack2(a, b); else S.hq[T][2 * (U & 1) + (PR & 1)] = pack2(a, b) ^ S.gel[U][T][PR & 1]; }
    return;
  }
  if constexpr (KIND == 1) {
    if constexpr (PH == 0 && SL == 0) { S.t0 = a * a; S.t1 = b * b; }
    if constexpr (PH == 0 && SL == 1) {
      S.t0 = __builtin_fmaf(S.t0, -0.10294323970f, -2.30220819814f);
      S.t1 = __builtin_fmaf(S.t1, -0.10294323970f, -2.30220819814f);
    }
    if constexpr (PH == 1 && SL == 0) { S.t0 *= a; S.t1 *= b; }
    if constexpr (PH == 1 && SL == 1) { S.t0 = fexp2(S.t0); S.t1 = fexp2(S.t1); }
    if constexpr (PH == 2 && SL == 0) { S.t0 += 1.f; S.t1 += 1.f; }
    if constexpr (PH == 2 && SL == 1) { S.t0 = frcp(S.t0); S.t1 = frcp(S.t1); }
    if constexpr (PH == 3 && SL == 0) { S.t0 *= a; S.t1 *= b; }
    if constexpr (PH == 3 && SL == 1) { unsigned v = pack2(S.t0, S.t1); PIN(v); S.gel[U][T][PR & 1] = v; }
    else { PIN(S.t0); PIN(S.t1); }
  } else {
    if constexpr (PH == 1 && SL == 0) { const unsigned u = S.gel[U][T][PR & 1]; S.t0 = bf_lo(u); S.t1 = bf_hi(u); PIN(S.t0); PIN(S.t1); }
    if constexpr (PH == 2 && SL == 0) { S.t0 *= a; S.t1 *= b; PIN(S.t0); PIN(S.t1); }
    if constexpr (PH == 3 && SL == 0) { unsigned v = pack2(S.t0, S.t1); PIN(v); S.hq[T][2 * (U & 1) + (PR & 1)] = v; }
  }
}
template <int KIND, int U, int A, int I = 0> DI void k1_epi_all(K1S& S) {
  if constexpr (I < 32) { k1_epi<KIND, U, A, I / 8, (I / 2) % 4, I % 2>(S); k1_epi_all<KIND, U, A, I + 1>(S); }
}
DI void k1_store_pair(K1S& S, rsrc_t rsH, unsigned voff, unsigned soff) {
  bstore16_sc1<0>(S.hq[0], voff, rsH, soff);
  bstore16_sc1<1024>(S.hq[1], voff, rsH, soff);
}
template <int KIND, int PREV, int NP, int STRIDE, int NSTRIDE, int STG, int SIG, int N>
DI void k1_step(K1S& S, unsigned ab, unsigned abn, const K1G& G, rsrc_t rsH, unsigned hvoff, unsigned hso, unsigned hsop,
                gu32* sig, gu32* sig2) {
  constexpr int P = N / 18, KS = N % 18, A = P & 1, NT = NP * 18;
  constexpr int EK = P == 0 ? (PREV == 3 ? 2 : PREV) : (NP == 2 ? 1 : KIND);
  constexpr int EU = P == 0 ? (PREV == 3 ? 0 : 3) : (NP == 2 ? 0 : P - 1);
  constexpr int EA = P == 0 ? 1 : (A ^ 1);
  constexpr bool EPI = EK != 0 && KS >= 1 && KS <= 16;
  const f32x4 z4 = {0.f, 0.f, 0.f, 0.f};
  const s16x8 wf = __builtin_shufflevector(S.wlo[N % 6], S.whi[N % 6], 0, 1, 2, 3, 4, 5, 6, 7);
  const s16x8 x0 = __builtin_bit_cast(s16x8, S.xf[0][KS]), x1 = __builtin_bit_cast(s16x8, S.xf[1][KS]);
  if (!ABL1_NOCOMP) S.acc[A][0] = mfma16(wf, x0, KS == 0 ? z4 : S.acc[A][0]);
  else if (KS == 0) { S.acc[A][0] = z4; S.acc[A][0][0] = (float)wf[0]; }
  SB();
  if constexpr (N + 5 < NT) k1_read<STRIDE, N + 5, 0>(S, ab);
  else if constexpr (NSTRIDE != 0) k1_read<NSTRIDE == 0 ? 4096 : NSTRIDE, N + 5 - NT, 0>(S, abn);
  if constexpr (EPI) k1_epi<EK, EU, EA, (KS - 1) / 4, (KS - 1) % 4, 0>(S);
  SB();
  if (!ABL1_NOCOMP) S.acc[A][1] = mfma16(wf, x1, KS == 0 ? z4 : S.acc[A][1]);
  else if (KS == 0) { S.acc[A][1] = z4; }
  SB();
  if constexpr (N + 5 < NT) k1_read<STRIDE, N + 5, 1>(S, ab);
  else if constexpr (NSTRIDE != 0) k1_read<NSTRIDE == 0 ? 4096 : NSTRIDE, N + 5 - NT, 1>(S, abn);
  if constexpr (EPI) k1_epi<EK, EU, EA, (KS - 1) / 4, (KS - 1) % 4, 1>(S);
  constexpr int PS = WARM_MODE == 2 ? 3 : 6;
  if constexpr (STG == 1 && NP == 4 && N % PS == 2 && N / PS < 9) k1_piece<N / PS>(G);
  if constexpr (STG == 1 && NP == 2 && N % 2 == 0 && N >= 2 && N / 2 <= 9) k1_piece<N / 2 - 1>(G);
  if constexpr (WARM_MODE == 2 && STG == 1 && N == (NP == 4 ? 45 : 22)) warm_lines(G.wm0, G.wvo, G.wrs);
  if constexpr (WARM_MODE == 1 && SIG == 1 && N == NT - 7) warm_lines(G.wm0, G.wvo, G.wrs);
  if constexpr (WARM_MODE == 1 && SIG == 1 && N == NT - 5) warm_lines(G.wm0, G.wvo2, G.wrs2);
  if constexpr (STG == 2 && N % 6 == 2 && N / 6 < 9) k1_piece2<N / 6>(G, hvoff >> 4);
  if constexpr (STG == 2 && N % 6 == 5 && N / 6 < 8) bload16_sc1<0>(S.kp[N / 6], G.vok, G.rsk, (N / 6) * 1408u);
  if constexpr (PREV == 2 && N == 17) k1_store_pair(S, rsH, hvoff, hsop);
  if constexpr (PREV == 3 && N == 17) {
#pragma unroll
    for (int T = 0; T < 2; ++T) {
      const u32x2 v = {S.hq[T][0], S.hq[T][1]};
      __builtin_amdgcn_raw_buffer_store_b64(v, rsH, hvoff + T * 1024u, hsop, AUX_SC1);
    }
  }
  if constexpr (KIND == 2 && NP == 4 && N == 2 * 18 + 17) k1_store_pair(S, rsH, hvoff, hso);
  if constexpr (N == 1) asm volatile("s_barrier" ::: "memory");
  if constexpr (SIG == 2 && N == NT - 8) { if (rfl(threadIdx.x >> 6) == 0) ctr_wait(sig, 32u); }
  if constexpr (SIG == 3 && N == 30) { if (G.w == 0) asm volatile("global_load_dword %0, %1, off sc1 ; role 3" : "=v"(S.flag) : "v"(sig2)); }
  if constexpr (SIG == 3 && N == NT - 8) {
    if (G.w == 0) {
      asm volatile("s_waitcnt vmcnt(9)" : "+v"(S.flag));
      if (rfl(S.flag) < 4u) ctr_wait(sig2, 4u);
    }
    asm volatile("s_barrier" ::: "memory");
  }
  if constexpr (NSTRIDE != 0 && N == NT - 8) wait_bar_nolgkm<(KIND == 2 ? 2 : 0) + (WARM_MODE == 2 ? 1 : 0)>();
  if constexpr (SIG == 1 && N == NT - 8) { if (threadIdx.x == 0) ctr_add(sig); }
  if constexpr (SIG == 2 && N == NT - 8) { if (threadIdx.x == 0) ctr_add(sig2); }
  SB();
  if constexpr (N + 1 < NT) k1_step<KIND, PREV, NP, STRIDE, NSTRIDE, STG, SIG, N + 1>(S, ab, abn, G, rsH, hvoff, hso, hsop, sig, sig2);
}
template <int STRIDE> DI void k1_first_reads(K1S& S, unsigned ab) {
  k1_read<STRIDE, 0, 0>(S, ab); k1_read<STRIDE, 0, 1>(S, ab); k1_read<STRIDE, 1, 0>(S, ab); k1_read<STRIDE, 1, 1>(S, ab);
  k1_read<STRIDE, 2, 0>(S, ab); k1_read<STRIDE, 2, 1>(S, ab); k1_read<STRIDE, 3, 0>(S, ab); k1_read<STRIDE, 3, 1>(S, ab);
  k1_read<STRIDE, 4, 0>(S, ab); k1_read<STRIDE, 4, 1>(S, ab);
  SB();
}
template <int KS> DI void k1_qkv_step(K1S& S, s16x8 (&wq)[2][4], f32x4 (&Q)[4][2], unsigned qa, unsigned xv, rsrc_t rsX,
                                      unsigned xsb, const K1G& G) {
  if constexpr (KS + 6 < 18) {
    bload16<((KS + 6) & 3) * 1024>(S.xf[0][KS + 6], xv, rsX, xsb + ((KS + 6) >> 2) * 4096u);
    bload16<((KS + 6) & 3) * 1024>(S.xf[1][KS + 6], xv, rsX, xsb + 18432u + ((KS + 6) >> 2) * 4096u);
  } else if constexpr (KS < 15) { k1_piece<2 * (KS - 12)>(G); k1_piece<2 * (KS - 12) + 1>(G); }
  else k1_piece<KS - 9>(G);
  constexpr int YOUNGER = (ABL1_NOSTAGE ? (KS < 12 ? 12 : 2 * (17 - KS)) : (KS < 15 ? 12 : 26 - KS)) + (KS < 6 && !NO_WARM0 ? 2 : 0);
  wait_vm2<YOUNGER>(S.xf[0][KS], S.xf[1][KS]);
  const s16x8 x0 = __builtin_bit_cast(s16x8, S.xf[0][KS]), x1 = __builtin_bit_cast(s16x8, S.xf[1][KS]);
  SB();
#pragma unroll
  for (int X = 0; X < 4; ++X) {
    Q[X][0] = mfma16(x0, wq[KS & 1][X], Q[X][0]);
    SB();
    if constexpr (KS + 1 < 18) wq[(KS + 1) & 1][X] = *LDSP(s16x8, qa + ((KS + 1) * 4 + X) * 1024u);
    SB();
    Q[X][1] = mfma16(x1, wq[KS & 1][X], Q[X][1]);
    SB();
  }
  if constexpr (KS + 1 < 18) k1_qkv_step<KS + 1>(S, wq, Q, qa, xv, rsX, xsb, G);
}

constexpr int K2D = 5;
constexpr unsigned L2_SLOT = 9216, L2_NS = K2D + 2;
constexpr unsigned L2_WO = L1_BUFA + 4 * L2_SLOT;
constexpr unsigned L2_MT = L1_BUFB + 3 * L2_SLOT, L2_KV = L1_SCR;
constexpr unsigned L2_OWN = L1_BUFB, L2_OUT = L1_BUFA;

struct K2S {
  u32x4 hr[K2D + 1][2];
  u32x4 ha[3][2];
  s16x4 wlo[6], whi[6];
  f32x4 acc[9][2];
  unsigned flag, ardm;
  unsigned ard;
};
struct K2C {
  rsrc_t rsH, rsW, rsB;
  unsigned vo0, vo1, hvoff, hsoF, hsoA, wsoF, xso, lb, lanec;
  int w, sp;
  gu32* chf;
  gu32* cxa;
};
constexpr bool k2_att(int n) { return n >= 18; }
constexpr unsigned k2_loc(int n) {
  return n < 8 ? 4 * (n >> 1) + (n & 1) : n < 10 ? 8 + n : 4 * ((n - 10) >> 1) + 2 + (n & 1);
}
constexpr unsigned k2_slot(int s) { return s < 4 ? L1_BUFA + s * L2_SLOT : L1_BUFB + (s - 4) * L2_SLOT; }
constexpr int k2_tW(int m) { return m == 4 ? -30 : 16 * (m - 5); }
constexpr int k2_tH(int m, int half) {
  return m <= 4 ? -48 + 2 * m + half : 16 * (m - 5) + 5 + 2 * half;
}
constexpr int k2_tA(int i) { return 16 * (6 + i) + 8; }
constexpr int k2_younger(int t0, int n) {
  int cnt = 0;
  const int tw = 16 * n + 3;
  for (int m = 4; m <= 17; ++m) if (k2_tW(m) > t0 && k2_tW(m) < tw) ++cnt;
  for (int m = 0; m <= 17; ++m)
    for (int h = 0; h < 2; ++h) if (k2_tH(m, h) > t0 && k2_tH(m, h) < tw) ++cnt;
  for (int i = 0; i < 6; ++i) if (k2_tA(i) > t0 && k2_tA(i) < tw) ++cnt;
  for (int i = 0; i < 4; ++i) if (-29 + i > t0) ++cnt;
  for (int i = 0; i < 2; ++i) if (-38 + i > t0) ++cnt;
  return cnt;
}
constexpr int k2_count(int n) {
  const int m = n + 1, th = k2_tH(m, 1), tw = m >= 4 ? k2_tW(m) : -100;
  return k2_younger(th > tw ? th : tw, n);
}
static_assert(k2_count(0) == 14 && k2_count(1) == 15 && k2_count(3) == 14 && k2_younger(-26, 1) == 4 && k2_count(8) == 12 && k2_count(12) == 14 && k2_count(13) == 12, "schedule");
static_assert(k2_younger(16 * 3 + 4, 5) == 6, "schedule");
template <int T, int I, int ROLE = 0> DI void k2_issue(K2S& S, const K2C& C) {
  if constexpr (T <= 17) {
    constexpr unsigned ks = k2_loc(T);
    const unsigned wso = ABL2_NOW ? 0xf0000000u : C.wsoF + ks * 36864u, hso = ABL2_NOH ? 0xf0000000u : C.hsoF + ks * 131072u;
    if constexpr (I == 0) glds16<ROLE>(C.lb + k2_slot(T % L2_NS) + C.w * 1024u, C.vo0, C.rsW, wso);
    if constexpr (I == 1) { if ((T & 7) == C.w) glds16<3>(C.lb + k2_slot(T % L2_NS) + 8192u, C.vo1, C.rsW, wso); }
    if constexpr (I == 2) bload16_sc1<0, ROLE>(S.hr[T % (K2D + 1)][0], C.hvoff, C.rsH, hso);
    if constexpr (I == 3) bload16_sc1<1024, ROLE>(S.hr[T % (K2D + 1)][1], C.hvoff, C.rsH, hso);
  }
}
constexpr int K2_SEQ = 135;
constexpr int k2_T(int M) { return M < K2_SEQ ? M / 9 : 15 + (M - K2_SEQ) % 6; }
constexpr int k2_c(int M) { return M < K2_SEQ ? M % 9 : (M - K2_SEQ) / 6; }
template <int M, int H> DI void k2_read(K2S& S, const K2C& C) {
  if constexpr (M < 189) {
    constexpr int T = k2_T(M), c = k2_c(M);
    if constexpr (M < K2_SEQ) {
      if constexpr (H == 0 && c == 0) { S.ard = C.lanec + k2_slot(T % L2_NS); PIN(S.ard); }
      if constexpr (H == 0) S.wlo[M % 6] = trread(S.ard + c * 32);
      else S.whi[M % 6] = trread(S.ard + c * 32 + 4608);
    } else {
      static_assert(k2_slot(16 % L2_NS) == k2_slot(15 % L2_NS) + L2_SLOT && k2_slot(17 % L2_NS) == k2_slot(15 % L2_NS) + 2 * L2_SLOT, "ring");
      if constexpr (H == 0 && M == K2_SEQ) { S.ard = C.lanec + k2_slot(15 % L2_NS); PIN(S.ard); S.ardm = C.lanec + L2_MT; PIN(S.ardm); }
      const unsigned a = T <= 17 ? S.ard + (T - 15) * L2_SLOT + c * 32 : S.ardm + (T - 18) * L2_SLOT + c * 32;
      if constexpr (H == 0) S.wlo[M % 6] = trread(a);
      else S.whi[M % 6] = trread(a + 4608);
    }
  }
}
template <int c> DI void k2_send(K2S& S, const K2C& C) {
  if (C.w != C.sp) {
    const u32x4 v = {pack2(S.acc[c][0][0], S.acc[c][0][1]), pack2(S.acc[c][0][2], S.acc[c][0][3]),
                     pack2(S.acc[c][1][0], S.acc[c][1][1]), pack2(S.acc[c][1][2], S.acc[c][1][3])};
    bstore16_sc1<0>(v, C.hvoff, C.rsB, C.xso + c * 1024u);
  } else {
    *LDSP(f32x4, C.lb + L2_OWN + (c * 2) * 1024u + C.hvoff) = S.acc[c][0];
    *LDSP(f32x4, C.lb + L2_OWN + (c * 2 + 1) * 1024u + C.hvoff) = S.acc[c][1];
  }
}
template <int T, int N> DI void k2_wait(K2S& S) {
  asm volatile("s_waitcnt vmcnt(%2)\n\ts_barrier" : "+v"(S.hr[T % (K2D + 1)][0]), "+v"(S.hr[T % (K2D + 1)][1]) : "i"(N) : "memory");
}
template <int M> DI void k2_step(K2S& S, const K2C& C) {
  constexpr int T = k2_T(M), c = k2_c(M);
  constexpr bool seq = M < K2_SEQ;
  const s16x8 wf = __builtin_shufflevector(S.wlo[M % 6], S.whi[M % 6], 0, 1, 2, 3, 4, 5, 6, 7);
  const s16x8 h0 = __builtin_bit_cast(s16x8, k2_att(T) ? S.ha[k2_att(T) ? T - 18 : 0][0] : S.hr[T % (K2D + 1)][0]);
  const s16x8 h1 = __builtin_bit_cast(s16x8, k2_att(T) ? S.ha[k2_att(T) ? T - 18 : 0][1] : S.hr[T % (K2D + 1)][1]);
  const f32x4 z4 = {0.f, 0.f, 0.f, 0.f};
  S.acc[c][0] = mfma16(wf, h0, T == 0 ? z4 : S.acc[c][0]);
  SB();
  k2_read<M + 5, 0>(S, C);
  SB();
  S.acc[c][1] = mfma16(wf, h1, T == 0 ? z4 : S.acc[c][1]);
  SB();
  k2_read<M + 5, 1>(S, C);
  if constexpr (seq) {
    if constexpr (T + K2D <= 17) {
      if constexpr (c == 0) { if (!STAGGER || C.w < 4) k2_issue<T + K2D, 0, STAGGER>(S, C); }
      if constexpr (STAGGER && c == 1) { if (C.w >= 4) k2_issue<T + K2D, 0, 2>(S, C); }
      if constexpr (c == 2) k2_issue<T + K2D, 1>(S, C);
      if constexpr (c == (STAGGER ? 4 : 5)) { if (!STAGGER || C.w < 4) k2_issue<T + K2D, 2, STAGGER>(S, C); }
      if constexpr (STAGGER && c == 5) { if (C.w >= 4) k2_issue<T + K2D, 2, 2>(S, C); }
      if constexpr (c == (STAGGER ? 6 : 7)) { if (!STAGGER || C.w < 4) k2_issue<T + K2D, 3, STAGGER>(S, C); }
      if constexpr (STAGGER && c == 7) { if (C.w >= 4) k2_issue<T + K2D, 3, 2>(S, C); }
    }
    if constexpr (T >= 6 && T <= 11 && c == 8)
      bload16_sc1<((T - 6) & 1) * 1024>(S.ha[(T - 6) >> 1][(T - 6) & 1], C.hvoff, C.rsH, C.hsoA + ((T - 6) >> 1) * 131072u);
    if constexpr (T == 3 && c == 4) {
      if (C.w == 0) asm volatile("global_load_dword %0, %1, off sc1 ; role 3" : "=v"(S.flag) : "v"(C.chf));
    }
    if constexpr (T == 5 && c == 3) {
      if (C.w == 0) {
        asm volatile("s_waitcnt vmcnt(%1)" : "+v"(S.flag) : "i"(k2_younger(16 * 3 + 4, 5) - (STAGGER ? 1 : 0)));
        if (rfl(S.flag) < 4u) ctr_wait(C.chf, 4u);
      }
    }
    if constexpr (c == 3 && T < 14) k2_wait<T + 1, T == 1 ? k2_younger(-26, 1) : k2_count(T)>(S);
    if constexpr (c == 3 && T == 1) { if (threadIdx.x == 0) ctr_add(C.chf); }
    if constexpr (c == 2 && T == 14)
      asm volatile("s_waitcnt vmcnt(0)"
                   : "+v"(S.ha[0][0]), "+v"(S.ha[0][1]), "+v"(S.ha[1][0]), "+v"(S.ha[1][1]), "+v"(S.ha[2][0]), "+v"(S.ha[2][1]),
                     "+v"(S.hr[15 % (K2D + 1)][0]), "+v"(S.hr[15 % (K2D + 1)][1]), "+v"(S.hr[16 % (K2D + 1)][0]), "+v"(S.hr[16 % (K2D + 1)][1]),
                     "+v"(S.hr[17 % (K2D + 1)][0]), "+v"(S.hr[17 % (K2D + 1)][1]));
    if constexpr (c == 3 && T == 14) asm volatile("s_barrier" ::: "memory");
  } else {
    if constexpr (T == 20 && c > 0) k2_send<c - 1>(S, C);
    if constexpr (T == 15 && c == 7) {
      asm volatile("s_waitcnt vmcnt(1)\n\ts_barrier" ::: "memory");
      if (C.w == 0 && (C.hvoff >> 4) < 8) ctr_add(C.cxa + (C.hvoff >> 4) * 32);
    }
  }
  SB();
  if constexpr (seq && c == 8 && T % 5 == 4) { STAMP(S.dg, 18 + T / 5) }
  if constexpr (M + 1 < 189) k2_step<M + 1>(S, C);
}

__global__ __launch_bounds__(512, 2) void kf_main(KFP p) {
  __shared__ __attribute__((aligned(128))) char smem[L1_TOTAL];
  const unsigned lb = (unsigned)(size_t)(__attribute__((address_space(3))) char*)smem;
  const int tid = threadIdx.x, l = tid & 63;
  const int w = rfl(tid >> 6);
  const int b = blockIdx.x, xc = b & 7, r = b >> 3;
  const int cr = r & 3, j = 4 * xc + cr, ib = r >> 2;
  const int g = l >> 4, q = (l & 15) >> 2, pp = l & 3, c16 = l & 15;

  STAMPR(p.dg, 30) STAMP(p.dg, 0)
  const rsrc_t rsX = mk_rsrc(p.xf, (unsigned)SZ_XF);
  const rsrc_t rsQ = mk_rsrc(p.wq, (unsigned)SZ_WQ);
  const rsrc_t rsG = mk_rsrc(p.wg, D_ * F_ * 2);
  const rsrc_t rsU = mk_rsrc(p.wu, D_ * F_ * 2);
  const rsrc_t rsH = mk_rsrc(p.hf, (unsigned)SZ_HF);
  const rsrc_t rsB = mk_rsrc(p.xb, (unsigned)SZ_XB);
  const unsigned xso = (((ib * 4 + cr) * 8 + w) * 8 + xc) * 9216u;
  gu32* const ctr = (gu32*)p.ctr;
  K1S S;

  if (!NO_CODEWARM) {
    unsigned long long pc;
    unsigned len;
    asm volatile("s_getpc_b64 %0\n98764:\n\ts_mov_b32 %1, 98765f-98764b" : "=s"(pc), "=s"(len));
    warm_lines(lb + L1_SCR + 15872u, l < 2 ? ((r * 8 + w) * 2 + l) * 128u : 0xf0000000u, mk_rsrc((const void*)pc, len));
  }

#pragma unroll
  for (int i = 0; i < 9; ++i) glds16(lb + L1_BUFA + (9 * w + i) * 1024u, l * 16u, rsQ, (j * 72u + 9 * w + i) * 1024u);
  const int T0 = 16 * ib + 2 * w;
  const unsigned hvoff = l * 16u, xsb = T0 * 18432u;
#define XL(s) bload16<((s) & 3) * 1024>(S.xf[0][s], hvoff, rsX, xsb + ((s) >> 2) * 4096u); \
              bload16<((s) & 3) * 1024>(S.xf[1][s], hvoff, rsX, xsb + 18432u + ((s) >> 2) * 4096u);
  XL(0) XL(1) XL(2) XL(3) XL(4) XL(5)
#undef XL
  const unsigned wcol = (576u * xc + 128u * cr) * 2u;
  if (!NO_WARM0) {
    const unsigned si = 3 * w + (l - 9);
    const unsigned vo = l < 9 ? (72u * ib + 9u * w + l) * 9216u + wcol
                      : (l < 12 && si < 18 ? (18u * r + si) * 9216u + (576u * xc + 512u) * 2u : 0xf0000000u);
    warm_lines(lb + L1_SCR + 15872u, vo, rsG);
    warm_lines(lb + L1_SCR + 15872u, vo, rsU);
  }
  K1G G;
  G.w = w;
  {
    const unsigned rowv = (l >> 3) * 9216u, c8 = l & 7, s0 = (l >> 4) & 1;
    const unsigned vA = rowv + ((c8 ^ (s0 << 1)) << 4), vB = rowv + ((c8 ^ ((s0 | 2) << 1)) << 4);
    G.vo0 = (w & 1) ? vB : vA;
    G.vo1 = (w & 1) ? vA : vB;
  }
  G.sstep = 8u * 9216u;
  const unsigned wsoff = 72u * w * 9216u, wdst = lb + 72u * w * 128u;
  G.rs = rsG; G.soff = wsoff + wcol; G.dst = wdst + L1_BUFB;

  STAMP(p.dg, 1)
  wait_bar<NO_WARM0 ? 12 : 14>();
  STAMP(p.dg, 2)

  {
    const unsigned qa = lb + L1_BUFA + l * 16u;
    const f32x4 z4 = {0.f, 0.f, 0.f, 0.f};
    f32x4 Q[4][2];
#pragma unroll
    for (int X = 0; X < 4; ++X) Q[X][0] = Q[X][1] = z4;
    s16x8 wq[2][4];
#pragma unroll
    for (int X = 0; X < 4; ++X) wq[0][X] = *LDSP(s16x8, qa + X * 1024u);
    k1_qkv_step<0>(S, wq, Q, qa, hvoff, rsX, xsb, G);
    STAMP(p.dg, 3)
    unsigned fKA[4], fKB[4], fVA[4], fVB[4], fQ2[4];
#pragma unroll
    for (int T = 0; T < 2; ++T)
#pragma unroll
      for (int h2 = 0; h2 < 2; ++h2) {
        fKA[2 * T + h2] = pack2(phi_f(Q[0][T][2 * h2]), phi_f(Q[0][T][2 * h2 + 1]));
        fKB[2 * T + h2] = pack2(phi_f(Q[1][T][2 * h2]), phi_f(Q[1][T][2 * h2 + 1]));
        fVA[2 * T + h2] = pack2(Q[2][T][2 * h2], Q[2][T][2 * h2 + 1]);
        const unsigned raw = pack2(Q[3][T][2 * h2], Q[3][T][2 * h2 + 1]);
        fVB[2 * T + h2] = c16 < 2 ? 0x3f803f80u : raw;
        fQ2[2 * T + h2] = pack2(phi_f(Q[3][T][2 * h2]), phi_f(Q[3][T][2 * h2 + 1]));
      }
    const s16x8 aKA = mk_frag(fKA[0], fKA[1], fKA[2], fKA[3]), aKB = mk_frag(fKB[0], fKB[1], fKB[2], fKB[3]);
    const s16x8 bVA = mk_frag(fVA[0], fVA[1], fVA[2], fVA[3]), bVB = mk_frag(fVB[0], fVB[1], fVB[2], fVB[3]);
    const s16x8 aQ2 = mk_frag(fQ2[0], fQ2[1], fQ2[2], fQ2[3]);
    const f32x4 R0 = mfma16(aKA, bVA, z4), R1 = mfma16(aKA, bVB, z4);
    const f32x4 R2 = mfma16(aKB, bVA, z4), R3 = mfma16(aKB, bVB, z4);
    {
      const unsigned one = (c16 >> 2) == g ? (0x3f80u << (16 * (c16 & 1))) : 0u;
      const unsigned i0 = (c16 & 2) ? 0u : one, i1 = (c16 & 2) ? one : 0u;
#pragma unroll
      for (int T = 0; T < 2; ++T) {
        const s16x8 id = T == 0 ? mk_frag(i0, i1, 0u, 0u) : mk_frag(0u, 0u, i0, i1);
        const f32x4 qa4 = mfma16(aKB, id, z4), qb4 = mfma16(aQ2, id, z4);
        const unsigned b01 = pack2(qb4[0], qb4[1]), b23 = pack2(qb4[2], qb4[3]);
        bstore16_sc1<0>(u32x4{pack2(qa4[0], qa4[1]), pack2(qa4[2], qa4[3]), g == 1 ? b01 : (g == 2 ? b23 : 0u), g == 1 ? b23 : 0u},
                        hvoff + T * 1024u, rsB, xso);
      }
    }
    {
      const unsigned sb = lb + L1_SCR + w * 1408u;
      const int eB = c16 == 0 ? 18 : c16 + 14;
      const bool okB = c16 == 0 || c16 == 2 || c16 == 3;
      const unsigned dummy = lb + L1_SCR + 8 * 1408u + l * 4u;
#pragma unroll
      for (int rr = 0; rr < 4; ++rr) {
        const int d = 4 * g + rr;
        *LDSP(float, sb + (d * 19 + c16) * 4) = R0[rr];
        *LDSP(float, okB ? sb + (d * 19 + eB) * 4 : dummy) = R1[rr];
        if (rr >= 2) {
          *LDSP(float, g == 0 ? sb + ((14 + rr) * 19 + c16) * 4 : dummy) = R2[rr];
          *LDSP(float, g == 0 && okB ? sb + ((14 + rr) * 19 + eB) * 4 : dummy) = R3[rr];
        }
      }
    }
  }
  STAMP(p.dg, 4)
  wait_bar<2>();
  STAMP(p.dg, 5)

  const unsigned lane0 = lb + (8 * g + q) * 128u + 8u * pp + ((((q >> 1) & 1) | ((g & 1) << 1)) << 5);
  const unsigned abA = lane0 + L1_BUFA, abB = lane0 + L1_BUFB;
  const unsigned ks0 = 18u * xc + 4u * cr;
  const unsigned hs0 = (ks0 * 128u + T0) * 1024u;

  k1_first_reads<4096>(S, abB);
  const rsrc_t rsKV = mk_rsrc(p.kv, (unsigned)SZ_KV);
  if (tid < 88) {
    f32x4 a = *LDSP(f32x4, lb + L1_SCR + tid * 16);
#pragma unroll
    for (int ww = 1; ww < 8; ++ww) a += *LDSP(f32x4, lb + L1_SCR + ww * 1408u + tid * 16);
    bstore16_sc1<0>(__builtin_bit_cast(u32x4, a), tid * 16u, rsKV, (j * 8 + ib) * 1408u);
  }
  SB();
  const rsrc_t rsW = mk_rsrc(p.wd, F_ * D_ * 2), rsO = mk_rsrc(p.wo, D_ * D_ * 2);
  constexpr unsigned OOB = 0xf0000000u;
  const unsigned wdl = (576u * xc * 9u + r * 162u) * 128u;
  const unsigned win1 = (72u * ib + 18u * (w & 3) + l) * 9216u + wcol + 128u;
  G.wm0 = lb + L1_SCR + 15872u;
  if (WARM_MODE == 1) {
    G.wvo = l < 9 ? (72u * ib + 9u * w + l) * 9216u + wcol + 128u : OOB;
    G.wrs = rsG;
    G.wvo2 = w < 3 ? (l < 54 ? wdl + (w * 54u + l) * 128u : OOB)
           : w == 3 ? (l < 21 && r * 21 + l < 648 ? (72u * xc * 9u + r * 21u + l) * 128u : OOB) : (l < 18 ? win1 : OOB);
    G.wrs2 = w < 3 ? rsW : w == 3 ? rsO : rsU;
  } else {
    const unsigned sl = (18u * r + 5u * (w & 3) + l) * 9216u + (576u * xc + 512u) * 2u, su = (18u * r + 9u * (w & 1) + l) * 9216u + (576u * xc + 512u) * 2u;
    G.wvo = w < 4 ? (l < 5 && 5 * w + l < 18 ? sl : OOB) : w < 6 ? (l < 9 ? su : OOB) : (l < 27 ? wdl + (27u * (w - 6) + l) * 128u : OOB);
    G.wrs = w < 4 ? rsG : w < 6 ? rsU : rsW;
  }
  gu32* const ckv = ctr + (CT_KV + xc) * 32;
  const unsigned gvo0 = G.vo0, gvo1 = G.vo1;
  G.rs = rsU; G.soff = wsoff + wcol; G.dst = wdst + L1_BUFA;
  gu32* const chA = ctr + (CT_H + xc * 8 + ib) * 32;
  k1_step<1, 0, 4, 4096, 4096, 1, 1, 0>(S, abB, abA, G, rsH, hvoff, 0, 0, ckv, chA);
  STAMP(p.dg, 6)
  G.rs = (w >> 2) ? rsU : rsG;
  G.vo0 = G.vo1 = (l >> 2) * 9216u + (((l & 3) ^ ((l >> 5) << 1)) * 16u);
  G.sstep = 16u * 9216u;
  G.soff = 144u * (w & 3) * 9216u + (576u * xc + 512u + 32u * (cr >> 1)) * 2u;
  G.dst = lb + L1_BUFB + (w >> 2) * 36864u + 144u * (w & 3) * 64u;
  const unsigned abS = lb + L1_BUFB + (8 * g + q) * 64u + 8u * pp + (((cr & 1) ^ (g & 1)) << 5);
  if (WARM_MODE == 2) {
    G.wvo = w < 4 ? (l < 18 ? win1 : OOB) : (l < 14 && 14 * (w - 4) + l < 54 ? wdl + (54u + 14u * (w - 4) + l) * 128u : OOB);
    G.wrs = w < 4 ? rsG : rsW;
  }
  k1_step<2, 1, 4, 4096, 2048, 1, 0, 0>(S, abA, abS, G, rsH, hvoff, hs0, 0, ckv, chA);
  STAMP(p.dg, 8)
  G.rs = rsG; G.vo0 = gvo0; G.vo1 = gvo1; G.sstep = 8u * 9216u; G.soff = wsoff + wcol + 128u; G.dst = wdst + L1_BUFA;
  if (WARM_MODE == 2) { G.wvo = w < 4 && l < 18 ? win1 : OOB; G.wrs = rsU; }
  k1_step<0, 2, 2, 2048, 4096, 1, 0, 0>(S, abS, abA, G, rsH, hvoff, 0, hs0 + 131072u, ckv, chA);
  STAMP(p.dg, 10)
  G.rs = rsU; G.soff = wsoff + wcol + 128u; G.dst = wdst + L1_BUFB;
  if (WARM_MODE == 2) {
    G.wvo = w < 2 ? (l < 27 ? wdl + (108u + 27u * w + l) * 128u : OOB)
                  : (w == 2 && l < 21 && r * 21 + l < 648 ? (72u * xc * 9u + r * 21u + l) * 128u : OOB);
    G.wrs = w < 2 ? rsW : rsO;
  }
  k1_step<1, 3, 4, 4096, 4096, 1, 2, 0>(S, abA, abB, G, rsH, hvoff, 0,
                                        ((18u * xc + 16u + (cr >> 1)) * 128u + T0) * 1024u + (cr & 1) * 8u, ckv, chA);
  STAMP(p.dg, 12)
  const unsigned wsoF = 18u * xc * 36864u + cr * 288u;
  G.rs = w < 4 ? rsW : rsO;
  G.soff = w < 4 ? wsoF + (4 * (w >> 1) + (w & 1)) * 36864u : 18u * (4 * xc + w - 4) * 1152u + cr * 288u;
  G.dst = lb + L1_BUFA + w * L2_SLOT;
  G.rsk = rsKV;
  { const unsigned hh = tid / 88, q4 = tid - 88 * hh; G.vok = tid < 352 ? ((4 * xc + hh) * 8 * 352 + q4 * 4) * 4u : 0xf0000000u; }
  k1_step<2, 1, 4, 4096, 0, 2, 3, 0>(S, abB, 0, G, rsH, hvoff, hs0 + 2 * 131072u, 0, ckv, chA);

  K2S S2;
  K2C C;
  C.rsH = rsH; C.rsW = rsW; C.w = w; C.sp = xc; C.lb = lb;
  C.rsB = rsB;
  C.xso = xso;
  C.cxa = ctr + (CT_X + (ib * 4 + cr) * 8) * 32;
  C.chf = ctr + (CT_H2 + xc * 8 + ib) * 32;
  { const int ci = 64 * w + l, rho = ci / 18, c = ci - 18 * rho; C.vo0 = rho * 1152u + c * 16u; }
  { const int ci = 512 + l, rho = ci / 18, c = ci - 18 * rho; C.vo1 = rho * 1152u + c * 16u; }
  C.hvoff = hvoff;
  const unsigned hT = T0 * 1024u;
  C.hsoF = hT + 18u * xc * 131072u;
  C.hsoA = hT + (KS_FFN + 3u * xc) * 131072u;
  C.wsoF = wsoF;
  C.lanec = lb + (4 * g + q) * 288u + 8u * pp;

  u32x4 pqv[2];
  bload16_sc1<0>(pqv[0], hvoff, rsB, xso);
  bload16_sc1<1024>(pqv[1], hvoff, rsB, xso);
  k2_issue<0, 2>(S2, C); k2_issue<0, 3>(S2, C); k2_issue<1, 2>(S2, C); k2_issue<1, 3>(S2, C);
  k2_issue<2, 2>(S2, C); k2_issue<2, 3>(S2, C); k2_issue<3, 2>(S2, C); k2_issue<3, 3>(S2, C);
  k2_issue<4, 2>(S2, C); k2_issue<4, 3>(S2, C);
  asm volatile("s_waitcnt vmcnt(14)" : "+v"(S.kp[0]), "+v"(S.kp[1]), "+v"(S.kp[2]), "+v"(S.kp[3]), "+v"(S.kp[4]), "+v"(S.kp[5]), "+v"(S.kp[6]), "+v"(S.kp[7]));
  {
    f32x4 a = __builtin_bit_cast(f32x4, S.kp[0]);
#pragma unroll
    for (int i = 1; i < 8; ++i) a += __builtin_bit_cast(f32x4, S.kp[i]);
    if (tid < 352) *LDSP(f32x4, lb + L2_KV + tid * 16u) = a;
  }
  k1_epi_all<2, 3, 1>(S);
  k1_store_pair(S, rsH, hvoff, hs0 + 3 * 131072u);
  STAMP(p.dg, 14) STAMPR(p.dg, 27)
  asm volatile("s_waitcnt lgkmcnt(0)\n\ts_barrier" ::: "memory");
  STAMP(p.dg, 15)
  k2_issue<4, 0>(S2, C); k2_issue<4, 1>(S2, C);
  STAMP(p.dg, 16)
  {
    float za[4], zb[4];
#pragma unroll
    for (int e = 0; e < 4; ++e) {
      const int da = 4 * (g - 1) + e, db = da + 12;
      const float ta = *LDSP(float, lb + L2_KV + (cr * 352 + (g > 0 ? da * 19 + 18 : 0)) * 4);
      const float tb = *LDSP(float, lb + L2_KV + (cr * 352 + (g > 0 && db < 18 ? db * 19 + 18 : 0)) * 4);
      za[e] = g > 0 ? ta : 0.f;
      zb[e] = g > 0 && db < 18 ? tb : 0.f;
    }
    const int piA = 6 * cr + g - 1, piB = 6 * cr + g + 2;
    const unsigned base = (KS_FFN + 3u * xc) * 131072u + T0 * 1024u + c16 * 16u;
    const unsigned oA = g == 0 ? 0xf0000000u : base + (piA >> 3) * 131072u + (piA & 3) * 256u + ((piA >> 2) & 1) * 8u;
    const unsigned oB = g == 0 ? 0xf0000000u : base + (piB >> 3) * 131072u + (piB & 3) * 256u + ((piB >> 2) & 1) * 8u;
    asm volatile("s_waitcnt vmcnt(13)" : "+v"(pqv[0]), "+v"(pqv[1]));
#pragma unroll
    for (int T = 0; T < 2; ++T) {
      const u32x4 u = pqv[T];
      const float v[8] = {bf_lo(u[0]), bf_hi(u[0]), bf_lo(u[1]), bf_hi(u[1]), bf_lo(u[2]), bf_hi(u[2]), bf_lo(u[3]), bf_hi(u[3])};
      float den = 0.f;
#pragma unroll
      for (int e = 0; e < 4; ++e) den += v[e] * za[e] + v[4 + e] * zb[e];
      {
        const unsigned d0 = __float_as_uint(den);
        const auto r16 = __builtin_amdgcn_permlane16_swap(d0, d0, false, false);
        const unsigned r0 = r16[0], r1 = r16[1];
        const unsigned d1 = __float_as_uint(__uint_as_float(r0) + __uint_as_float(r1));
        const auto r32 = __builtin_amdgcn_permlane32_swap(d1, d1, false, false);
        const unsigned s0 = r32[0], s1 = r32[1];
        den = __uint_as_float(s0) + __uint_as_float(s1);
      }
      const float inv = frcp(den + 1e-6f);
      const u32x2 va = {pack2(v[0] * inv, v[1] * inv), pack2(v[2] * inv, v[3] * inv)};
      const u32x2 vb = {pack2(v[4] * inv, v[5] * inv), pack2(v[6] * inv, v[7] * inv)};
      __builtin_amdgcn_raw_buffer_store_b64(va, rsH, oA + T * 1024u, 0, AUX_SC1);
      __builtin_amdgcn_raw_buffer_store_b64(vb, rsH, oB + T * 1024u, 0, AUX_SC1);
    }
  }
  SB();
  STAMP(p.dg, 7)
  const f32x4 z4 = {0.f, 0.f, 0.f, 0.f};
  {
    const int hh = w >> 1, d = 16 * (w & 1) + c16;
    const unsigned ao = lb + L2_WO + hh * L2_SLOT + (8 * g + q) * 288u + 8u * pp;
    float v[8];
    const unsigned ka0 = lb + L2_KV + (hh * 352 + (d < 18 ? d : 0) * 19 + 8 * (g < 2 ? g : 2)) * 4;
#pragma unroll
    for (int e = 0; e < 8; ++e) v[e] = *LDSP(float, ka0 + e * 4);
    s16x8 A[9];
#pragma unroll
    for (int c = 0; c < 9; ++c) A[c] = trfrag(ao + c * 32, ao + c * 32 + 4 * 288);
    unsigned bh[4], bl[4];
#pragma unroll
    for (int e2 = 0; e2 < 4; ++e2) {
      const float v0 = (d < 18 && 8 * g + 2 * e2 < 18) ? v[2 * e2] : 0.f, v1 = (d < 18 && 8 * g + 2 * e2 + 1 < 18) ? v[2 * e2 + 1] : 0.f;
      bh[e2] = pack2(v0, v1);
      bl[e2] = pack2(v0 - bf_lo(bh[e2]), v1 - bf_hi(bh[e2]));
    }
    const s16x8 Bh = mk_frag(bh[0], bh[1], bh[2], bh[3]), Bl = mk_frag(bl[0], bl[1], bl[2], bl[3]);
    const int ka = 24 * hh + d;
    const unsigned mo = d < 24 ? lb + L2_MT + (ka >> 5) * L2_SLOT + (ka & 31) * 288u + 8u * g : lb + k2_slot(L2_NS - 1) + l * 8u;
    f32x4 D[9];
#pragma unroll
    for (int c = 0; c < 9; ++c) D[c] = mfma16(A[c], Bh, z4);
#pragma unroll
    for (int c = 0; c < 9; ++c) D[c] = mfma16(A[c], Bl, D[c]);
#pragma unroll
    for (int c = 0; c < 9; ++c) *LDSP(u32x2, mo + c * 32) = u32x2{pack2(D[c][0], D[c][1]), pack2(D[c][2], D[c][3])};
  }
  SB();
  STAMP(p.dg, 9)
  asm volatile("s_waitcnt lgkmcnt(0)\n\ts_barrier" ::: "memory");
  STAMP(p.dg, 17)
  asm volatile("s_waitcnt vmcnt(15)" : "+v"(S2.hr[0][0]), "+v"(S2.hr[0][1]));
  k2_read<0, 0>(S2, C); k2_read<0, 1>(S2, C); k2_read<1, 0>(S2, C); k2_read<1, 1>(S2, C); k2_read<2, 0>(S2, C);
  k2_read<2, 1>(S2, C); k2_read<3, 0>(S2, C); k2_read<3, 1>(S2, C); k2_read<4, 0>(S2, C); k2_read<4, 1>(S2, C);
  SB();
  k2_step<0>(S2, C);
  STAMP(p.dg, 23) STAMPR(p.dg, 28)

  const int sp = xc, ni = cr, mi = ib;
  const int grp = mi * 4 + ni;
  k2_send<8>(S2, C);
  const int tok0 = 256 * mi + 32 * sp;
  const rsrc_t rsXr = mk_rsrc(p.x, S_ * D_ * 2);
  u32x4 xr[2];
  unsigned xo[2];
#pragma unroll
  for (int i = 0; i < 2; ++i) {
    const int it = tid + 512 * i, tk = it / 18, ch = it - tk * 18;
    xo[i] = it < 576 ? ((tok0 + tk) * D_ + 144 * ni + 8 * ch) * 2u : 0xf0000000u;
  }
  bload16<0>(xr[0], xo[0], rsXr, 0);
  bload16<0>(xr[1], xo[1], rsXr, 0);
  if (w == sp) ctr_wait(C.cxa + sp * 32, 8u);
  asm volatile("s_waitcnt lgkmcnt(0)\n\ts_barrier" ::: "memory");
  STAMP(p.dg, 24)
  u32x4 v[8];
  auto fetch = [&](int c) {
#pragma unroll
    for (int src = 0; src < 8; ++src)
      bload16_sc1<0>(v[src], src == sp ? 0xf0000000u : hvoff + c * 1024u, rsB, ((grp * 8 + sp) * 8 + src) * 9216u);
  };
  auto reduce = [&](int c) {
    asm volatile("s_waitcnt vmcnt(0)" : "+v"(v[0]), "+v"(v[1]), "+v"(v[2]), "+v"(v[3]), "+v"(v[4]), "+v"(v[5]), "+v"(v[6]), "+v"(v[7]));
    f32x4 a0 = *LDSP(f32x4, lb + L2_OWN + (2 * c) * 1024u + l * 16u), a1 = *LDSP(f32x4, lb + L2_OWN + (2 * c + 1) * 1024u + l * 16u);
#pragma unroll
    for (int src = 0; src < 8; ++src) {
      a0 += f32x4{bf_lo(v[src][0]), bf_hi(v[src][0]), bf_lo(v[src][1]), bf_hi(v[src][1])};
      a1 += f32x4{bf_lo(v[src][2]), bf_hi(v[src][2]), bf_lo(v[src][3]), bf_hi(v[src][3])};
    }
    *LDSP(f32x4, lb + L2_OUT + (c16 * 148 + 16 * c + 4 * g) * 4) = a0;
    *LDSP(f32x4, lb + L2_OUT + ((16 + c16) * 148 + 16 * c + 4 * g) * 4) = a1;
  };
  if (w < 5) {
    fetch(w);
    asm volatile("s_waitcnt vmcnt(8)\n\ts_barrier" ::: "memory");
  } else {
    asm volatile("s_waitcnt vmcnt(0)\n\ts_barrier" ::: "memory");
  }
  gu32* const cxb = ctr + (CT_X2 + grp * 8) * 32;
  if (w == 7) {
    if (l < 8) ctr_add(cxb + l * 32);
    ctr_wait(cxb + sp * 32, 8u);
  }
  if (w < 5) reduce(w);
  asm volatile("s_waitcnt vmcnt(0) lgkmcnt(0)\n\ts_barrier" ::: "memory");
  STAMP(p.dg, 25) STAMPR(p.dg, 29)
  if (w >= 4) { fetch(w + 1); reduce(w + 1); }
  asm volatile("s_waitcnt vmcnt(0)" : "+v"(xr[0]), "+v"(xr[1]));
  __syncthreads();
#pragma unroll
  for (int i = 0; i < 2; ++i) {
    const int it = tid + 512 * i;
    if (it < 576) {
      const int tk = it / 18, ch = it - tk * 18;
      const f32x4 a = *LDSP(f32x4, lb + L2_OUT + (tk * 148 + 8 * ch) * 4), c = *LDSP(f32x4, lb + L2_OUT + (tk * 148 + 8 * ch + 4) * 4);
      u32x4 ov;
      ov[0] = pack2(a[0] + bf_lo(xr[i][0]), a[1] + bf_hi(xr[i][0]));
      ov[1] = pack2(a[2] + bf_lo(xr[i][1]), a[3] + bf_hi(xr[i][1]));
      ov[2] = pack2(c[0] + bf_lo(xr[i][2]), c[1] + bf_hi(xr[i][2]));
      ov[3] = pack2(c[2] + bf_lo(xr[i][3]), c[3] + bf_hi(xr[i][3]));
      *(u32x4*)((char*)p.out + xo[i]) = ov;
    }
  }
  STAMP(p.dg, 26) STAMPR(p.dg, 31) STAMP_DUMP(p.dg)
  asm volatile("98765:");
}

extern "C" void kernel_launch(void* const* d_in, const int* in_sizes, int n_in, void* d_out, int out_size,
                              void* d_ws, size_t ws_size, hipStream_t stream) {
  const uint16_t* x = (const uint16_t*)d_in[0];
  const void* wqkv = d_in[1];
  const uint16_t* wo = (const uint16_t*)d_in[2];
  const void* wg = d_in[4];
  const void* wu = d_in[5];
  const void* wd = d_in[6];
  char* ws = (char*)d_ws + (WS_TOP ? ((ws_size - (OFF_CT + SZ_CT)) & ~(size_t)0x1fffff) : 0);

  k0_ln<<<256, 512, 0, stream>>>(x, (u32x4*)(ws + OFF_XF), (const uint16_t*)wqkv, (u32x4*)(ws + OFF_WQ),
                                 (unsigned*)(ws + OFF_CT));
  KFP p;
  p.xf = ws + OFF_XF; p.wq = ws + OFF_WQ; p.wg = wg; p.wu = wu; p.wd = wd; p.wo = wo; p.x = x;
  p.hf = ws + OFF_HF; p.kv = ws + OFF_KV; p.xb = ws + OFF_XB; p.ctr = ws + OFF_CT; p.out = d_out;
  kf_main<<<256, 512, 0, stream>>>(p);
}
```

```cpp
#include <hip/hip_runtime.h>
#include <hip/hip_bf16.h>
#include <stdint.h>

#define DI __device__ __forceinline__
typedef __attribute__((ext_vector_type(8))) short s16x8;
typedef __attribute__((ext_vector_type(4))) short s16x4;
typedef __attribute__((ext_vector_type(4))) float f32x4;
typedef __attribute__((ext_vector_type(4))) unsigned u32x4;
typedef __attribute__((ext_vector_type(2))) unsigned u32x2;
typedef __amdgpu_buffer_rsrc_t rsrc_t;
#define LDSP(T, a) ((__attribute__((address_space(3))) T*)(size_t)(a))

constexpr int S_ = 2048, D_ = 576, F_ = 4608, NH = 32, DH = 18;
constexpr int KS_FFN = 144, KS_ALL = 168;
constexpr size_t OFF_XF = 0;
constexpr size_t SZ_XF = (size_t)128 * 18 * 1024;
constexpr size_t OFF_WQ = OFF_XF + SZ_XF;
constexpr size_t SZ_WQ = (size_t)32 * 72 * 1024;
constexpr size_t OFF_HF = OFF_WQ + SZ_WQ;
constexpr size_t SZ_HF = (size_t)KS_ALL * 128 * 1024;
constexpr size_t OFF_KV = OFF_HF + SZ_HF;
constexpr size_t SZ_KV = (size_t)32 * 8 * 352 * 4;
constexpr size_t OFF_XB = OFF_KV + SZ_KV;
constexpr size_t SZ_XB = (size_t)32 * 8 * 8 * 9 * 1024;
constexpr size_t OFF_CT = OFF_XB + SZ_XB;
constexpr int CT_X = 0, CT_X2 = 256, CT_KV = 512, CT_H = 520, CT_H2 = 584, CT_N = 648;
constexpr size_t SZ_CT = (size_t)CT_N * 128;

DI rsrc_t mk_rsrc(const void* p, unsigned bytes) {
  return __builtin_amdgcn_make_buffer_rsrc((void*)p, (short)0, (int)bytes, 0x00020000);
}
DI unsigned rfl(unsigned v) { return __builtin_amdgcn_readfirstlane(v); }
DI float bf_lo(unsigned u) { return __uint_as_float(u << 16); }
DI float bf_hi(unsigned u) { return __uint_as_float(u & 0xffff0000u); }
DI unsigned pack2(float lo, float hi) {
  typedef __attribute__((ext_vector_type(2))) __bf16 bf2;
  typedef __attribute__((ext_vector_type(2))) float f2;
  f2 v = {lo, hi};
  bf2 r = __builtin_convertvector(v, bf2);
  return __builtin_bit_cast(unsigned, r);
}
DI s16x8 mk_frag(unsigned a, unsigned b, unsigned c, unsigned d) {
  u32x4 v = {a, b, c, d};
  return __builtin_bit_cast(s16x8, v);
}
DI f32x4 mfma16(s16x8 a, s16x8 b, f32x4 c) { return __builtin_amdgcn_mfma_f32_16x16x32_bf16(a, b, c, 0, 0, 0); }
DI s16x4 trread(unsigned addr) { return __builtin_amdgcn_ds_read_tr16_b64_v4i16(LDSP(s16x4, addr)); }
DI s16x8 trfrag(unsigned a0, unsigned a1) {
  s16x4 lo = trread(a0), hi = trread(a1);
  return __builtin_shufflevector(lo, hi, 0, 1, 2, 3, 4, 5, 6, 7);
}
template <int ROLE = 0> DI void glds16(unsigned m0v, unsigned voff, rsrc_t rs, unsigned soff) {
  asm volatile("s_mov_b32 m0, %0\n\ts_nop 0\n\tbuffer_load_dwordx4 %1, %2, %3 offen lds ; role %4"
               :: "s"(m0v), "v"(voff), "s"(rs), "s"(soff), "i"(ROLE) : "memory");
}
DI void warm_lines(unsigned m0v, unsigned voff, rsrc_t rs) {
  asm volatile("s_mov_b32 m0, %0\n\ts_nop 0\n\tbuffer_load_dword %1, %2, 0 offen lds" :: "s"(m0v), "v"(voff), "s"(rs) : "memory");
}
template <int IMM> DI void bload16(u32x4& d, unsigned voff, rsrc_t rs, unsigned soff) {
  asm volatile("buffer_load_dwordx4 %0, %1, %2, %3 offen offset:%4"
               : "=v"(d) : "v"(voff), "s"(rs), "s"(soff), "i"(IMM));
}
template <int IMM, int ROLE = 0> DI void bload16_sc1(u32x4& d, unsigned voff, rsrc_t rs, unsigned soff) {
  asm volatile("buffer_load_dwordx4 %0, %1, %2, %3 offen offset:%4 sc1 ; role %5"
               : "=v"(d) : "v"(voff), "s"(rs), "s"(soff), "i"(IMM), "i"(ROLE));
}
template <int IMM> DI void bstore16_sc1(u32x4 v, unsigned voff, rsrc_t rs, unsigned soff) {
  asm volatile("buffer_store_dwordx4 %0, %1, %2, %3 offen offset:%4 sc1\n\ts_nop 1"
               :: "v"(v), "v"(voff), "s"(rs), "s"(soff), "i"(IMM) : "memory");
}
typedef __attribute__((address_space(1))) unsigned gu32;
DI void ctr_add(gu32* c) { __hip_atomic_fetch_add(c, 1u, __ATOMIC_RELAXED, __HIP_MEMORY_SCOPE_AGENT); }
DI void ctr_wait(gu32* c, unsigned n) {
  for (unsigned spins = 0; spins < (1u << 20); ++spins) {
    if ((unsigned)__builtin_amdgcn_readfirstlane(__hip_atomic_load(c, __ATOMIC_RELAXED, __HIP_MEMORY_SCOPE_AGENT)) >= n) break;
    __builtin_amdgcn_s_sleep(1);
  }
}
template <int N> DI void wait_vm2(u32x4& a, u32x4& b) {
  asm volatile("s_waitcnt vmcnt(%2)" : "+v"(a), "+v"(b) : "i"(N));
}
template <int N> DI void wait_bar_nolgkm() {
  asm volatile("s_waitcnt vmcnt(%0)\n\ts_barrier" :: "i"(N) : "memory");
}
template <int N> DI void wait_bar() {
  asm volatile("s_waitcnt vmcnt(%0) lgkmcnt(0)\n\ts_barrier" :: "i"(N) : "memory");
}
DI float fexp2(float x) { return __builtin_amdgcn_exp2f(x); }
DI float frcp(float x) { return __builtin_amdgcn_rcpf(x); }
DI float phi_f(float t) {
  return (fmaxf(t, 0.f) + __builtin_amdgcn_fmed3f(fexp2(t * 1.4426950408889634f), 0.f, 1.f)) + 1e-6f;
}
DI float geglu(float g, float u) {
  float e = fexp2(g * __builtin_fmaf(g * g, -0.10294323970f, -2.30220819814f));
  return (g * u) * frcp(1.f + e);
}
template <int CTRL> DI float dpp_add(float v) {
  return v + __builtin_bit_cast(float, __builtin_amdgcn_update_dpp(0, __builtin_bit_cast(int, v), CTRL, 0xf, 0xf, true));
}
DI float reduce32(float v) {
  v = dpp_add<0xB1>(v);
  v = dpp_add<0x4E>(v);
  v = dpp_add<0x141>(v);
  v = dpp_add<0x140>(v);
  const unsigned u = __float_as_uint(v);
  const auto r = __builtin_amdgcn_permlane16_swap(u, u, false, false);
  const unsigned r0 = r[0], r1 = r[1];
  return __uint_as_float(r0) + __uint_as_float(r1);
}

#define ABL2_NOW 0
#define ABL2_NOH 0
#define K0_NORETILE 0
#define K0_NOLN 0
#define STAGGER 0
#define WS_TOP 0
#define NO_CODEWARM0 0
#define NO_CODEWARM 0
#define NO_WARM0 1
#define WARM_N 33
#define WARM_MODE 2
#define ABL_NOW 0
#define ABL_NOH 0
#define ABL_NOCOMP 0
#define ABL1_NOCOMP 0
#define ABL1_NOEPI 0
#define ABL1_NOSTAGE 0
#define STAMP(buf, slot)
#define STAMPR(buf, slot)
#define STAMP_DUMP(buf)
__global__ __launch_bounds__(512, 2) void k0_ln(const uint16_t* __restrict__ x, u32x4* __restrict__ xf,
                                                const uint16_t* __restrict__ wqkv, u32x4* __restrict__ wq,
                                                unsigned* __restrict__ ctr) {
  __shared__ u32x4 stage[4][288];
  const int l = threadIdx.x & 63, w = threadIdx.x >> 6;
  if (threadIdx.x < CT_N / 8) ctr[blockIdx.x * (CT_N / 8) + threadIdx.x] = 0u;
  if (w == 7 && !NO_CODEWARM0) {
    unsigned long long pc;
    unsigned off, len;
    asm volatile("s_getpc_b64 %0\n98763:\n\ts_mov_b32 %1, 98764f-98763b\n\ts_mov_b32 %2, 98765f-98764f" : "=s"(pc), "=s"(off), "=s"(len));
    const unsigned v = __builtin_amdgcn_raw_buffer_load_b32(mk_rsrc((const char*)pc + off, len), l < 2 ? (blockIdx.x * 2 + l) * 128u : 0xf0000000u, 0, 0);
    asm volatile("" :: "v"(v));
    return;
  }
  if (w >= 4) {
    const int item = blockIdx.x + 256 * (w - 4);
    if (item >= 576 || K0_NORETILE) return;
    const int h = item / 18, s = item - 18 * h, g = l >> 4, c = l & 15;
    typedef u32x4 __attribute__((aligned(4))) u32x4_a4;
    u32x4* st = stage[w - 4];
    u32x4 t[5];
#pragma unroll
    for (int i = 0; i < 5; ++i) {
      const int ci = 64 * i + l < 288 ? 64 * i + l : 0, row = ci / 9, c9 = ci - 9 * row, sec = c9 / 3, cc = c9 - 3 * sec;
      const int col = (sec == 0 ? 576 : (sec == 1 ? 0 : 1152)) + 18 * h + (cc == 2 ? 10 : 8 * cc);
      t[i] = *(const u32x4_a4*)(wqkv + (size_t)(32 * s + row) * (3 * D_) + col);
    }
    asm volatile("" : "+v"(t[0]), "+v"(t[1]), "+v"(t[2]), "+v"(t[3]), "+v"(t[4]));
#pragma unroll
    for (int i = 0; i < 5; ++i)
      if (64 * i + l < 288) st[64 * i + l] = t[i];
    __builtin_amdgcn_fence(__ATOMIC_RELEASE, "wavefront");
    __builtin_amdgcn_wave_barrier();
    const int sec[4] = {0, c < 4 ? 0 : 48, 96, c < 4 ? 96 : 48};
    const int dim[4] = {c, c < 4 ? 14 + c : c - 4, c, c < 4 ? 14 + c : (c < 8 ? c + 8 : 14 + (c & 3))};
    unsigned short v[4][8];
#pragma unroll
    for (int X = 0; X < 4; ++X) {
      const uint16_t* src = (const uint16_t*)((const char*)st + 8 * g * 144 + sec[X] + 2 * dim[X] + (dim[X] >= 16 ? 12 : 0));
#pragma unroll
      for (int e = 0; e < 8; ++e) v[X][e] = src[e * 72];
    }
#pragma unroll
    for (int X = 0; X < 4; ++X) {
      u32x4 o;
#pragma unroll
      for (int e = 0; e < 4; ++e) o[e] = (unsigned)v[X][2 * e] | ((unsigned)v[X][2 * e + 1] << 16);
      wq[((size_t)item * 4 + X) * 64 + l] = o;
    }
    return;
  }
  if (K0_NOLN) return;
  const int tok = blockIdx.x * 8 + w * 2 + (l >> 5), p = l & 31;
  const u32x4* row = (const u32x4*)(x + (size_t)tok * D_);
  u32x4 c[3];
  c[0] = row[p];
  c[1] = row[p + 32];
  c[2] = row[p < 8 ? p + 64 : p];
  const bool has2 = p < 8;
  float v[24];
#pragma unroll
  for (int i = 0; i < 3; ++i)
#pragma unroll
    for (int j = 0; j < 4; ++j) { v[i * 8 + 2 * j] = bf_lo(c[i][j]); v[i * 8 + 2 * j + 1] = bf_hi(c[i][j]); }
  float s = 0.f, s2 = 0.f;
#pragma unroll
  for (int j = 0; j < 16; ++j) s += v[j];
#pragma unroll
  for (int j = 16; j < 24; ++j) s2 += v[j];
  s += has2 ? s2 : 0.f;
  const float mean = reduce32(s) * (1.f / 576.f);
  float q = 0.f, q2 = 0.f;
#pragma unroll
  for (int j = 0; j < 24; ++j) v[j] -= mean;
#pragma unroll
  for (int j = 0; j < 16; ++j) q += v[j] * v[j];
#pragma unroll
  for (int j = 16; j < 24; ++j) q2 += v[j] * v[j];
  q += has2 ? q2 : 0.f;
  const float var = reduce32(q) * (1.f / 576.f);
  const float rstd = 1.f / sqrtf(var + 1e-5f);
  const int T = tok >> 4, t16 = tok & 15;
#pragma unroll
  for (int i = 0; i < 3; ++i) {
    u32x4 o;
#pragma unroll
    for (int j = 0; j < 4; ++j) o[j] = pack2(v[i * 8 + 2 * j] * rstd, v[i * 8 + 2 * j + 1] * rstd);
    const int ch = p + 32 * i;
    if (i < 2 || has2) xf[((size_t)T * 18 + (ch >> 2)) * 64 + (ch & 3) * 16 + t16] = o;
  }
}

constexpr unsigned L1_BUFA = 0, L1_BUFB = 73728, L1_SCR = 147456, L1_TOTAL = 163840;
constexpr int AUX_SC1 = 16;
#define SB() __builtin_amdgcn_sched_barrier(0)
#define PIN(x) asm volatile("" : "+v"(x))

struct KFP {
  const void* xf; const void* wq; const void* wg; const void* wu; const void* wd; const void* wo; const void* x;
  void* hf; void* kv; void* xb; void* ctr; void* out;
};

struct K1S {
  u32x4 xf[2][18];
  s16x4 wlo[6], whi[6];
  f32x4 acc[2][2];
  unsigned gel[4][2][2];
  u32x4 hq[2];
  float t0, t1;
  unsigned ard;
  u32x4 kp[8];
  unsigned flag;
};
struct K1G { unsigned dst, soff, sstep, vo0, vo1, vok, wm0, wvo, wvo2; rsrc_t rs, rsk, wrs, wrs2; int w; };
template <int I, int ROLE = 0> DI void k1_piece(const K1G& G) {
  if (!ABL1_NOSTAGE) glds16<ROLE>(G.dst + I * 1024u, (I & 1) ? G.vo1 : G.vo0, G.rs, G.soff + I * G.sstep);
}
template <int I, int ROLE = 0> DI void k1_piece2(const K1G& G, int l) {
  const int ci = 64 * I + l, rho = ci / 18, c = ci - 18 * rho;
  glds16<ROLE>(G.dst + I * 1024u, rho * 1152u + c * 16u, G.rs, G.soff);
}
template <int STRIDE, int N, int H> DI void k1_read(K1S& S, unsigned a0, int slot_unused = 0) {
  constexpr int P = N / 18, KS = N % 18;
  if constexpr (H == 0 && KS == 0) { S.ard = STRIDE == 4096 ? (a0 ^ (P << 5)) : a0 + P * 36864u; PIN(S.ard); }
  if constexpr (H == 0 && KS == 9) { S.ard += 9 * STRIDE; PIN(S.ard); }
  if constexpr (H == 0) S.wlo[N % 6] = trread(S.ard + (KS % 9) * STRIDE);
  else S.whi[N % 6] = trread(S.ard + (KS % 9) * STRIDE + STRIDE / 8);
}
template <int KIND, int U, int A, int PR, int PH, int SL> DI void k1_epi(K1S& S) {
  constexpr int T = PR >> 1, R = 2 * (PR & 1);
  const float a = S.acc[A][T][R], b = S.acc[A][T][R + 1];
  if (ABL1_NOEPI) {
    if (PH == 3 && SL == 1) { if (KIND == 1) S.gel[U][T][PR & 1] = pack2(a, b); else S.hq[T][2 * (U & 1) + (PR & 1)] = pack2(a, b) ^ S.gel[U][T][PR & 1]; }
    return;
  }
  if constexpr (KIND == 1) {
    if constexpr (PH == 0 && SL == 0) { S.t0 = a * a; S.t1 = b * b; }
    if constexpr (PH == 0 && SL == 1) {
      S.t0 = __builtin_fmaf(S.t0, -0.10294323970f, -2.30220819814f);
      S.t1 = __builtin_fmaf(S.t1, -0.10294323970f, -2.30220819814f);
    }
    if constexpr (PH == 1 && SL == 0) { S.t0 *= a; S.t1 *= b; }
    if constexpr (PH == 1 && SL == 1) { S.t0 = fexp2(S.t0); S.t1 = fexp2(S.t1); }
    if constexpr (PH == 2 && SL == 0) { S.t0 += 1.f; S.t1 += 1.f; }
    if constexpr (PH == 2 && SL == 1) { S.t0 = frcp(S.t0); S.t1 = frcp(S.t1); }
    if constexpr (PH == 3 && SL == 0) { S.t0 *= a; S.t1 *= b; }
    if constexpr (PH == 3 && SL == 1) { unsigned v = pack2(S.t0, S.t1); PIN(v); S.gel[U][T][PR & 1] = v; }
    else { PIN(S.t0); PIN(S.t1); }
  } else {
    if constexpr (PH == 1 && SL == 0) { const unsigned u = S.gel[U][T][PR & 1]; S.t0 = bf_lo(u); S.t1 = bf_hi(u); PIN(S.t0); PIN(S.t1); }
    if constexpr (PH == 2 && SL == 0) { S.t0 *= a; S.t1 *= b; PIN(S.t0); PIN(S.t1); }
    if constexpr (PH == 3 && SL == 0) { unsigned v = pack2(S.t0, S.t1); PIN(v); S.hq[T][2 * (U & 1) + (PR & 1)] = v; }
  }
}
template <int KIND, int U, int A, int I = 0> DI void k1_epi_all(K1S& S) {
  if constexpr (I < 32) { k1_epi<KIND, U, A, I / 8, (I / 2) % 4, I % 2>(S); k1_epi_all<KIND, U, A, I + 1>(S); }
}
DI void k1_store_pair(K1S& S, rsrc_t rsH, unsigned voff, unsigned soff) {
  bstore16_sc1<0>(S.hq[0], voff, rsH, soff);
  bstore16_sc1<1024>(S.hq[1], voff, rsH, soff);
}
template <int KIND, int PREV, int NP, int STRIDE, int NSTRIDE, int STG, int SIG, int N>
DI void k1_step(K1S& S, unsigned ab, unsigned abn, const K1G& G, rsrc_t rsH, unsigned hvoff, unsigned hso, unsigned hsop,
                gu32* sig, gu32* sig2) {
  constexpr int P = N / 18, KS = N % 18, A = P & 1, NT = NP * 18;
  constexpr int EK = P == 0 ? (PREV == 3 ? 2 : PREV) : (NP == 2 ? 1 : KIND);
  constexpr int EU = P == 0 ? (PREV == 3 ? 0 : 3) : (NP == 2 ? 0 : P - 1);
  constexpr int EA = P == 0 ? 1 : (A ^ 1);
  constexpr bool EPI = EK != 0 && KS >= 1 && KS <= 16;
  const f32x4 z4 = {0.f, 0.f, 0.f, 0.f};
  const s16x8 wf = __builtin_shufflevector(S.wlo[N % 6], S.whi[N % 6], 0, 1, 2, 3, 4, 5, 6, 7);
  const s16x8 x0 = __builtin_bit_cast(s16x8, S.xf[0][KS]), x1 = __builtin_bit_cast(s16x8, S.xf[1][KS]);
  if (!ABL1_NOCOMP) S.acc[A][0] = mfma16(wf, x0, KS == 0 ? z4 : S.acc[A][0]);
  else if (KS == 0) { S.acc[A][0] = z4; S.acc[A][0][0] = (float)wf[0]; }
  SB();
  if constexpr (N + 5 < NT) k1_read<STRIDE, N + 5, 0>(S, ab);
  else if constexpr (NSTRIDE != 0) k1_read<NSTRIDE == 0 ? 4096 : NSTRIDE, N + 5 - NT, 0>(S, abn);
  if constexpr (EPI) k1_epi<EK, EU, EA, (KS - 1) / 4, (KS - 1) % 4, 0>(S);
  SB();
  if (!ABL1_NOCOMP) S.acc[A][1] = mfma16(wf, x1, KS == 0 ? z4 : S.acc[A][1]);
  else if (KS == 0) { S.acc[A][1] = z4; }
  SB();
  if constexpr (N + 5 < NT) k1_read<STRIDE, N + 5, 1>(S, ab);
  else if constexpr (NSTRIDE != 0) k1_read<NSTRIDE == 0 ? 4096 : NSTRIDE, N + 5 - NT, 1>(S, abn);
  if constexpr (EPI) k1_epi<EK, EU, EA, (KS - 1) / 4, (KS - 1) % 4, 1>(S);
  constexpr int PS = WARM_MODE == 2 ? 3 : 6;
  if constexpr (STG == 1 && NP == 4 && N % PS == 2 && N / PS < 9) k1_piece<N / PS>(G);
  if constexpr (STG == 1 && NP == 2 && N % 2 == 0 && N >= 2 && N / 2 <= 9) k1_piece<N / 2 - 1>(G);
  if constexpr (WARM_MODE == 2 && STG == 1 && N == (NP == 4 ? WARM_N : 22)) warm_lines(G.wm0, G.wvo, G.wrs);
  if constexpr (WARM_MODE == 1 && SIG == 1 && N == NT - 7) warm_lines(G.wm0, G.wvo, G.wrs);
  if constexpr (WARM_MODE == 1 && SIG == 1 && N == NT - 5) warm_lines(G.wm0, G.wvo2, G.wrs2);
  if constexpr (STG == 2 && N % 6 == 2 && N / 6 < 9) k1_piece2<N / 6>(G, hvoff >> 4);
  if constexpr (STG == 2 && N % 6 == 5 && N / 6 < 8) bload16_sc1<0>(S.kp[N / 6], G.vok, G.rsk, (N / 6) * 1408u);
  if constexpr (PREV == 2 && N == 17) k1_store_pair(S, rsH, hvoff, hsop);
  if constexpr (PREV == 3 && N == 17) {
#pragma unroll
    for (int T = 0; T < 2; ++T) {
      const u32x2 v = {S.hq[T][0], S.hq[T][1]};
      __builtin_amdgcn_raw_buffer_store_b64(v, rsH, hvoff + T * 1024u, hsop, AUX_SC1);
    }
  }
  if constexpr (KIND == 2 && NP == 4 && N == 2 * 18 + 17) k1_store_pair(S, rsH, hvoff, hso);
  if constexpr (N == 1) asm volatile("s_barrier" ::: "memory");
  if constexpr (SIG == 2 && N == NT - 8) { if (rfl(threadIdx.x >> 6) == 0) ctr_wait(sig, 32u); }
  if constexpr (SIG == 3 && N == 30) { if (G.w == 0) asm volatile("global_load_dword %0, %1, off sc1 ; role 3" : "=v"(S.flag) : "v"(sig2)); }
  if constexpr (SIG == 3 && N == NT - 8) {
    if (G.w == 0) {
      asm volatile("s_waitcnt vmcnt(9)" : "+v"(S.flag));
      if (rfl(S.flag) < 4u) ctr_wait(sig2, 4u);
    }
    asm volatile("s_barrier" ::: "memory");
  }
  if constexpr (NSTRIDE != 0 && N == NT - 8) wait_bar_nolgkm<(KIND == 2 ? 2 : 0) + (WARM_MODE == 2 ? 1 : 0)>();
  if constexpr (SIG == 1 && N == NT - 8) { if (threadIdx.x == 0) ctr_add(sig); }
  if constexpr (SIG == 2 && N == NT - 8) { if (threadIdx.x == 0) ctr_add(sig2); }
  SB();
  if constexpr (N + 1 < NT) k1_step<KIND, PREV, NP, STRIDE, NSTRIDE, STG, SIG, N + 1>(S, ab, abn, G, rsH, hvoff, hso, hsop, sig, sig2);
}
template <int STRIDE> DI void k1_first_reads(K1S& S, unsigned ab) {
  k1_read<STRIDE, 0, 0>(S, ab); k1_read<STRIDE, 0, 1>(S, ab); k1_read<STRIDE, 1, 0>(S, ab); k1_read<STRIDE, 1, 1>(S, ab);
  k1_read<STRIDE, 2, 0>(S, ab); k1_read<STRIDE, 2, 1>(S, ab); k1_read<STRIDE, 3, 0>(S, ab); k1_read<STRIDE, 3, 1>(S, ab);
  k1_read<STRIDE, 4, 0>(S, ab); k1_read<STRIDE, 4, 1>(S, ab);
  SB();
}
template <int KS> DI void k1_qkv_step(K1S& S, s16x8 (&wq)[2][4], f32x4 (&Q)[4][2], unsigned qa, unsigned xv, rsrc_t rsX,
                                      unsigned xsb, const K1G& G) {
  if constexpr (KS + 6 < 18) {
    bload16<((KS + 6) & 3) * 1024>(S.xf[0][KS + 6], xv, rsX, xsb + ((KS + 6) >> 2) * 4096u);
    bload16<((KS + 6) & 3) * 1024>(S.xf[1][KS + 6], xv, rsX, xsb + 18432u + ((KS + 6) >> 2) * 4096u);
  } else if constexpr (KS < 15) { k1_piece<2 * (KS - 12)>(G); k1_piece<2 * (KS - 12) + 1>(G); }
  else k1_piece<KS - 9>(G);
  constexpr int YOUNGER = (ABL1_NOSTAGE ? (KS < 12 ? 12 : 2 * (17 - KS)) : (KS < 15 ? 12 : 26 - KS)) + (KS < 6 && !NO_WARM0 ? 2 : 0);
  wait_vm2<YOUNGER>(S.xf[0][KS], S.xf[1][KS]);
  const s16x8 x0 = __builtin_bit_cast(s16x8, S.xf[0][KS]), x1 = __builtin_bit_cast(s16x8, S.xf[1][KS]);
  SB();
#pragma unroll
  for (int X = 0; X < 4; ++X) {
    Q[X][0] = mfma16(x0, wq[KS & 1][X], Q[X][0]);
    SB();
    if constexpr (KS + 1 < 18) wq[(KS + 1) & 1][X] = *LDSP(s16x8, qa + ((KS + 1) * 4 + X) * 1024u);
    SB();
    Q[X][1] = mfma16(x1, wq[KS & 1][X], Q[X][1]);
    SB();
  }
  if constexpr (KS + 1 < 18) k1_qkv_step<KS + 1>(S, wq, Q, qa, xv, rsX, xsb, G);
}

constexpr int K2D = 5;
constexpr unsigned L2_SLOT = 9216, L2_NS = K2D + 2;
constexpr unsigned L2_WO = L1_BUFA + 4 * L2_SLOT;
constexpr unsigned L2_MT = L1_BUFB + 3 * L2_SLOT, L2_KV = L1_SCR;
constexpr unsigned L2_OWN = L1_BUFB, L2_OUT = L1_BUFA;

struct K2S {
  u32x4 hr[K2D + 1][2];
  u32x4 ha[3][2];
  s16x4 wlo[6], whi[6];
  f32x4 acc[9][2];
  unsigned flag, ardm;
  unsigned ard;
};
struct K2C {
  rsrc_t rsH, rsW, rsB;
  unsigned vo0, vo1, hvoff, hsoF, hsoA, wsoF, xso, lb, lanec;
  int w, sp;
  gu32* chf;
  gu32* cxa;
};
constexpr bool k2_att(int n) { return n >= 18; }
constexpr unsigned k2_loc(int n) {
  return n < 8 ? 4 * (n >> 1) + (n & 1) : n < 10 ? 8 + n : 4 * ((n - 10) >> 1) + 2 + (n & 1);
}
constexpr unsigned k2_slot(int s) { return s < 4 ? L1_BUFA + s * L2_SLOT : L1_BUFB + (s - 4) * L2_SLOT; }
constexpr int k2_tW(int m) { return m == 4 ? -30 : 16 * (m - 5); }
constexpr int k2_tH(int m, int half) {
  return m <= 2 ? -56 + 2 * m + half : m <= 4 ? -44 + 2 * m + half : 16 * (m - 5) + 5 + 2 * half;
}
constexpr int k2_tA(int i) { return 16 * (6 + i) + 8; }
constexpr int k2_younger(int t0, int n) {
  int cnt = 0;
  const int tw = 16 * n + 3;
  for (int m = 4; m <= 17; ++m) if (k2_tW(m) > t0 && k2_tW(m) < tw) ++cnt;
  for (int m = 0; m <= 17; ++m)
    for (int h = 0; h < 2; ++h) if (k2_tH(m, h) > t0 && k2_tH(m, h) < tw) ++cnt;
  for (int i = 0; i < 6; ++i) if (k2_tA(i) > t0 && k2_tA(i) < tw) ++cnt;
  for (int i = 0; i < 4; ++i) if (-44 + i > t0) ++cnt;
  return cnt;
}
constexpr int k2_count(int n) {
  const int m = n + 1, th = k2_tH(m, 1), tw = m >= 4 ? k2_tW(m) : -100;
  return k2_younger(th > tw ? th : tw, n);
}
static_assert(k2_count(0) == 12 && k2_count(1) == 13 && k2_count(2) == 10 && k2_count(3) == 10 && k2_count(8) == 12 && k2_count(13) == 12 && k2_younger(-41, 1) == 9, "schedule");
static_assert(k2_younger(16 * 3 + 4, 5) == 6, "schedule");
template <int T, int I, int ROLE = 0> DI void k2_issue(K2S& S, const K2C& C) {
  if constexpr (T <= 17) {
    constexpr unsigned ks = k2_loc(T);
    const unsigned wso = ABL2_NOW ? 0xf0000000u : C.wsoF + ks * 36864u, hso = ABL2_NOH ? 0xf0000000u : C.hsoF + ks * 131072u;
    if constexpr (I == 0) glds16<ROLE>(C.lb + k2_slot(T % L2_NS) + C.w * 1024u, C.vo0, C.rsW, wso);
    if constexpr (I == 1) { if ((T & 7) == C.w) glds16<3>(C.lb + k2_slot(T % L2_NS) + 8192u, C.vo1, C.rsW, wso); }
    if constexpr (I == 2) bload16_sc1<0, ROLE>(S.hr[T % (K2D + 1)][0], C.hvoff, C.rsH, hso);
    if constexpr (I == 3) bload16_sc1<1024, ROLE>(S.hr[T % (K2D + 1)][1], C.hvoff, C.rsH, hso);
  }
}
constexpr int K2_SEQ = 135;
constexpr int k2_T(int M) { return M < K2_SEQ ? M / 9 : 15 + (M - K2_SEQ) % 6; }
constexpr int k2_c(int M) { return M < K2_SEQ ? M % 9 : (M - K2_SEQ) / 6; }
template <int M, int H> DI void k2_read(K2S& S, const K2C& C) {
  if constexpr (M < 189) {
    constexpr int T = k2_T(M), c = k2_c(M);
    if constexpr (M < K2_SEQ) {
      if constexpr (H == 0 && c == 0) { S.ard = C.lanec + k2_slot(T % L2_NS); PIN(S.ard); }
      if constexpr (H == 0) S.wlo[M % 6] = trread(S.ard + c * 32);
      else S.whi[M % 6] = trread(S.ard + c * 32 + 4608);
    } else {
      static_assert(k2_slot(16 % L2_NS) == k2_slot(15 % L2_NS) + L2_SLOT && k2_slot(17 % L2_NS) == k2_slot(15 % L2_NS) + 2 * L2_SLOT, "ring");
      if constexpr (H == 0 && M == K2_SEQ) { S.ard = C.lanec + k2_slot(15 % L2_NS); PIN(S.ard); S.ardm = C.lanec + L2_MT; PIN(S.ardm); }
      const unsigned a = T <= 17 ? S.ard + (T - 15) * L2_SLOT + c * 32 : S.ardm + (T - 18) * L2_SLOT + c * 32;
      if constexpr (H == 0) S.wlo[M % 6] = trread(a);
      else S.whi[M % 6] = trread(a + 4608);
    }
  }
}
template <int c> DI void k2_send(K2S& S, const K2C& C) {
  if (C.w != C.sp) {
    const u32x4 v = {pack2(S.acc[c][0][0], S.acc[c][0][1]), pack2(S.acc[c][0][2], S.acc[c][0][3]),
                     pack2(S.acc[c][1][0], S.acc[c][1][1]), pack2(S.acc[c][1][2], S.acc[c][1][3])};
    bstore16_sc1<0>(v, C.hvoff, C.rsB, C.xso + c * 1024u);
  } else {
    *LDSP(f32x4, C.lb + L2_OWN + (c * 2) * 1024u + C.hvoff) = S.acc[c][0];
    *LDSP(f32x4, C.lb + L2_OWN + (c * 2 + 1) * 1024u + C.hvoff) = S.acc[c][1];
  }
}
template <int T, int N> DI void k2_wait(K2S& S) {
  asm volatile("s_waitcnt vmcnt(%2)\n\ts_barrier" : "+v"(S.hr[T % (K2D + 1)][0]), "+v"(S.hr[T % (K2D + 1)][1]) : "i"(N) : "memory");
}
template <int M> DI void k2_step(K2S& S, const K2C& C) {
  constexpr int T = k2_T(M), c = k2_c(M);
  constexpr bool seq = M < K2_SEQ;
  const s16x8 wf = __builtin_shufflevector(S.wlo[M % 6], S.whi[M % 6], 0, 1, 2, 3, 4, 5, 6, 7);
  const s16x8 h0 = __builtin_bit_cast(s16x8, k2_att(T) ? S.ha[k2_att(T) ? T - 18 : 0][0] : S.hr[T % (K2D + 1)][0]);
  const s16x8 h1 = __builtin_bit_cast(s16x8, k2_att(T) ? S.ha[k2_att(T) ? T - 18 : 0][1] : S.hr[T % (K2D + 1)][1]);
  const f32x4 z4 = {0.f, 0.f, 0.f, 0.f};
  S.acc[c][0] = mfma16(wf, h0, T == 0 ? z4 : S.acc[c][0]);
  SB();
  k2_read<M + 5, 0>(S, C);
  SB();
  S.acc[c][1] = mfma16(wf, h1, T == 0 ? z4 : S.acc[c][1]);
  SB();
  k2_read<M + 5, 1>(S, C);
  if constexpr (seq) {
    if constexpr (T + K2D <= 17) {
      if constexpr (c == 0) { if (!STAGGER || C.w < 4) k2_issue<T + K2D, 0, STAGGER>(S, C); }
      if constexpr (STAGGER && c == 1) { if (C.w >= 4) k2_issue<T + K2D, 0, 2>(S, C); }
      if constexpr (c == 2) k2_issue<T + K2D, 1>(S, C);
      if constexpr (c == (STAGGER ? 4 : 5)) { if (!STAGGER || C.w < 4) k2_issue<T + K2D, 2, STAGGER>(S, C); }
      if constexpr (STAGGER && c == 5) { if (C.w >= 4) k2_issue<T + K2D, 2, 2>(S, C); }
      if constexpr (c == (STAGGER ? 6 : 7)) { if (!STAGGER || C.w < 4) k2_issue<T + K2D, 3, STAGGER>(S, C); }
      if constexpr (STAGGER && c == 7) { if (C.w >= 4) k2_issue<T + K2D, 3, 2>(S, C); }
    }
    if constexpr (T >= 6 && T <= 11 && c == 8)
      bload16_sc1<((T - 6) & 1) * 1024>(S.ha[(T - 6) >> 1][(T - 6) & 1], C.hvoff, C.rsH, C.hsoA + ((T - 6) >> 1) * 131072u);
    if constexpr (T == 3 && c == 4) {
      if (C.w == 0) asm volatile("global_load_dword %0, %1, off sc1 ; role 3" : "=v"(S.flag) : "v"(C.chf));
    }
    if constexpr (T == 5 && c == 3) {
      if (C.w == 0) {
        asm volatile("s_waitcnt vmcnt(%1)" : "+v"(S.flag) : "i"(k2_younger(16 * 3 + 4, 5) - (STAGGER ? 1 : 0)));
        if (rfl(S.flag) < 4u) ctr_wait(C.chf, 4u);
      }
    }
    if constexpr (c == 3 && T < 14) k2_wait<T + 1, T == 1 ? k2_younger(-41, 1) : k2_count(T)>(S);
    if constexpr (c == 3 && T == 1) { if (threadIdx.x == 0) ctr_add(C.chf); }
    if constexpr (c == 2 && T == 14)
      asm volatile("s_waitcnt vmcnt(0)"
                   : "+v"(S.ha[0][0]), "+v"(S.ha[0][1]), "+v"(S.ha[1][0]), "+v"(S.ha[1][1]), "+v"(S.ha[2][0]), "+v"(S.ha[2][1]),
                     "+v"(S.hr[15 % (K2D + 1)][0]), "+v"(S.hr[15 % (K2D + 1)][1]), "+v"(S.hr[16 % (K2D + 1)][0]), "+v"(S.hr[16 % (K2D + 1)][1]),
                     "+v"(S.hr[17 % (K2D + 1)][0]), "+v"(S.hr[17 % (K2D + 1)][1]));
    if constexpr (c == 3 && T == 14) asm volatile("s_barrier" ::: "memory");
  } else {
    if constexpr (T == 20 && c > 0) k2_send<c - 1>(S, C);
    if constexpr (T == 15 && c == 7) {
      asm volatile("s_waitcnt vmcnt(1)\n\ts_barrier" ::: "memory");
      if (C.w == 0 && (C.hvoff >> 4) < 8) ctr_add(C.cxa + (C.hvoff >> 4) * 32);
    }
  }
  SB();
  if constexpr (seq && c == 8 && T % 5 == 4) { STAMP(S.dg, 18 + T / 5) }
  if constexpr (M + 1 < 189) k2_step<M + 1>(S, C);
}

__global__ __launch_bounds__(512, 2) void kf_main(KFP p) {
  __shared__ __attribute__((aligned(128))) char smem[L1_TOTAL];
  const unsigned lb = (unsigned)(size_t)(__attribute__((address_space(3))) char*)smem;
  const int tid = threadIdx.x, l = tid & 63;
  const int w = rfl(tid >> 6);
  const int b = blockIdx.x, xc = b & 7, r = b >> 3;
  const int cr = r & 3, j = 4 * xc + cr, ib = r >> 2;
  const int g = l >> 4, q = (l & 15) >> 2, pp = l & 3, c16 = l & 15;

  STAMPR(p.dg, 30) STAMP(p.dg, 0)
  const rsrc_t rsX = mk_rsrc(p.xf, (unsigned)SZ_XF);
  const rsrc_t rsQ = mk_rsrc(p.wq, (unsigned)SZ_WQ);
  const rsrc_t rsG = mk_rsrc(p.wg, D_ * F_ * 2);
  const rsrc_t rsU = mk_rsrc(p.wu, D_ * F_ * 2);
  const rsrc_t rsH = mk_rsrc(p.hf, (unsigned)SZ_HF);
  const rsrc_t rsB = mk_rsrc(p.xb, (unsigned)SZ_XB);
  const unsigned xso = (((ib * 4 + cr) * 8 + w) * 8 + xc) * 9216u;
  gu32* const ctr = (gu32*)p.ctr;
  K1S S;

  if (!NO_CODEWARM) {
    unsigned long long pc;
    unsigned len;
    asm volatile("s_getpc_b64 %0\n98764:\n\ts_mov_b32 %1, 98765f-98764b" : "=s"(pc), "=s"(len));
    warm_lines(lb + L1_SCR + 15872u, l < 2 ? ((r * 8 + w) * 2 + l) * 128u : 0xf0000000u, mk_rsrc((const void*)pc, len));
  }

#pragma unroll
  for (int i = 0; i < 9; ++i) glds16(lb + L1_BUFA + (9 * w + i) * 1024u, l * 16u, rsQ, (j * 72u + 9 * w + i) * 1024u);
  const int T0 = 16 * ib + 2 * w;
  const unsigned hvoff = l * 16u, xsb = T0 * 18432u;
#define XL(s) bload16<((s) & 3) * 1024>(S.xf[0][s], hvoff, rsX, xsb + ((s) >> 2) * 4096u); \
              bload16<((s) & 3) * 1024>(S.xf[1][s], hvoff, rsX, xsb + 18432u + ((s) >> 2) * 4096u);
  XL(0) XL(1) XL(2) XL(3) XL(4) XL(5)
#undef XL
  const unsigned wcol = (576u * xc + 128u * cr) * 2u;
  if (!NO_WARM0) {
    const unsigned si = 3 * w + (l - 9);
    const unsigned vo = l < 9 ? (72u * ib + 9u * w + l) * 9216u + wcol
                      : (l < 12 && si < 18 ? (18u * r + si) * 9216u + (576u * xc + 512u) * 2u : 0xf0000000u);
    warm_lines(lb + L1_SCR + 15872u, vo, rsG);
    warm_lines(lb + L1_SCR + 15872u, vo, rsU);
  }
  K1G G;
  G.w = w;
  {
    const unsigned rowv = (l >> 3) * 9216u, c8 = l & 7, s0 = (l >> 4) & 1;
    const unsigned vA = rowv + ((c8 ^ (s0 << 1)) << 4), vB = rowv + ((c8 ^ ((s0 | 2) << 1)) << 4);
    G.vo0 = (w & 1) ? vB : vA;
    G.vo1 = (w & 1) ? vA : vB;
  }
  G.sstep = 8u * 9216u;
  const unsigned wsoff = 72u * w * 9216u, wdst = lb + 72u * w * 128u;
  G.rs = rsG; G.soff = wsoff + wcol; G.dst = wdst + L1_BUFB;

  STAMP(p.dg, 1)
  wait_bar<NO_WARM0 ? 12 : 14>();
  STAMP(p.dg, 2)

  {
    const unsigned qa = lb + L1_BUFA + l * 16u;
    const f32x4 z4 = {0.f, 0.f, 0.f, 0.f};
    f32x4 Q[4][2];
#pragma unroll
    for (int X = 0; X < 4; ++X) Q[X][0] = Q[X][1] = z4;
    s16x8 wq[2][4];
#pragma unroll
    for (int X = 0; X < 4; ++X) wq[0][X] = *LDSP(s16x8, qa + X * 1024u);
    k1_qkv_step<0>(S, wq, Q, qa, hvoff, rsX, xsb, G);
    STAMP(p.dg, 3)
    unsigned fKA[4], fKB[4], fVA[4], fVB[4], fQ2[4];
#pragma unroll
    for (int T = 0; T < 2; ++T)
#pragma unroll
      for (int h2 = 0; h2 < 2; ++h2) {
        fKA[2 * T + h2] = pack2(phi_f(Q[0][T][2 * h2]), phi_f(Q[0][T][2 * h2 + 1]));
        fKB[2 * T + h2] = pack2(phi_f(Q[1][T][2 * h2]), phi_f(Q[1][T][2 * h2 + 1]));
        fVA[2 * T + h2] = pack2(Q[2][T][2 * h2], Q[2][T][2 * h2 + 1]);
        const unsigned raw = pack2(Q[3][T][2 * h2], Q[3][T][2 * h2 + 1]);
        fVB[2 * T + h2] = c16 < 2 ? 0x3f803f80u : raw;
        fQ2[2 * T + h2] = pack2(phi_f(Q[3][T][2 * h2]), phi_f(Q[3][T][2 * h2 + 1]));
      }
    const s16x8 aKA = mk_frag(fKA[0], fKA[1], fKA[2], fKA[3]), aKB = mk_frag(fKB[0], fKB[1], fKB[2], fKB[3]);
    const s16x8 bVA = mk_frag(fVA[0], fVA[1], fVA[2], fVA[3]), bVB = mk_frag(fVB[0], fVB[1], fVB[2], fVB[3]);
    const s16x8 aQ2 = mk_frag(fQ2[0], fQ2[1], fQ2[2], fQ2[3]);
    const f32x4 R0 = mfma16(aKA, bVA, z4), R1 = mfma16(aKA, bVB, z4);
    const f32x4 R2 = mfma16(aKB, bVA, z4), R3 = mfma16(aKB, bVB, z4);
    {
      const unsigned one = (c16 >> 2) == g ? (0x3f80u << (16 * (c16 & 1))) : 0u;
      const unsigned i0 = (c16 & 2) ? 0u : one, i1 = (c16 & 2) ? one : 0u;
#pragma unroll
      for (int T = 0; T < 2; ++T) {
        const s16x8 id = T == 0 ? mk_frag(i0, i1, 0u, 0u) : mk_frag(0u, 0u, i0, i1);
        const f32x4 qa4 = mfma16(aKB, id, z4), qb4 = mfma16(aQ2, id, z4);
        const unsigned b01 = pack2(qb4[0], qb4[1]), b23 = pack2(qb4[2], qb4[3]);
        bstore16_sc1<0>(u32x4{pack2(qa4[0], qa4[1]), pack2(qa4[2], qa4[3]), g == 1 ? b01 : (g == 2 ? b23 : 0u), g == 1 ? b23 : 0u},
                        hvoff + T * 1024u, rsB, xso);
      }
    }
    {
      const unsigned sb = lb + L1_SCR + w * 1408u;
      const int eB = c16 == 0 ? 18 : c16 + 14;
      const bool okB = c16 == 0 || c16 == 2 || c16 == 3;
      const unsigned dummy = lb + L1_SCR + 8 * 1408u + l * 4u;
#pragma unroll
      for (int rr = 0; rr < 4; ++rr) {
        const int d = 4 * g + rr;
        *LDSP(float, sb + (d * 19 + c16) * 4) = R0[rr];
        *LDSP(float, okB ? sb + (d * 19 + eB) * 4 : dummy) = R1[rr];
        if (rr >= 2) {
          *LDSP(float, g == 0 ? sb + ((14 + rr) * 19 + c16) * 4 : dummy) = R2[rr];
          *LDSP(float, g == 0 && okB ? sb + ((14 + rr) * 19 + eB) * 4 : dummy) = R3[rr];
        }
      }
    }
  }
  STAMP(p.dg, 4)
  wait_bar<2>();
  STAMP(p.dg, 5)

  const unsigned lane0 = lb + (8 * g + q) * 128u + 8u * pp + ((((q >> 1) & 1) | ((g & 1) << 1)) << 5);
  const unsigned abA = lane0 + L1_BUFA, abB = lane0 + L1_BUFB;
  const unsigned ks0 = 18u * xc + 4u * cr;
  const unsigned hs0 = (ks0 * 128u + T0) * 1024u;

  k1_first_reads<4096>(S, abB);
  const rsrc_t rsKV = mk_rsrc(p.kv, (unsigned)SZ_KV);
  if (tid < 88) {
    f32x4 a = *LDSP(f32x4, lb + L1_SCR + tid * 16);
#pragma unroll
    for (int ww = 1; ww < 8; ++ww) a += *LDSP(f32x4, lb + L1_SCR + ww * 1408u + tid * 16);
    bstore16_sc1<0>(__builtin_bit_cast(u32x4, a), tid * 16u, rsKV, (j * 8 + ib) * 1408u);
  }
  SB();
  const rsrc_t rsW = mk_rsrc(p.wd, F_ * D_ * 2), rsO = mk_rsrc(p.wo, D_ * D_ * 2);
  constexpr unsigned OOB = 0xf0000000u;
  const unsigned wdl = (576u * xc * 9u + r * 162u) * 128u;
  const unsigned win1 = (72u * ib + 18u * (w & 3) + l) * 9216u + wcol + 128u;
  G.wm0 = lb + L1_SCR + 15872u;
  if (WARM_MODE == 1) {
    G.wvo = l < 9 ? (72u * ib + 9u * w + l) * 9216u + wcol + 128u : OOB;
    G.wrs = rsG;
    G.wvo2 = w < 3 ? (l < 54 ? wdl + (w * 54u + l) * 128u : OOB)
           : w == 3 ? (l < 21 && r * 21 + l < 648 ? (72u * xc * 9u + r * 21u + l) * 128u : OOB) : (l < 18 ? win1 : OOB);
    G.wrs2 = w < 3 ? rsW : w == 3 ? rsO : rsU;
  } else {
    const unsigned sl = (18u * r + 5u * (w & 3) + l) * 9216u + (576u * xc + 512u) * 2u, su = (18u * r + 9u * (w & 1) + l) * 9216u + (576u * xc + 512u) * 2u;
    G.wvo = w < 4 ? (l < 5 && 5 * w + l < 18 ? sl : OOB) : w < 6 ? (l < 9 ? su : OOB) : (l < 27 ? wdl + (27u * (w - 6) + l) * 128u : OOB);
    G.wrs = w < 4 ? rsG : w < 6 ? rsU : rsW;
  }
  gu32* const ckv = ctr + (CT_KV + xc) * 32;
  const unsigned gvo0 = G.vo0, gvo1 = G.vo1;
  G.rs = rsU; G.soff = wsoff + wcol; G.dst = wdst + L1_BUFA;
  gu32* const chA = ctr + (CT_H + xc * 8 + ib) * 32;
  k1_step<1, 0, 4, 4096, 4096, 1, 1, 0>(S, abB, abA, G, rsH, hvoff, 0, 0, ckv, chA);
  STAMP(p.dg, 6)
  G.rs = (w >> 2) ? rsU : rsG;
  G.vo0 = G.vo1 = (l >> 2) * 9216u + (((l & 3) ^ ((l >> 5) << 1)) * 16u);
  G.sstep = 16u * 9216u;
  G.soff = 144u * (w & 3) * 9216u + (576u * xc + 512u + 32u * (cr >> 1)) * 2u;
  G.dst = lb + L1_BUFB + (w >> 2) * 36864u + 144u * (w & 3) * 64u;
  const unsigned abS = lb + L1_BUFB + (8 * g + q) * 64u + 8u * pp + (((cr & 1) ^ (g & 1)) << 5);
  if (WARM_MODE == 2) {
    G.wvo = w < 4 ? (l < 18 ? win1 : OOB) : (l < 14 && 14 * (w - 4) + l < 54 ? wdl + (54u + 14u * (w - 4) + l) * 128u : OOB);
    G.wrs = w < 4 ? rsG : rsW;
  }
  k1_step<2, 1, 4, 4096, 2048, 1, 0, 0>(S, abA, abS, G, rsH, hvoff, hs0, 0, ckv, chA);
  STAMP(p.dg, 8)
  G.rs = rsG; G.vo0 = gvo0; G.vo1 = gvo1; G.sstep = 8u * 9216u; G.soff = wsoff + wcol + 128u; G.dst = wdst + L1_BUFA;
  if (WARM_MODE == 2) { G.wvo = w < 4 && l < 18 ? win1 : OOB; G.wrs = rsU; }
  k1_step<0, 2, 2, 2048, 4096, 1, 0, 0>(S, abS, abA, G, rsH, hvoff, 0, hs0 + 131072u, ckv, chA);
  STAMP(p.dg, 10)
  G.rs = rsU; G.soff = wsoff + wcol + 128u; G.dst = wdst + L1_BUFB;
  if (WARM_MODE == 2) {
    G.wvo = w < 2 ? (l < 27 ? wdl + (108u + 27u * w + l) * 128u : OOB)
                  : (w == 2 && l < 21 && r * 21 + l < 648 ? (72u * xc * 9u + r * 21u + l) * 128u : OOB);
    G.wrs = w < 2 ? rsW : rsO;
  }
  k1_step<1, 3, 4, 4096, 4096, 1, 2, 0>(S, abA, abB, G, rsH, hvoff, 0,
                                        ((18u * xc + 16u + (cr >> 1)) * 128u + T0) * 1024u + (cr & 1) * 8u, ckv, chA);
  STAMP(p.dg, 12)
  const unsigned wsoF = 18u * xc * 36864u + cr * 288u;
  G.rs = w < 4 ? rsW : rsO;
  G.soff = w < 4 ? wsoF + (4 * (w >> 1) + (w & 1)) * 36864u : 18u * (4 * xc + w - 4) * 1152u + cr * 288u;
  G.dst = lb + L1_BUFA + w * L2_SLOT;
  G.rsk = rsKV;
  { const unsigned hh = tid / 88, q4 = tid - 88 * hh; G.vok = tid < 352 ? ((4 * xc + hh) * 8 * 352 + q4 * 4) * 4u : 0xf0000000u; }
  k1_step<2, 1, 4, 4096, 0, 2, 3, 0>(S, abB, 0, G, rsH, hvoff, hs0 + 2 * 131072u, 0, ckv, chA);

  K2S S2;
  K2C C;
  C.rsH = rsH; C.rsW = rsW; C.w = w; C.sp = xc; C.lb = lb;
  C.rsB = rsB;
  C.xso = xso;
  C.cxa = ctr + (CT_X + (ib * 4 + cr) * 8) * 32;
  C.chf = ctr + (CT_H2 + xc * 8 + ib) * 32;
  { const int ci = 64 * w + l, rho = ci / 18, c = ci - 18 * rho; C.vo0 = rho * 1152u + c * 16u; }
  { const int ci = 512 + l, rho = ci / 18, c = ci - 18 * rho; C.vo1 = rho * 1152u + c * 16u; }
  C.hvoff = hvoff;
  const unsigned hT = T0 * 1024u;
  C.hsoF = hT + 18u * xc * 131072u;
  C.hsoA = hT + (KS_FFN + 3u * xc) * 131072u;
  C.wsoF = wsoF;
  C.lanec = lb + (4 * g + q) * 288u + 8u * pp;

  asm volatile("s_waitcnt vmcnt(2)" : "+v"(S.kp[0]), "+v"(S.kp[1]), "+v"(S.kp[2]), "+v"(S.kp[3]), "+v"(S.kp[4]), "+v"(S.kp[5]), "+v"(S.kp[6]), "+v"(S.kp[7]));
  {
    f32x4 a = __builtin_bit_cast(f32x4, S.kp[0]);
#pragma unroll
    for (int i = 1; i < 8; ++i) a += __builtin_bit_cast(f32x4, S.kp[i]);
    if (tid < 352) *LDSP(f32x4, lb + L2_KV + tid * 16u) = a;
  }
  k1_epi_all<2, 3, 1>(S);
  k1_store_pair(S, rsH, hvoff, hs0 + 3 * 131072u);
  u32x4 pqv[2];
  bload16_sc1<0>(pqv[0], hvoff, rsB, xso);
  bload16_sc1<1024>(pqv[1], hvoff, rsB, xso);
  k2_issue<0, 2>(S2, C); k2_issue<0, 3>(S2, C);
  STAMP(p.dg, 14) STAMPR(p.dg, 27)
  asm volatile("s_waitcnt lgkmcnt(0)\n\ts_barrier" ::: "memory");
  STAMP(p.dg, 15)
  const f32x4 z4 = {0.f, 0.f, 0.f, 0.f};
  {
    const int hh = w >> 1, d = 16 * (w & 1) + c16;
    const unsigned ao = lb + L2_WO + hh * L2_SLOT + (8 * g + q) * 288u + 8u * pp;
    float v[8];
    const unsigned ka0 = lb + L2_KV + (hh * 352 + (d < 18 ? d : 0) * 19 + 8 * (g < 2 ? g : 2)) * 4;
#pragma unroll
    for (int e = 0; e < 8; ++e) v[e] = *LDSP(float, ka0 + e * 4);
    s16x8 A[9];
#pragma unroll
    for (int c = 0; c < 9; ++c) A[c] = trfrag(ao + c * 32, ao + c * 32 + 4 * 288);
    unsigned bh[4];
#pragma unroll
    for (int e2 = 0; e2 < 4; ++e2) {
      const float v0 = (d < 18 && 8 * g + 2 * e2 < 18) ? v[2 * e2] : 0.f, v1 = (d < 18 && 8 * g + 2 * e2 + 1 < 18) ? v[2 * e2 + 1] : 0.f;
      bh[e2] = pack2(v0, v1);
    }
    const s16x8 Bh = mk_frag(bh[0], bh[1], bh[2], bh[3]);
    const int ka = 24 * hh + d;
    const unsigned mo = d < 24 ? lb + L2_MT + (ka >> 5) * L2_SLOT + (ka & 31) * 288u + 8u * g : lb + k2_slot(L2_NS - 1) + l * 8u;
    f32x4 D[9];
#pragma unroll
    for (int c = 0; c < 9; ++c) D[c] = mfma16(A[c], Bh, z4);
#pragma unroll
    for (int c = 0; c < 9; ++c) *LDSP(u32x2, mo + c * 32) = u32x2{pack2(D[c][0], D[c][1]), pack2(D[c][2], D[c][3])};
  }
  SB();
  k2_issue<1, 2>(S2, C); k2_issue<1, 3>(S2, C); k2_issue<2, 2>(S2, C); k2_issue<2, 3>(S2, C);
  STAMP(p.dg, 16)
  {
    float za[4], zb[4];
#pragma unroll
    for (int e = 0; e < 4; ++e) {
      const int da = 4 * (g - 1) + e, db = da + 12;
      const float ta = *LDSP(float, lb + L2_KV + (cr * 352 + (g > 0 ? da * 19 + 18 : 0)) * 4);
      const float tb = *LDSP(float, lb + L2_KV + (cr * 352 + (g > 0 && db < 18 ? db * 19 + 18 : 0)) * 4);
      za[e] = g > 0 ? ta : 0.f;
      zb[e] = g > 0 && db < 18 ? tb : 0.f;
    }
    const int piA = 6 * cr + g - 1, piB = 6 * cr + g + 2;
    const unsigned base = (KS_FFN + 3u * xc) * 131072u + T0 * 1024u + c16 * 16u;
    const unsigned oA = g == 0 ? 0xf0000000u : base + (piA >> 3) * 131072u + (piA & 3) * 256u + ((piA >> 2) & 1) * 8u;
    const unsigned oB = g == 0 ? 0xf0000000u : base + (piB >> 3) * 131072u + (piB & 3) * 256u + ((piB >> 2) & 1) * 8u;
    asm volatile("s_waitcnt vmcnt(6)" : "+v"(pqv[0]), "+v"(pqv[1]));
#pragma unroll
    for (int T = 0; T < 2; ++T) {
      const u32x4 u = pqv[T];
      const float v[8] = {bf_lo(u[0]), bf_hi(u[0]), bf_lo(u[1]), bf_hi(u[1]), bf_lo(u[2]), bf_hi(u[2]), bf_lo(u[3]), bf_hi(u[3])};
      float den = 0.f;
#pragma unroll
      for (int e = 0; e < 4; ++e) den += v[e] * za[e] + v[4 + e] * zb[e];
      {
        const unsigned d0 = __float_as_uint(den);
        const auto r16 = __builtin_amdgcn_permlane16_swap(d0, d0, false, false);
        const unsigned r0 = r16[0], r1 = r16[1];
        const unsigned d1 = __float_as_uint(__uint_as_float(r0) + __uint_as_float(r1));
        const auto r32 = __builtin_amdgcn_permlane32_swap(d1, d1, false, false);
        const unsigned s0 = r32[0], s1 = r32[1];
        den = __uint_as_float(s0) + __uint_as_float(s1);
      }
      const float inv = frcp(den + 1e-6f);
      const u32x2 va = {pack2(v[0] * inv, v[1] * inv), pack2(v[2] * inv, v[3] * inv)};
      const u32x2 vb = {pack2(v[4] * inv, v[5] * inv), pack2(v[6] * inv, v[7] * inv)};
      __builtin_amdgcn_raw_buffer_store_b64(va, rsH, oA + T * 1024u, 0, AUX_SC1);
      __builtin_amdgcn_raw_buffer_store_b64(vb, rsH, oB + T * 1024u, 0, AUX_SC1);
    }
  }
  SB();
  k2_issue<3, 2>(S2, C); k2_issue<3, 3>(S2, C); k2_issue<4, 2>(S2, C); k2_issue<4, 3>(S2, C);
  k2_issue<4, 0>(S2, C); k2_issue<4, 1>(S2, C);
  asm volatile("s_waitcnt lgkmcnt(0)\n\ts_barrier" ::: "memory");
  STAMP(p.dg, 17)
  asm volatile("s_waitcnt vmcnt(13)" : "+v"(S2.hr[0][0]), "+v"(S2.hr[0][1]));
  k2_read<0, 0>(S2, C); k2_read<0, 1>(S2, C); k2_read<1, 0>(S2, C); k2_read<1, 1>(S2, C); k2_read<2, 0>(S2, C);
  k2_read<2, 1>(S2, C); k2_read<3, 0>(S2, C); k2_read<3, 1>(S2, C); k2_read<4, 0>(S2, C); k2_read<4, 1>(S2, C);
  SB();
  k2_step<0>(S2, C);
  STAMP(p.dg, 23) STAMPR(p.dg, 28)

  const int sp = xc, ni = cr, mi = ib;
  const int grp = mi * 4 + ni;
  k2_send<8>(S2, C);
  const int tok0 = 256 * mi + 32 * sp;
  const rsrc_t rsXr = mk_rsrc(p.x, S_ * D_ * 2);
  u32x4 xr[2];
  unsigned xo[2];
#pragma unroll
  for (int i = 0; i < 2; ++i) {
    const int it = tid + 512 * i, tk = it / 18, ch = it - tk * 18;
    xo[i] = it < 576 ? ((tok0 + tk) * D_ + 144 * ni + 8 * ch) * 2u : 0xf0000000u;
  }
  bload16<0>(xr[0], xo[0], rsXr, 0);
  bload16<0>(xr[1], xo[1], rsXr, 0);
  if (w == sp) ctr_wait(C.cxa + sp * 32, 8u);
  asm volatile("s_waitcnt lgkmcnt(0)\n\ts_barrier" ::: "memory");
  STAMP(p.dg, 24)
  u32x4 v[8];
  auto fetch = [&](int c) {
#pragma unroll
    for (int src = 0; src < 8; ++src)
      bload16_sc1<0>(v[src], src == sp ? 0xf0000000u : hvoff + c * 1024u, rsB, ((grp * 8 + sp) * 8 + src) * 9216u);
  };
  auto reduce = [&](int c) {
    asm volatile("s_waitcnt vmcnt(0)" : "+v"(v[0]), "+v"(v[1]), "+v"(v[2]), "+v"(v[3]), "+v"(v[4]), "+v"(v[5]), "+v"(v[6]), "+v"(v[7]));
    f32x4 a0 = *LDSP(f32x4, lb + L2_OWN + (2 * c) * 1024u + l * 16u), a1 = *LDSP(f32x4, lb + L2_OWN + (2 * c + 1) * 1024u + l * 16u);
#pragma unroll
    for (int src = 0; src < 8; ++src) {
      a0 += f32x4{bf_lo(v[src][0]), bf_hi(v[src][0]), bf_lo(v[src][1]), bf_hi(v[src][1])};
      a1 += f32x4{bf_lo(v[src][2]), bf_hi(v[src][2]), bf_lo(v[src][3]), bf_hi(v[src][3])};
    }
    *LDSP(f32x4, lb + L2_OUT + (c16 * 148 + 16 * c + 4 * g) * 4) = a0;
    *LDSP(f32x4, lb + L2_OUT + ((16 + c16) * 148 + 16 * c + 4 * g) * 4) = a1;
  };
  if (w < 5) {
    fetch(w);
    asm volatile("s_waitcnt vmcnt(8)\n\ts_barrier" ::: "memory");
  } else {
    asm volatile("s_waitcnt vmcnt(0)\n\ts_barrier" ::: "memory");
  }
  gu32* const cxb = ctr + (CT_X2 + grp * 8) * 32;
  if (w == 7) {
    if (l < 8) ctr_add(cxb + l * 32);
    ctr_wait(cxb + sp * 32, 8u);
  }
  if (w < 5) reduce(w);
  asm volatile("s_waitcnt vmcnt(0) lgkmcnt(0)\n\ts_barrier" ::: "memory");
  STAMP(p.dg, 25) STAMPR(p.dg, 29)
  if (w >= 4) { fetch(w + 1); reduce(w + 1); }
  asm volatile("s_waitcnt vmcnt(0)" : "+v"(xr[0]), "+v"(xr[1]));
  __syncthreads();
#pragma unroll
  for (int i = 0; i < 2; ++i) {
    const int it = tid + 512 * i;
    if (it < 576) {
      const int tk = it / 18, ch = it - tk * 18;
      const f32x4 a = *LDSP(f32x4, lb + L2_OUT + (tk * 148 + 8 * ch) * 4), c = *LDSP(f32x4, lb + L2_OUT + (tk * 148 + 8 * ch + 4) * 4);
      u32x4 ov;
      ov[0] = pack2(a[0] + bf_lo(xr[i][0]), a[1] + bf_hi(xr[i][0]));
      ov[1] = pack2(a[2] + bf_lo(xr[i][1]), a[3] + bf_hi(xr[i][1]));
      ov[2] = pack2(c[0] + bf_lo(xr[i][2]), c[1] + bf_hi(xr[i][2]));
      ov[3] = pack2(c[2] + bf_lo(xr[i][3]), c[3] + bf_hi(xr[i][3]));
      *(u32x4*)((char*)p.out + xo[i]) = ov;
    }
  }
  STAMP(p.dg, 26) STAMPR(p.dg, 31) STAMP_DUMP(p.dg)
  asm volatile("98765:");
}

extern "C" void kernel_launch(void* const* d_in, const int* in_sizes, int n_in, void* d_out, int out_size,
                              void* d_ws, size_t ws_size, hipStream_t stream) {
  const uint16_t* x = (const uint16_t*)d_in[0];
  const void* wqkv = d_in[1];
  const uint16_t* wo = (const uint16_t*)d_in[2];
  const void* wg = d_in[4];
  const void* wu = d_in[5];
  const void* wd = d_in[6];
  char* ws = (char*)d_ws + (WS_TOP ? ((ws_size - (OFF_CT + SZ_CT)) & ~(size_t)0x1fffff) : 0);

  k0_ln<<<256, 512, 0, stream>>>(x, (u32x4*)(ws + OFF_XF), (const uint16_t*)wqkv, (u32x4*)(ws + OFF_WQ),
                                 (unsigned*)(ws + OFF_CT));
  KFP p;
  p.xf = ws + OFF_XF; p.wq = ws + OFF_WQ; p.wg = wg; p.wu = wu; p.wd = wd; p.wo = wo; p.x = x;
  p.hf = ws + OFF_HF; p.kv = ws + OFF_KV; p.xb = ws + OFF_XB; p.ctr = ws + OFF_CT; p.out = d_out;
  kf_main<<<256, 512, 0, stream>>>(p);
}
```

```cpp
#include <hip/hip_runtime.h>
#include <hip/hip_bf16.h>
#include <stdint.h>

#define DI __device__ __forceinline__
typedef __attribute__((ext_vector_type(8))) short s16x8;
typedef __attribute__((ext_vector_type(4))) short s16x4;
typedef __attribute__((ext_vector_type(4))) float f32x4;
typedef __attribute__((ext_vector_type(4))) unsigned u32x4;
typedef __attribute__((ext_vector_type(2))) unsigned u32x2;
typedef __amdgpu_buffer_rsrc_t rsrc_t;
#define LDSP(T, a) ((__attribute__((address_space(3))) T*)(size_t)(a))

constexpr int S_ = 2048, D_ = 576, F_ = 4608, NH = 32, DH = 18;
constexpr int KS_FFN = 144, KS_ALL = 168;
constexpr size_t OFF_XF = 0;
constexpr size_t SZ_XF = (size_t)128 * 18 * 1024;
constexpr size_t OFF_WQ = OFF_XF + SZ_XF;
constexpr size_t SZ_WQ = (size_t)32 * 72 * 1024;
constexpr size_t OFF_HF = OFF_WQ + SZ_WQ;
constexpr size_t SZ_HF = (size_t)KS_ALL * 128 * 1024;
constexpr size_t OFF_KV = OFF_HF + SZ_HF;
constexpr size_t SZ_KV = (size_t)32 * 8 * 352 * 4;
constexpr size_t OFF_XB = OFF_KV + SZ_KV;
constexpr size_t SZ_XB = (size_t)32 * 8 * 8 * 9 * 1024;
constexpr size_t OFF_CT = OFF_XB + SZ_XB;
constexpr int CT_X = 0, CT_X2 = 256, CT_KV = 512, CT_H = 520, CT_H2 = 584, CT_N = 648;
constexpr size_t SZ_CT = (size_t)CT_N * 128;

DI rsrc_t mk_rsrc(const void* p, unsigned bytes) {
  return __builtin_amdgcn_make_buffer_rsrc((void*)p, (short)0, (int)bytes, 0x00020000);
}
DI unsigned rfl(unsigned v) { return __builtin_amdgcn_readfirstlane(v); }
DI float bf_lo(unsigned u) { return __uint_as_float(u << 16); }
DI float bf_hi(unsigned u) { return __uint_as_float(u & 0xffff0000u); }
DI unsigned pack2(float lo, float hi) {
  typedef __attribute__((ext_vector_type(2))) __bf16 bf2;
  typedef __attribute__((ext_vector_type(2))) float f2;
  f2 v = {lo, hi};
  bf2 r = __builtin_convertvector(v, bf2);
  return __builtin_bit_cast(unsigned, r);
}
DI s16x8 mk_frag(unsigned a, unsigned b, unsigned c, unsigned d) {
  u32x4 v = {a, b, c, d};
  return __builtin_bit_cast(s16x8, v);
}
DI f32x4 mfma16(s16x8 a, s16x8 b, f32x4 c) { return __builtin_amdgcn_mfma_f32_16x16x32_bf16(a, b, c, 0, 0, 0); }
DI s16x4 trread(unsigned addr) { return __builtin_amdgcn_ds_read_tr16_b64_v4i16(LDSP(s16x4, addr)); }
DI s16x8 trfrag(unsigned a0, unsigned a1) {
  s16x4 lo = trread(a0), hi = trread(a1);
  return __builtin_shufflevector(lo, hi, 0, 1, 2, 3, 4, 5, 6, 7);
}
template <int ROLE = 0> DI void glds16(unsigned m0v, unsigned voff, rsrc_t rs, unsigned soff) {
  asm volatile("s_mov_b32 m0, %0\n\ts_nop 0\n\tbuffer_load_dwordx4 %1, %2, %3 offen lds ; role %4"
               :: "s"(m0v), "v"(voff), "s"(rs), "s"(soff), "i"(ROLE) : "memory");
}
DI void warm_lines(unsigned m0v, unsigned voff, rsrc_t rs) {
  asm volatile("s_mov_b32 m0, %0\n\ts_nop 0\n\tbuffer_load_dword %1, %2, 0 offen lds" :: "s"(m0v), "v"(voff), "s"(rs) : "memory");
}
template <int IMM> DI void bload16(u32x4& d, unsigned voff, rsrc_t rs, unsigned soff) {
  asm volatile("buffer_load_dwordx4 %0, %1, %2, %3 offen offset:%4"
               : "=v"(d) : "v"(voff), "s"(rs), "s"(soff), "i"(IMM));
}
template <int IMM, int ROLE = 0> DI void bload16_sc1(u32x4& d, unsigned voff, rsrc_t rs, unsigned soff) {
  asm volatile("buffer_load_dwordx4 %0, %1, %2, %3 offen offset:%4 sc1 ; role %5"
               : "=v"(d) : "v"(voff), "s"(rs), "s"(soff), "i"(IMM), "i"(ROLE));
}
template <int IMM> DI void bstore16_sc1(u32x4 v, unsigned voff, rsrc_t rs, unsigned soff) {
  asm volatile("buffer_store_dwordx4 %0, %1, %2, %3 offen offset:%4 sc1\n\ts_nop 1"
               :: "v"(v), "v"(voff), "s"(rs), "s"(soff), "i"(IMM) : "memory");
}
typedef __attribute__((address_space(1))) unsigned gu32;
DI void ctr_add(gu32* c) { __hip_atomic_fetch_add(c, 1u, __ATOMIC_RELAXED, __HIP_MEMORY_SCOPE_AGENT); }
DI void ctr_wait(gu32* c, unsigned n) {
  for (unsigned spins = 0; spins < (1u << 20); ++spins) {
    if ((unsigned)__builtin_amdgcn_readfirstlane(__hip_atomic_load(c, __ATOMIC_RELAXED, __HIP_MEMORY_SCOPE_AGENT)) >= n) break;
    __builtin_amdgcn_s_sleep(1);
  }
}
template <int N> DI void wait_vm2(u32x4& a, u32x4& b) {
  asm volatile("s_waitcnt vmcnt(%2)" : "+v"(a), "+v"(b) : "i"(N));
}
template <int N> DI void wait_bar_nolgkm() {
  asm volatile("s_waitcnt vmcnt(%0)\n\ts_barrier" :: "i"(N) : "memory");
}
template <int N> DI void wait_bar() {
  asm volatile("s_waitcnt vmcnt(%0) lgkmcnt(0)\n\ts_barrier" :: "i"(N) : "memory");
}
DI float fexp2(float x) { return __builtin_amdgcn_exp2f(x); }
DI float frcp(float x) { return __builtin_amdgcn_rcpf(x); }
DI float phi_f(float t) {
  return __builtin_fmaf(t + __builtin_fabsf(t), 0.5f, __builtin_amdgcn_fmed3f(fexp2(t * 1.4426950408889634f), 0.f, 1.f)) + 1e-6f;
}
DI float geglu(float g, float u) {
  float e = fexp2(g * __builtin_fmaf(g * g, -0.10294323970f, -2.30220819814f));
  return (g * u) * frcp(1.f + e);
}
template <int CTRL> DI float dpp_add(float v) {
  return v + __builtin_bit_cast(float, __builtin_amdgcn_update_dpp(0, __builtin_bit_cast(int, v), CTRL, 0xf, 0xf, true));
}
DI float reduce32(float v) {
  v = dpp_add<0xB1>(v);
  v = dpp_add<0x4E>(v);
  v = dpp_add<0x141>(v);
  v = dpp_add<0x140>(v);
  const unsigned u = __float_as_uint(v);
  const auto r = __builtin_amdgcn_permlane16_swap(u, u, false, false);
  const unsigned r0 = r[0], r1 = r[1];
  return __uint_as_float(r0) + __uint_as_float(r1);
}

#define ABL2_NOW 0
#define ABL2_NOH 0
#define K0_NORETILE 0
#define K0_NOLN 0
#define STAGGER 0
#define WS_TOP 0
#define NO_CODEWARM0 0
#define NO_CODEWARM 0
#define NO_WARM0 1
#define PRIO 0
#define XA 5
#define WARM_N 33
#define WARM_MODE 2
#define ABL_NOW 0
#define ABL_NOH 0
#define ABL_NOCOMP 0
#define ABL1_NOCOMP 0
#define ABL1_NOEPI 0
#define ABL1_NOSTAGE 0
#define STAMP(buf, slot)
#define STAMPR(buf, slot)
#define STAMP_DUMP(buf)
__global__ __launch_bounds__(512, 2) void k0_ln(const uint16_t* __restrict__ x, u32x4* __restrict__ xf,
                                                const uint16_t* __restrict__ wqkv, u32x4* __restrict__ wq,
                                                unsigned* __restrict__ ctr) {
  __shared__ u32x4 stage[4][288];
  const int l = threadIdx.x & 63, w = threadIdx.x >> 6;
  if (threadIdx.x < CT_N / 8) ctr[blockIdx.x * (CT_N / 8) + threadIdx.x] = 0u;
  if (w == 7 && !NO_CODEWARM0) {
    unsigned long long pc;
    unsigned off, len;
    asm volatile("s_getpc_b64 %0\n98763:\n\ts_mov_b32 %1, 98764f-98763b\n\ts_mov_b32 %2, 98765f-98764f" : "=s"(pc), "=s"(off), "=s"(len));
    const unsigned v = __builtin_amdgcn_raw_buffer_load_b32(mk_rsrc((const char*)pc + off, len), l < 2 ? (blockIdx.x * 2 + l) * 128u : 0xf0000000u, 0, 0);
    asm volatile("" :: "v"(v));
    return;
  }
  if (w >= 4) {
    const int item = blockIdx.x + 256 * (w - 4);
    if (item >= 576 || K0_NORETILE) return;
    const int h = item / 18, s = item - 18 * h, g = l >> 4;
    typedef u32x4 __attribute__((aligned(4))) u32x4_a4;
    u32x4* st = stage[w - 4];
    u32x4 t[5];
#pragma unroll
    for (int i = 0; i < 5; ++i) {
      const int ci = 64 * i + l < 288 ? 64 * i + l : 0, row = ci / 9, c9 = ci - 9 * row, sec = c9 / 3, cc = c9 - 3 * sec;
      const int col = (sec == 0 ? 576 : (sec == 1 ? 0 : 1152)) + 18 * h + (cc == 2 ? 10 : 8 * cc);
      t[i] = *(const u32x4_a4*)(wqkv + (size_t)(32 * s + row) * (3 * D_) + col);
    }
    asm volatile("" : "+v"(t[0]), "+v"(t[1]), "+v"(t[2]), "+v"(t[3]), "+v"(t[4]));
#pragma unroll
    for (int i = 0; i < 5; ++i)
      if (64 * i + l < 288) st[64 * i + l] = t[i];
    __builtin_amdgcn_fence(__ATOMIC_RELEASE, "wavefront");
    __builtin_amdgcn_wave_barrier();
    const int q4 = (l & 15) >> 2, pp = l & 3;
    const unsigned rowb = (unsigned)(size_t)(__attribute__((address_space(3))) char*)st + (8 * g + q4) * 144u;
    const unsigned off[4] = {8u * pp, 40u + 8u * pp, 96u + 8u * pp, pp == 0 ? 136u : (pp == 1 ? 72u : 88u)};
#pragma unroll
    for (int X = 0; X < 4; ++X)
      wq[((size_t)item * 4 + X) * 64 + l] = __builtin_bit_cast(u32x4, trfrag(rowb + off[X], rowb + off[X] + 4 * 144));
    return;
  }
  if (K0_NOLN) return;
  const int tok = blockIdx.x * 8 + w * 2 + (l >> 5), p = l & 31;
  const u32x4* row = (const u32x4*)(x + (size_t)tok * D_);
  u32x4 c[3];
  c[0] = row[p];
  c[1] = row[p + 32];
  c[2] = row[p < 8 ? p + 64 : p];
  const bool has2 = p < 8;
  float v[24];
#pragma unroll
  for (int i = 0; i < 3; ++i)
#pragma unroll
    for (int j = 0; j < 4; ++j) { v[i * 8 + 2 * j] = bf_lo(c[i][j]); v[i * 8 + 2 * j + 1] = bf_hi(c[i][j]); }
  float s = 0.f, s2 = 0.f;
#pragma unroll
  for (int j = 0; j < 16; ++j) s += v[j];
#pragma unroll
  for (int j = 16; j < 24; ++j) s2 += v[j];
  s += has2 ? s2 : 0.f;
  const float mean = reduce32(s) * (1.f / 576.f);
  float q = 0.f, q2 = 0.f;
#pragma unroll
  for (int j = 0; j < 24; ++j) v[j] -= mean;
#pragma unroll
  for (int j = 0; j < 16; ++j) q += v[j] * v[j];
#pragma unroll
  for (int j = 16; j < 24; ++j) q2 += v[j] * v[j];
  q += has2 ? q2 : 0.f;
  const float var = reduce32(q) * (1.f / 576.f);
  const float rstd = __builtin_amdgcn_rsqf(var + 1e-5f);
  const int T = tok >> 4, t16 = tok & 15;
#pragma unroll
  for (int i = 0; i < 3; ++i) {
    u32x4 o;
#pragma unroll
    for (int j = 0; j < 4; ++j) o[j] = pack2(v[i * 8 + 2 * j] * rstd, v[i * 8 + 2 * j + 1] * rstd);
    const int ch = p + 32 * i;
    if (i < 2 || has2) xf[((size_t)T * 18 + (ch >> 2)) * 64 + (ch & 3) * 16 + t16] = o;
  }
}

constexpr unsigned L1_BUFA = 0, L1_BUFB = 73728, L1_SCR = 147456, L1_TOTAL = 163840;
constexpr int AUX_SC1 = 16;
#define SB() __builtin_amdgcn_sched_barrier(0)
#define PIN(x) asm volatile("" : "+v"(x))

struct KFP {
  const void* xf; const void* wq; const void* wg; const void* wu; const void* wd; const void* wo; const void* x;
  void* hf; void* kv; void* xb; void* ctr; void* out;
};

struct K1S {
  u32x4 xf[2][18];
  s16x4 wlo[6], whi[6];
  f32x4 acc[2][2];
  unsigned gel[4][2][2];
  u32x4 hq[2];
  float t0, t1;
  unsigned ard;
  u32x4 kp[8];
  unsigned flag;
};
struct K1G { unsigned dst, soff, sstep, vo0, vo1, vok, kvso, scr, wm0, wvo, wvo2; rsrc_t rs, rsk, wrs, wrs2; int w; };
template <int I, int ROLE = 0> DI void k1_piece(const K1G& G) {
  if (!ABL1_NOSTAGE) glds16<ROLE>(G.dst + I * 1024u, (I & 1) ? G.vo1 : G.vo0, G.rs, G.soff + I * G.sstep);
}
template <int I, int ROLE = 0> DI void k1_piece2(const K1G& G, int l) {
  const int ci = 64 * I + l, rho = ci / 18, c = ci - 18 * rho;
  glds16<ROLE>(G.dst + I * 1024u, rho * 1152u + c * 16u, G.rs, G.soff);
}
template <int STRIDE, int N, int H> DI void k1_read(K1S& S, unsigned a0, int slot_unused = 0) {
  constexpr int P = N / 18, KS = N % 18;
  if constexpr (H == 0 && KS == 0) { S.ard = STRIDE == 4096 ? (a0 ^ (P << 5)) : a0 + P * 36864u; PIN(S.ard); }
  if constexpr (H == 0 && KS == 9) { S.ard += 9 * STRIDE; PIN(S.ard); }
  if constexpr (H == 0) S.wlo[N % 6] = trread(S.ard + (KS % 9) * STRIDE);
  else S.whi[N % 6] = trread(S.ard + (KS % 9) * STRIDE + STRIDE / 8);
}
template <int KIND, int U, int A, int PR, int PH, int SL> DI void k1_epi(K1S& S) {
  constexpr int T = PR >> 1, R = 2 * (PR & 1);
  const float a = S.acc[A][T][R], b = S.acc[A][T][R + 1];
  if (ABL1_NOEPI) {
    if (PH == 3 && SL == 1) { if (KIND == 1) S.gel[U][T][PR & 1] = pack2(a, b); else S.hq[T][2 * (U & 1) + (PR & 1)] = pack2(a, b) ^ S.gel[U][T][PR & 1]; }
    return;
  }
  if constexpr (KIND == 1) {
    if constexpr (PH == 0 && SL == 0) { S.t0 = a * a; S.t1 = b * b; }
    if constexpr (PH == 0 && SL == 1) {
      S.t0 = __builtin_fmaf(S.t0, -0.10294323970f, -2.30220819814f);
      S.t1 = __builtin_fmaf(S.t1, -0.10294323970f, -2.30220819814f);
    }
    if constexpr (PH == 1 && SL == 0) { S.t0 *= a; S.t1 *= b; }
    if constexpr (PH == 1 && SL == 1) { S.t0 = fexp2(S.t0); S.t1 = fexp2(S.t1); }
    if constexpr (PH == 2 && SL == 0) { S.t0 += 1.f; S.t1 += 1.f; }
    if constexpr (PH == 2 && SL == 1) { S.t0 = frcp(S.t0); S.t1 = frcp(S.t1); }
    if constexpr (PH == 3 && SL == 0) { S.t0 *= a; S.t1 *= b; }
    if constexpr (PH == 3 && SL == 1) { unsigned v = pack2(S.t0, S.t1); PIN(v); S.gel[U][T][PR & 1] = v; }
    else { PIN(S.t0); PIN(S.t1); }
  } else {
    if constexpr (PH == 1 && SL == 0) { const unsigned u = S.gel[U][T][PR & 1]; S.t0 = bf_lo(u); S.t1 = bf_hi(u); PIN(S.t0); PIN(S.t1); }
    if constexpr (PH == 2 && SL == 0) { S.t0 *= a; S.t1 *= b; PIN(S.t0); PIN(S.t1); }
    if constexpr (PH == 3 && SL == 0) { unsigned v = pack2(S.t0, S.t1); PIN(v); S.hq[T][2 * (U & 1) + (PR & 1)] = v; }
  }
}
template <int KIND, int U, int A, int I = 0> DI void k1_epi_all(K1S& S) {
  if constexpr (I < 32) { k1_epi<KIND, U, A, I / 8, (I / 2) % 4, I % 2>(S); k1_epi_all<KIND, U, A, I + 1>(S); }
}
DI void k1_store_pair(K1S& S, rsrc_t rsH, unsigned voff, unsigned soff) {
  bstore16_sc1<0>(S.hq[0], voff, rsH, soff);
  bstore16_sc1<1024>(S.hq[1], voff, rsH, soff);
}
template <int KIND, int PREV, int NP, int STRIDE, int NSTRIDE, int STG, int SIG, int N>
DI void k1_step(K1S& S, unsigned ab, unsigned abn, const K1G& G, rsrc_t rsH, unsigned hvoff, unsigned hso, unsigned hsop,
                gu32* sig, gu32* sig2) {
  constexpr int P = N / 18, KS = N % 18, A = P & 1, NT = NP * 18;
  constexpr int EK = P == 0 ? (PREV == 3 ? 2 : PREV) : (NP == 2 ? 1 : KIND);
  constexpr int EU = P == 0 ? (PREV == 3 ? 0 : 3) : (NP == 2 ? 0 : P - 1);
  constexpr int EA = P == 0 ? 1 : (A ^ 1);
  constexpr bool EPI = EK != 0 && KS >= 1 && KS <= 16;
  const f32x4 z4 = {0.f, 0.f, 0.f, 0.f};
  const s16x8 wf = __builtin_shufflevector(S.wlo[N % 6], S.whi[N % 6], 0, 1, 2, 3, 4, 5, 6, 7);
  const s16x8 x0 = __builtin_bit_cast(s16x8, S.xf[0][KS]), x1 = __builtin_bit_cast(s16x8, S.xf[1][KS]);
  if (!ABL1_NOCOMP) S.acc[A][0] = mfma16(wf, x0, KS == 0 ? z4 : S.acc[A][0]);
  else if (KS == 0) { S.acc[A][0] = z4; S.acc[A][0][0] = (float)wf[0]; }
  SB();
  if constexpr (N + 5 < NT) k1_read<STRIDE, N + 5, 0>(S, ab);
  else if constexpr (NSTRIDE != 0) k1_read<NSTRIDE == 0 ? 4096 : NSTRIDE, N + 5 - NT, 0>(S, abn);
  if constexpr (EPI) k1_epi<EK, EU, EA, (KS - 1) / 4, (KS - 1) % 4, 0>(S);
  SB();
  if (!ABL1_NOCOMP) S.acc[A][1] = mfma16(wf, x1, KS == 0 ? z4 : S.acc[A][1]);
  else if (KS == 0) { S.acc[A][1] = z4; }
  SB();
  if constexpr (N + 5 < NT) k1_read<STRIDE, N + 5, 1>(S, ab);
  else if constexpr (NSTRIDE != 0) k1_read<NSTRIDE == 0 ? 4096 : NSTRIDE, N + 5 - NT, 1>(S, abn);
  if constexpr (EPI) k1_epi<EK, EU, EA, (KS - 1) / 4, (KS - 1) % 4, 1>(S);
  constexpr int PS = WARM_MODE == 2 ? 3 : 6;
  if constexpr (STG == 1 && NP == 4 && N % PS == 2 && N / PS < 9) k1_piece<N / PS>(G);
  if constexpr (STG == 1 && NP == 2 && N % 2 == 0 && N >= 2 && N / 2 <= 9) k1_piece<N / 2 - 1>(G);
  if constexpr (SIG == 1 && N == 9) {
    if (threadIdx.x < 88) {
      f32x4 a = *LDSP(f32x4, G.scr + threadIdx.x * 16);
#pragma unroll
      for (int ww = 1; ww < 8; ++ww) a += *LDSP(f32x4, G.scr + ww * 1408u + threadIdx.x * 16);
      bstore16_sc1<0>(__builtin_bit_cast(u32x4, a), threadIdx.x * 16u, G.rsk, G.kvso);
    }
  }
  if constexpr (WARM_MODE == 2 && STG == 1 && N == (NP == 4 ? WARM_N : 22)) warm_lines(G.wm0, G.wvo, G.wrs);
  if constexpr (WARM_MODE == 1 && SIG == 1 && N == NT - 7) warm_lines(G.wm0, G.wvo, G.wrs);
  if constexpr (WARM_MODE == 1 && SIG == 1 && N == NT - 5) warm_lines(G.wm0, G.wvo2, G.wrs2);
  if constexpr (STG == 2 && N % 6 == 2 && N / 6 < 9) k1_piece2<N / 6>(G, hvoff >> 4);
  if constexpr (STG == 2 && N % 6 == 5 && N / 6 < 8) bload16_sc1<0>(S.kp[N / 6], G.vok, G.rsk, (N / 6) * 1408u);
  if constexpr (PREV == 2 && N == 17) k1_store_pair(S, rsH, hvoff, hsop);
  if constexpr (PREV == 3 && N == 17) {
#pragma unroll
    for (int T = 0; T < 2; ++T) {
      const u32x2 v = {S.hq[T][0], S.hq[T][1]};
      __builtin_amdgcn_raw_buffer_store_b64(v, rsH, hvoff + T * 1024u, hsop, AUX_SC1);
    }
  }
  if constexpr (KIND == 2 && NP == 4 && N == 2 * 18 + 17) k1_store_pair(S, rsH, hvoff, hso);
  if constexpr (N == 1) asm volatile("s_barrier" ::: "memory");
  if constexpr (SIG == 2 && N == NT - 8) { if (rfl(threadIdx.x >> 6) == 0) ctr_wait(sig, 32u); }
  if constexpr (SIG == 3 && N == 30) { if (G.w == 0) asm volatile("global_load_dword %0, %1, off sc1 ; role 3" : "=v"(S.flag) : "v"(sig2)); }
  if constexpr (SIG == 3 && N == NT - 8) {
    if (G.w == 0) {
      asm volatile("s_waitcnt vmcnt(9)" : "+v"(S.flag));
      if (rfl(S.flag) < 4u) ctr_wait(sig2, 4u);
    }
    asm volatile("s_barrier" ::: "memory");
  }
  if constexpr (NSTRIDE != 0 && N == NT - 8) wait_bar_nolgkm<(KIND == 2 ? 2 : 0) + (WARM_MODE == 2 ? 1 : 0)>();
  if constexpr (SIG == 1 && N == NT - 8) { if (threadIdx.x == 0) ctr_add(sig); }
  if constexpr (SIG == 2 && N == NT - 8) { if (threadIdx.x == 0) ctr_add(sig2); }
  SB();
  if constexpr (N + 1 < NT) k1_step<KIND, PREV, NP, STRIDE, NSTRIDE, STG, SIG, N + 1>(S, ab, abn, G, rsH, hvoff, hso, hsop, sig, sig2);
}
template <int STRIDE> DI void k1_first_reads(K1S& S, unsigned ab) {
  k1_read<STRIDE, 0, 0>(S, ab); k1_read<STRIDE, 0, 1>(S, ab); k1_read<STRIDE, 1, 0>(S, ab); k1_read<STRIDE, 1, 1>(S, ab);
  k1_read<STRIDE, 2, 0>(S, ab); k1_read<STRIDE, 2, 1>(S, ab); k1_read<STRIDE, 3, 0>(S, ab); k1_read<STRIDE, 3, 1>(S, ab);
  k1_read<STRIDE, 4, 0>(S, ab); k1_read<STRIDE, 4, 1>(S, ab);
  SB();
}
template <int KS> DI void k1_qkv_step(K1S& S, s16x8 (&wq)[2][4], f32x4 (&Q)[4][2], unsigned qa, unsigned xv, rsrc_t rsX,
                                      unsigned xsb, const K1G& G) {
  if constexpr (KS + 6 < 18) {
    bload16<((KS + 6) & 3) * 1024>(S.xf[0][KS + 6], xv, rsX, xsb + ((KS + 6) >> 2) * 4096u);
    bload16<((KS + 6) & 3) * 1024>(S.xf[1][KS + 6], xv, rsX, xsb + 18432u + ((KS + 6) >> 2) * 4096u);
  } else if constexpr (KS < 15) { k1_piece<2 * (KS - 12)>(G); k1_piece<2 * (KS - 12) + 1>(G); }
  else k1_piece<KS - 9>(G);
  constexpr int YOUNGER = (ABL1_NOSTAGE ? (KS < 12 ? 12 : 2 * (17 - KS)) : (KS < 15 ? 12 : 26 - KS)) + (KS < 6 && !NO_WARM0 ? 2 : 0);
  wait_vm2<YOUNGER>(S.xf[0][KS], S.xf[1][KS]);
  const s16x8 x0 = __builtin_bit_cast(s16x8, S.xf[0][KS]), x1 = __builtin_bit_cast(s16x8, S.xf[1][KS]);
  SB();
#pragma unroll
  for (int X = 0; X < 4; ++X) {
    Q[X][0] = mfma16(x0, wq[KS & 1][X], Q[X][0]);
    SB();
    if constexpr (KS + 1 < 18) wq[(KS + 1) & 1][X] = *LDSP(s16x8, qa + ((KS + 1) * 4 + X) * 1024u);
    SB();
    Q[X][1] = mfma16(x1, wq[KS & 1][X], Q[X][1]);
    SB();
  }
  if constexpr (KS + 1 < 18) k1_qkv_step<KS + 1>(S, wq, Q, qa, xv, rsX, xsb, G);
}

constexpr int K2D = 5;
constexpr unsigned L2_SLOT = 9216, L2_NS = K2D + 2;
constexpr unsigned L2_WO = L1_BUFA + 4 * L2_SLOT;
constexpr unsigned L2_MT = L1_BUFB + 3 * L2_SLOT, L2_KV = L1_SCR;
constexpr unsigned L2_OWN = L1_BUFB, L2_OUT = L1_BUFA;

struct K2S {
  u32x4 hr[K2D + 1][2];
  u32x4 ha[3][2];
  s16x4 wlo[6], whi[6];
  f32x4 acc[9][2];
  unsigned flag, ardm;
  unsigned ard;
};
struct K2C {
  rsrc_t rsH, rsW, rsB;
  unsigned vo0, vo1, hvoff, hsoF, hsoA, wsoF, xso, lb, lanec;
  int w, sp;
  gu32* chf;
  gu32* cxa;
};
constexpr bool k2_att(int n) { return n >= 18; }
constexpr unsigned k2_loc(int n) {
  return n < 8 ? 4 * (n >> 1) + (n & 1) : n < 10 ? 8 + n : 4 * ((n - 10) >> 1) + 2 + (n & 1);
}
constexpr unsigned k2_slot(int s) { return s < 4 ? L1_BUFA + s * L2_SLOT : L1_BUFB + (s - 4) * L2_SLOT; }
constexpr int k2_tW(int m) { return m == 4 ? -30 : 16 * (m - 5); }
constexpr int k2_tH(int m, int half) {
  return m <= 2 ? -56 + 2 * m + half : m <= 4 ? -44 + 2 * m + half : 16 * (m - 5) + 5 + 2 * half;
}
constexpr int k2_tA(int i) { return 16 * (6 + i) + 8; }
constexpr int k2_younger(int t0, int n) {
  int cnt = 0;
  const int tw = 16 * n + 3;
  for (int m = 4; m <= 17; ++m) if (k2_tW(m) > t0 && k2_tW(m) < tw) ++cnt;
  for (int m = 0; m <= 17; ++m)
    for (int h = 0; h < 2; ++h) if (k2_tH(m, h) > t0 && k2_tH(m, h) < tw) ++cnt;
  for (int i = 0; i < 6; ++i) if (k2_tA(i) > t0 && k2_tA(i) < tw) ++cnt;
  for (int i = 0; i < 4; ++i) if (-44 + i > t0) ++cnt;
  return cnt;
}
constexpr int k2_count(int n) {
  const int m = n + 1, th = k2_tH(m, 1), tw = m >= 4 ? k2_tW(m) : -100;
  return k2_younger(th > tw ? th : tw, n);
}
static_assert(k2_count(0) == 12 && k2_count(1) == 13 && k2_count(2) == 10 && k2_count(3) == 10 && k2_count(8) == 12 && k2_count(13) == 12 && k2_younger(-41, 1) == 9, "schedule");
static_assert(k2_younger(16 * 3 + 4, 5) == 6, "schedule");
template <int T, int I, int ROLE = 0> DI void k2_issue(K2S& S, const K2C& C) {
  if constexpr (T <= 17) {
    constexpr unsigned ks = k2_loc(T);
    const unsigned wso = ABL2_NOW ? 0xf0000000u : C.wsoF + ks * 36864u, hso = ABL2_NOH ? 0xf0000000u : C.hsoF + ks * 131072u;
    if constexpr (I == 0) glds16<ROLE>(C.lb + k2_slot(T % L2_NS) + C.w * 1024u, C.vo0, C.rsW, wso);
    if constexpr (I == 1) { if ((T & 7) == C.w) glds16<3>(C.lb + k2_slot(T % L2_NS) + 8192u, C.vo1, C.rsW, wso); }
    if constexpr (I == 2) bload16_sc1<0, ROLE>(S.hr[T % (K2D + 1)][0], C.hvoff, C.rsH, hso);
    if constexpr (I == 3) bload16_sc1<1024, ROLE>(S.hr[T % (K2D + 1)][1], C.hvoff, C.rsH, hso);
  }
}
constexpr int K2_SEQ = 135;
constexpr int k2_T(int M) { return M < K2_SEQ ? M / 9 : 15 + (M - K2_SEQ) % 6; }
constexpr int k2_c(int M) { return M < K2_SEQ ? M % 9 : (M - K2_SEQ) / 6; }
template <int M, int H> DI void k2_read(K2S& S, const K2C& C) {
  if constexpr (M < 189) {
    constexpr int T = k2_T(M), c = k2_c(M);
    if constexpr (M < K2_SEQ) {
      if constexpr (H == 0 && c == 0) { S.ard = C.lanec + k2_slot(T % L2_NS); PIN(S.ard); }
      if constexpr (H == 0) S.wlo[M % 6] = trread(S.ard + c * 32);
      else S.whi[M % 6] = trread(S.ard + c * 32 + 4608);
    } else {
      static_assert(k2_slot(16 % L2_NS) == k2_slot(15 % L2_NS) + L2_SLOT && k2_slot(17 % L2_NS) == k2_slot(15 % L2_NS) + 2 * L2_SLOT, "ring");
      if constexpr (H == 0 && M == K2_SEQ) { S.ard = C.lanec + k2_slot(15 % L2_NS); PIN(S.ard); S.ardm = C.lanec + L2_MT; PIN(S.ardm); }
      const unsigned a = T <= 17 ? S.ard + (T - 15) * L2_SLOT + c * 32 : S.ardm + (T - 18) * L2_SLOT + c * 32;
      if constexpr (H == 0) S.wlo[M % 6] = trread(a);
      else S.whi[M % 6] = trread(a + 4608);
    }
  }
}
template <int c> DI void k2_send(K2S& S, const K2C& C) {
  if (C.w != C.sp) {
    const u32x4 v = {pack2(S.acc[c][0][0], S.acc[c][0][1]), pack2(S.acc[c][0][2], S.acc[c][0][3]),
                     pack2(S.acc[c][1][0], S.acc[c][1][1]), pack2(S.acc[c][1][2], S.acc[c][1][3])};
    bstore16_sc1<0>(v, C.hvoff, C.rsB, C.xso + c * 1024u);
  } else {
    *LDSP(f32x4, C.lb + L2_OWN + (c * 2) * 1024u + C.hvoff) = S.acc[c][0];
    *LDSP(f32x4, C.lb + L2_OWN + (c * 2 + 1) * 1024u + C.hvoff) = S.acc[c][1];
  }
}
template <int T, int N> DI void k2_wait(K2S& S) {
  asm volatile("s_waitcnt vmcnt(%2)\n\ts_barrier" : "+v"(S.hr[T % (K2D + 1)][0]), "+v"(S.hr[T % (K2D + 1)][1]) : "i"(N) : "memory");
}
template <int M> DI void k2_step(K2S& S, const K2C& C) {
  constexpr int T = k2_T(M), c = k2_c(M);
  constexpr bool seq = M < K2_SEQ;
  const s16x8 wf = __builtin_shufflevector(S.wlo[M % 6], S.whi[M % 6], 0, 1, 2, 3, 4, 5, 6, 7);
  const s16x8 h0 = __builtin_bit_cast(s16x8, k2_att(T) ? S.ha[k2_att(T) ? T - 18 : 0][0] : S.hr[T % (K2D + 1)][0]);
  const s16x8 h1 = __builtin_bit_cast(s16x8, k2_att(T) ? S.ha[k2_att(T) ? T - 18 : 0][1] : S.hr[T % (K2D + 1)][1]);
  const f32x4 z4 = {0.f, 0.f, 0.f, 0.f};
  S.acc[c][0] = mfma16(wf, h0, T == 0 ? z4 : S.acc[c][0]);
  SB();
  k2_read<M + 5, 0>(S, C);
  SB();
  S.acc[c][1] = mfma16(wf, h1, T == 0 ? z4 : S.acc[c][1]);
  SB();
  k2_read<M + 5, 1>(S, C);
  if constexpr (seq) {
    if constexpr (T + K2D <= 17) {
      if constexpr (c == 0) { if (!STAGGER || C.w < 4) k2_issue<T + K2D, 0, STAGGER>(S, C); }
      if constexpr (STAGGER && c == 1) { if (C.w >= 4) k2_issue<T + K2D, 0, 2>(S, C); }
      if constexpr (c == 2) k2_issue<T + K2D, 1>(S, C);
      if constexpr (c == (STAGGER ? 4 : 5)) { if (!STAGGER || C.w < 4) k2_issue<T + K2D, 2, STAGGER>(S, C); }
      if constexpr (STAGGER && c == 5) { if (C.w >= 4) k2_issue<T + K2D, 2, 2>(S, C); }
      if constexpr (c == (STAGGER ? 6 : 7)) { if (!STAGGER || C.w < 4) k2_issue<T + K2D, 3, STAGGER>(S, C); }
      if constexpr (STAGGER && c == 7) { if (C.w >= 4) k2_issue<T + K2D, 3, 2>(S, C); }
    }
    if constexpr (T >= 6 && T <= 11 && c == 8)
      bload16_sc1<((T - 6) & 1) * 1024>(S.ha[(T - 6) >> 1][(T - 6) & 1], C.hvoff, C.rsH, C.hsoA + ((T - 6) >> 1) * 131072u);
    if constexpr (T == 3 && c == 4) {
      if (C.w == 0) asm volatile("global_load_dword %0, %1, off sc1 ; role 3" : "=v"(S.flag) : "v"(C.chf));
    }
    if constexpr (T == 5 && c == 3) {
      if (C.w == 0) {
        asm volatile("s_waitcnt vmcnt(%1)" : "+v"(S.flag) : "i"(k2_younger(16 * 3 + 4, 5) - (STAGGER ? 1 : 0)));
        if (rfl(S.flag) < 4u) ctr_wait(C.chf, 4u);
      }
    }
    if constexpr (c == 3 && T < 14) k2_wait<T + 1, T == 1 ? k2_younger(-41, 1) : k2_count(T)>(S);
    if constexpr (c == 3 && T == 1) { if (threadIdx.x == 0) ctr_add(C.chf); }
    if constexpr (c == 2 && T == 14)
      asm volatile("s_waitcnt vmcnt(0)"
                   : "+v"(S.ha[0][0]), "+v"(S.ha[0][1]), "+v"(S.ha[1][0]), "+v"(S.ha[1][1]), "+v"(S.ha[2][0]), "+v"(S.ha[2][1]),
                     "+v"(S.hr[15 % (K2D + 1)][0]), "+v"(S.hr[15 % (K2D + 1)][1]), "+v"(S.hr[16 % (K2D + 1)][0]), "+v"(S.hr[16 % (K2D + 1)][1]),
                     "+v"(S.hr[17 % (K2D + 1)][0]), "+v"(S.hr[17 % (K2D + 1)][1]));
    if constexpr (c == 3 && T == 14) asm volatile("s_barrier" ::: "memory");
  } else {
    if constexpr (T == 20 && c > 0) k2_send<c - 1>(S, C);
    if constexpr (T == 15 && c == 7) {
      asm volatile("s_waitcnt vmcnt(%0)\n\ts_barrier" :: "i"(6 - XA) : "memory");
      if (C.w == 0 && (C.hvoff >> 4) < 8) ctr_add(C.cxa + (C.hvoff >> 4) * 32);
    }
  }
  SB();
  if constexpr (seq && c == 8 && T % 5 == 4) { STAMP(S.dg, 18 + T / 5) }
  if constexpr (M + 1 < 189) k2_step<M + 1>(S, C);
}

__global__ __launch_bounds__(512, 2) void kf_main(KFP p) {
  __shared__ __attribute__((aligned(128))) char smem[L1_TOTAL];
  const unsigned lb = (unsigned)(size_t)(__attribute__((address_space(3))) char*)smem;
  const int tid = threadIdx.x, l = tid & 63;
  const int w = rfl(tid >> 6);
  const int b = blockIdx.x, xc = b & 7, r = b >> 3;
  const int cr = r & 3, j = 4 * xc + cr, ib = r >> 2;
  const int g = l >> 4, q = (l & 15) >> 2, pp = l & 3, c16 = l & 15;

  STAMPR(p.dg, 30) STAMP(p.dg, 0)
  const rsrc_t rsX = mk_rsrc(p.xf, (unsigned)SZ_XF);
  const rsrc_t rsQ = mk_rsrc(p.wq, (unsigned)SZ_WQ);
  const rsrc_t rsG = mk_rsrc(p.wg, D_ * F_ * 2);
  const rsrc_t rsU = mk_rsrc(p.wu, D_ * F_ * 2);
  const rsrc_t rsH = mk_rsrc(p.hf, (unsigned)SZ_HF);
  const rsrc_t rsB = mk_rsrc(p.xb, (unsigned)SZ_XB);
  const unsigned xso = (((ib * 4 + cr) * 8 + w) * 8 + xc) * 9216u;
  gu32* const ctr = (gu32*)p.ctr;
  if (PRIO == 1 && w >= 4) __builtin_amdgcn_s_setprio(1);
  if (PRIO == 2 && w < 4) __builtin_amdgcn_s_setprio(1);
  K1S S;

  if (!NO_CODEWARM) {
    unsigned long long pc;
    unsigned len;
    asm volatile("s_getpc_b64 %0\n98764:\n\ts_mov_b32 %1, 98765f-98764b" : "=s"(pc), "=s"(len));
    warm_lines(lb + L1_SCR + 15872u, l < 2 ? ((r * 8 + w) * 2 + l) * 128u : 0xf0000000u, mk_rsrc((const void*)pc, len));
  }

#pragma unroll
  for (int i = 0; i < 9; ++i) glds16(lb + L1_BUFA + (9 * w + i) * 1024u, l * 16u, rsQ, (j * 72u + 9 * w + i) * 1024u);
  const int T0 = 16 * ib + 2 * w;
  const unsigned hvoff = l * 16u, xsb = T0 * 18432u;
#define XL(s) bload16<((s) & 3) * 1024>(S.xf[0][s], hvoff, rsX, xsb + ((s) >> 2) * 4096u); \
              bload16<((s) & 3) * 1024>(S.xf[1][s], hvoff, rsX, xsb + 18432u + ((s) >> 2) * 4096u);
  XL(0) XL(1) XL(2) XL(3) XL(4) XL(5)
#undef XL
  const unsigned wcol = (576u * xc + 128u * cr) * 2u;
  if (!NO_WARM0) {
    const unsigned si = 3 * w + (l - 9);
    const unsigned vo = l < 9 ? (72u * ib + 9u * w + l) * 9216u + wcol
                      : (l < 12 && si < 18 ? (18u * r + si) * 9216u + (576u * xc + 512u) * 2u : 0xf0000000u);
    warm_lines(lb + L1_SCR + 15872u, vo, rsG);
    warm_lines(lb + L1_SCR + 15872u, vo, rsU);
  }
  K1G G;
  G.w = w;
  {
    const unsigned rowv = (l >> 3) * 9216u, c8 = l & 7, s0 = (l >> 4) & 1;
    const unsigned vA = rowv + ((c8 ^ (s0 << 1)) << 4), vB = rowv + ((c8 ^ ((s0 | 2) << 1)) << 4);
    G.vo0 = (w & 1) ? vB : vA;
    G.vo1 = (w & 1) ? vA : vB;
  }
  G.sstep = 8u * 9216u;
  const unsigned wsoff = 72u * w * 9216u, wdst = lb + 72u * w * 128u;
  G.rs = rsG; G.soff = wsoff + wcol; G.dst = wdst + L1_BUFB;

  STAMP(p.dg, 1)
  wait_bar<NO_WARM0 ? 12 : 14>();
  STAMP(p.dg, 2)

  {
    const unsigned qa = lb + L1_BUFA + l * 16u;
    const f32x4 z4 = {0.f, 0.f, 0.f, 0.f};
    f32x4 Q[4][2];
#pragma unroll
    for (int X = 0; X < 4; ++X) Q[X][0] = Q[X][1] = z4;
    s16x8 wq[2][4];
#pragma unroll
    for (int X = 0; X < 4; ++X) wq[0][X] = *LDSP(s16x8, qa + X * 1024u);
    k1_qkv_step<0>(S, wq, Q, qa, hvoff, rsX, xsb, G);
    STAMP(p.dg, 3)
    unsigned fKA[4], fKB[4], fVA[4], fVB[4], fQ2[4];
#pragma unroll
    for (int T = 0; T < 2; ++T)
#pragma unroll
      for (int h2 = 0; h2 < 2; ++h2) {
        fKA[2 * T + h2] = pack2(phi_f(Q[0][T][2 * h2]), phi_f(Q[0][T][2 * h2 + 1]));
        fKB[2 * T + h2] = pack2(phi_f(Q[1][T][2 * h2]), phi_f(Q[1][T][2 * h2 + 1]));
        fVA[2 * T + h2] = pack2(Q[2][T][2 * h2], Q[2][T][2 * h2 + 1]);
        const unsigned raw = pack2(Q[3][T][2 * h2], Q[3][T][2 * h2 + 1]);
        fVB[2 * T + h2] = c16 < 2 ? 0x3f803f80u : raw;
        fQ2[2 * T + h2] = pack2(phi_f(Q[3][T][2 * h2]), phi_f(Q[3][T][2 * h2 + 1]));
      }
    const s16x8 aKA = mk_frag(fKA[0], fKA[1], fKA[2], fKA[3]), aKB = mk_frag(fKB[0], fKB[1], fKB[2], fKB[3]);
    const s16x8 bVA = mk_frag(fVA[0], fVA[1], fVA[2], fVA[3]), bVB = mk_frag(fVB[0], fVB[1], fVB[2], fVB[3]);
    const s16x8 aQ2 = mk_frag(fQ2[0], fQ2[1], fQ2[2], fQ2[3]);
    const f32x4 R0 = mfma16(aKA, bVA, z4), R1 = mfma16(aKA, bVB, z4);
    const f32x4 R2 = mfma16(aKB, bVA, z4), R3 = mfma16(aKB, bVB, z4);
    {
      const unsigned one = (c16 >> 2) == g ? (0x3f80u << (16 * (c16 & 1))) : 0u;
      const unsigned i0 = (c16 & 2) ? 0u : one, i1 = (c16 & 2) ? one : 0u;
#pragma unroll
      for (int T = 0; T < 2; ++T) {
        const s16x8 id = T == 0 ? mk_frag(i0, i1, 0u, 0u) : mk_frag(0u, 0u, i0, i1);
        const f32x4 qa4 = mfma16(aKB, id, z4), qb4 = mfma16(aQ2, id, z4);
        const unsigned b01 = pack2(qb4[0], qb4[1]), b23 = pack2(qb4[2], qb4[3]);
        bstore16_sc1<0>(u32x4{pack2(qa4[0], qa4[1]), pack2(qa4[2], qa4[3]), g == 1 ? b01 : (g == 2 ? b23 : 0u), g == 1 ? b23 : 0u},
                        hvoff + T * 1024u, rsB, xso);
      }
    }
    {
      const unsigned sb = lb + L1_SCR + w * 1408u;
      const int eB = c16 == 0 ? 18 : c16 + 14;
      const bool okB = c16 == 0 || c16 == 2 || c16 == 3;
      const unsigned dummy = lb + L1_SCR + 8 * 1408u + l * 4u;
#pragma unroll
      for (int rr = 0; rr < 4; ++rr) {
        const int d = 4 * g + rr;
        *LDSP(float, sb + (d * 19 + c16) * 4) = R0[rr];
        *LDSP(float, okB ? sb + (d * 19 + eB) * 4 : dummy) = R1[rr];
        if (rr >= 2) {
          *LDSP(float, g == 0 ? sb + ((14 + rr) * 19 + c16) * 4 : dummy) = R2[rr];
          *LDSP(float, g == 0 && okB ? sb + ((14 + rr) * 19 + eB) * 4 : dummy) = R3[rr];
        }
      }
    }
  }
  STAMP(p.dg, 4)
  wait_bar<2>();
  STAMP(p.dg, 5)

  const unsigned lane0 = lb + (8 * g + q) * 128u + 8u * pp + ((((q >> 1) & 1) | ((g & 1) << 1)) << 5);
  const unsigned abA = lane0 + L1_BUFA, abB = lane0 + L1_BUFB;
  const unsigned ks0 = 18u * xc + 4u * cr;
  const unsigned hs0 = (ks0 * 128u + T0) * 1024u;

  k1_first_reads<4096>(S, abB);
  const rsrc_t rsKV = mk_rsrc(p.kv, (unsigned)SZ_KV);
  G.rsk = rsKV; G.kvso = (j * 8 + ib) * 1408u; G.scr = lb + L1_SCR;
  const rsrc_t rsW = mk_rsrc(p.wd, F_ * D_ * 2), rsO = mk_rsrc(p.wo, D_ * D_ * 2);
  constexpr unsigned OOB = 0xf0000000u;
  const unsigned wdl = (576u * xc * 9u + r * 162u) * 128u;
  const unsigned win1 = (72u * ib + 18u * (w & 3) + l) * 9216u + wcol + 128u;
  G.wm0 = lb + L1_SCR + 15872u;
  if (WARM_MODE == 1) {
    G.wvo = l < 9 ? (72u * ib + 9u * w + l) * 9216u + wcol + 128u : OOB;
    G.wrs = rsG;
    G.wvo2 = w < 3 ? (l < 54 ? wdl + (w * 54u + l) * 128u : OOB)
           : w == 3 ? (l < 21 && r * 21 + l < 648 ? (72u * xc * 9u + r * 21u + l) * 128u : OOB) : (l < 18 ? win1 : OOB);
    G.wrs2 = w < 3 ? rsW : w == 3 ? rsO : rsU;
  } else {
    const unsigned sl = (18u * r + 5u * (w & 3) + l) * 9216u + (576u * xc + 512u) * 2u, su = (18u * r + 9u * (w & 1) + l) * 9216u + (576u * xc + 512u) * 2u;
    G.wvo = w < 4 ? (l < 5 && 5 * w + l < 18 ? sl : OOB) : w < 6 ? (l < 9 ? su : OOB) : (l < 27 ? wdl + (27u * (w - 6) + l) * 128u : OOB);
    G.wrs = w < 4 ? rsG : w < 6 ? rsU : rsW;
  }
  gu32* const ckv = ctr + (CT_KV + xc) * 32;
  const unsigned gvo0 = G.vo0, gvo1 = G.vo1;
  G.rs = rsU; G.soff = wsoff + wcol; G.dst = wdst + L1_BUFA;
  gu32* const chA = ctr + (CT_H + xc * 8 + ib) * 32;
  k1_step<1, 0, 4, 4096, 4096, 1, 1, 0>(S, abB, abA, G, rsH, hvoff, 0, 0, ckv, chA);
  STAMP(p.dg, 6)
  G.rs = (w >> 2) ? rsU : rsG;
  G.vo0 = G.vo1 = (l >> 2) * 9216u + (((l & 3) ^ ((l >> 5) << 1)) * 16u);
  G.sstep = 16u * 9216u;
  G.soff = 144u * (w & 3) * 9216u + (576u * xc + 512u + 32u * (cr >> 1)) * 2u;
  G.dst = lb + L1_BUFB + (w >> 2) * 36864u + 144u * (w & 3) * 64u;
  const unsigned abS = lb + L1_BUFB + (8 * g + q) * 64u + 8u * pp + (((cr & 1) ^ (g & 1)) << 5);
  if (WARM_MODE == 2) {
    G.wvo = w < 4 ? (l < 18 ? win1 : OOB) : (l < 14 && 14 * (w - 4) + l < 54 ? wdl + (54u + 14u * (w - 4) + l) * 128u : OOB);
    G.wrs = w < 4 ? rsG : rsW;
  }
  k1_step<2, 1, 4, 4096, 2048, 1, 0, 0>(S, abA, abS, G, rsH, hvoff, hs0, 0, ckv, chA);
  STAMP(p.dg, 8)
  G.rs = rsG; G.vo0 = gvo0; G.vo1 = gvo1; G.sstep = 8u * 9216u; G.soff = wsoff + wcol + 128u; G.dst = wdst + L1_BUFA;
  if (WARM_MODE == 2) { G.wvo = w < 4 && l < 18 ? win1 : OOB; G.wrs = rsU; }
  k1_step<0, 2, 2, 2048, 4096, 1, 0, 0>(S, abS, abA, G, rsH, hvoff, 0, hs0 + 131072u, ckv, chA);
  STAMP(p.dg, 10)
  G.rs = rsU; G.soff = wsoff + wcol + 128u; G.dst = wdst + L1_BUFB;
  if (WARM_MODE == 2) {
    G.wvo = w < 2 ? (l < 27 ? wdl + (108u + 27u * w + l) * 128u : OOB)
                  : (w == 2 && l < 21 && r * 21 + l < 648 ? (72u * xc * 9u + r * 21u + l) * 128u : OOB);
    G.wrs = w < 2 ? rsW : rsO;
  }
  k1_step<1, 3, 4, 4096, 4096, 1, 2, 0>(S, abA, abB, G, rsH, hvoff, 0,
                                        ((18u * xc + 16u + (cr >> 1)) * 128u + T0) * 1024u + (cr & 1) * 8u, ckv, chA);
  STAMP(p.dg, 12)
  const unsigned wsoF = 18u * xc * 36864u + cr * 288u;
  G.rs = w < 4 ? rsW : rsO;
  G.soff = w < 4 ? wsoF + (4 * (w >> 1) + (w & 1)) * 36864u : 18u * (4 * xc + w - 4) * 1152u + cr * 288u;
  G.dst = lb + L1_BUFA + w * L2_SLOT;
  { const unsigned hh = tid / 88, q4 = tid - 88 * hh; G.vok = tid < 352 ? ((4 * xc + hh) * 8 * 352 + q4 * 4) * 4u : 0xf0000000u; }
  k1_step<2, 1, 4, 4096, 0, 2, 3, 0>(S, abB, 0, G, rsH, hvoff, hs0 + 2 * 131072u, 0, ckv, chA);

  K2S S2;
  K2C C;
  C.rsH = rsH; C.rsW = rsW; C.w = w; C.sp = xc; C.lb = lb;
  C.rsB = rsB;
  C.xso = xso;
  C.cxa = ctr + (CT_X + (ib * 4 + cr) * 8) * 32;
  C.chf = ctr + (CT_H2 + xc * 8 + ib) * 32;
  { const int ci = 64 * w + l, rho = ci / 18, c = ci - 18 * rho; C.vo0 = rho * 1152u + c * 16u; }
  { const int ci = 512 + l, rho = ci / 18, c = ci - 18 * rho; C.vo1 = rho * 1152u + c * 16u; }
  C.hvoff = hvoff;
  const unsigned hT = T0 * 1024u;
  C.hsoF = hT + 18u * xc * 131072u;
  C.hsoA = hT + (KS_FFN + 3u * xc) * 131072u;
  C.wsoF = wsoF;
  C.lanec = lb + (4 * g + q) * 288u + 8u * pp;

  asm volatile("s_waitcnt vmcnt(2)" : "+v"(S.kp[0]), "+v"(S.kp[1]), "+v"(S.kp[2]), "+v"(S.kp[3]), "+v"(S.kp[4]), "+v"(S.kp[5]), "+v"(S.kp[6]), "+v"(S.kp[7]));
  {
    f32x4 a = __builtin_bit_cast(f32x4, S.kp[0]);
#pragma unroll
    for (int i = 1; i < 8; ++i) a += __builtin_bit_cast(f32x4, S.kp[i]);
    if (tid < 352) *LDSP(f32x4, lb + L2_KV + tid * 16u) = a;
  }
  k1_epi_all<2, 3, 1>(S);
  k1_store_pair(S, rsH, hvoff, hs0 + 3 * 131072u);
  u32x4 pqv[2];
  bload16_sc1<0>(pqv[0], hvoff, rsB, xso);
  bload16_sc1<1024>(pqv[1], hvoff, rsB, xso);
  k2_issue<0, 2>(S2, C); k2_issue<0, 3>(S2, C);
  STAMP(p.dg, 14) STAMPR(p.dg, 27)
  asm volatile("s_waitcnt lgkmcnt(0)\n\ts_barrier" ::: "memory");
  STAMP(p.dg, 15)
  const f32x4 z4 = {0.f, 0.f, 0.f, 0.f};
  {
    const int hh = w >> 1, d = 16 * (w & 1) + c16;
    const unsigned ao = lb + L2_WO + hh * L2_SLOT + (8 * g + q) * 288u + 8u * pp;
    float v[8];
    const unsigned ka0 = lb + L2_KV + (hh * 352 + (d < 18 ? d : 0) * 19 + 8 * (g < 2 ? g : 2)) * 4;
#pragma unroll
    for (int e = 0; e < 8; ++e) v[e] = *LDSP(float, ka0 + e * 4);
    s16x8 A[9];
#pragma unroll
    for (int c = 0; c < 9; ++c) A[c] = trfrag(ao + c * 32, ao + c * 32 + 4 * 288);
    unsigned bh[4];
#pragma unroll
    for (int e2 = 0; e2 < 4; ++e2) {
      const float v0 = (d < 18 && 8 * g + 2 * e2 < 18) ? v[2 * e2] : 0.f, v1 = (d < 18 && 8 * g + 2 * e2 + 1 < 18) ? v[2 * e2 + 1] : 0.f;
      bh[e2] = pack2(v0, v1);
    }
    const s16x8 Bh = mk_frag(bh[0], bh[1], bh[2], bh[3]);
    const int ka = 24 * hh + d;
    const unsigned mo = d < 24 ? lb + L2_MT + (ka >> 5) * L2_SLOT + (ka & 31) * 288u + 8u * g : lb + k2_slot(L2_NS - 1) + l * 8u;
    f32x4 D[9];
#pragma unroll
    for (int c = 0; c < 9; ++c) D[c] = mfma16(A[c], Bh, z4);
#pragma unroll
    for (int c = 0; c < 9; ++c) *LDSP(u32x2, mo + c * 32) = u32x2{pack2(D[c][0], D[c][1]), pack2(D[c][2], D[c][3])};
  }
  SB();
  k2_issue<1, 2>(S2, C); k2_issue<1, 3>(S2, C); k2_issue<2, 2>(S2, C); k2_issue<2, 3>(S2, C);
  STAMP(p.dg, 16)
  {
    float za[4], zb[4];
#pragma unroll
    for (int e = 0; e < 4; ++e) {
      const int da = 4 * (g - 1) + e, db = da + 12;
      const float ta = *LDSP(float, lb + L2_KV + (cr * 352 + (g > 0 ? da * 19 + 18 : 0)) * 4);
      const float tb = *LDSP(float, lb + L2_KV + (cr * 352 + (g > 0 && db < 18 ? db * 19 + 18 : 0)) * 4);
      za[e] = g > 0 ? ta : 0.f;
      zb[e] = g > 0 && db < 18 ? tb : 0.f;
    }
    const int piA = 6 * cr + g - 1, piB = 6 * cr + g + 2;
    const unsigned base = (KS_FFN + 3u * xc) * 131072u + T0 * 1024u + c16 * 16u;
    const unsigned oA = g == 0 ? 0xf0000000u : base + (piA >> 3) * 131072u + (piA & 3) * 256u + ((piA >> 2) & 1) * 8u;
    const unsigned oB = g == 0 ? 0xf0000000u : base + (piB >> 3) * 131072u + (piB & 3) * 256u + ((piB >> 2) & 1) * 8u;
    asm volatile("s_waitcnt vmcnt(6)" : "+v"(pqv[0]), "+v"(pqv[1]));
#pragma unroll
    for (int T = 0; T < 2; ++T) {
      const u32x4 u = pqv[T];
      const float v[8] = {bf_lo(u[0]), bf_hi(u[0]), bf_lo(u[1]), bf_hi(u[1]), bf_lo(u[2]), bf_hi(u[2]), bf_lo(u[3]), bf_hi(u[3])};
      float den = 0.f;
#pragma unroll
      for (int e = 0; e < 4; ++e) den += v[e] * za[e] + v[4 + e] * zb[e];
      {
        const unsigned d0 = __float_as_uint(den);
        const auto r16 = __builtin_amdgcn_permlane16_swap(d0, d0, false, false);
        const unsigned r0 = r16[0], r1 = r16[1];
        const unsigned d1 = __float_as_uint(__uint_as_float(r0) + __uint_as_float(r1));
        const auto r32 = __builtin_amdgcn_permlane32_swap(d1, d1, false, false);
        const unsigned s0 = r32[0], s1 = r32[1];
        den = __uint_as_float(s0) + __uint_as_float(s1);
      }
      const float inv = frcp(den + 1e-6f);
      const u32x2 va = {pack2(v[0] * inv, v[1] * inv), pack2(v[2] * inv, v[3] * inv)};
      const u32x2 vb = {pack2(v[4] * inv, v[5] * inv), pack2(v[6] * inv, v[7] * inv)};
      __builtin_amdgcn_raw_buffer_store_b64(va, rsH, oA + T * 1024u, 0, AUX_SC1);
      __builtin_amdgcn_raw_buffer_store_b64(vb, rsH, oB + T * 1024u, 0, AUX_SC1);
    }
  }
  SB();
  k2_issue<3, 2>(S2, C); k2_issue<3, 3>(S2, C); k2_issue<4, 2>(S2, C); k2_issue<4, 3>(S2, C);
  k2_issue<4, 0>(S2, C); k2_issue<4, 1>(S2, C);
  asm volatile("s_waitcnt lgkmcnt(0)\n\ts_barrier" ::: "memory");
  STAMP(p.dg, 17)
  asm volatile("s_waitcnt vmcnt(13)" : "+v"(S2.hr[0][0]), "+v"(S2.hr[0][1]));
  k2_read<0, 0>(S2, C); k2_read<0, 1>(S2, C); k2_read<1, 0>(S2, C); k2_read<1, 1>(S2, C); k2_read<2, 0>(S2, C);
  k2_read<2, 1>(S2, C); k2_read<3, 0>(S2, C); k2_read<3, 1>(S2, C); k2_read<4, 0>(S2, C); k2_read<4, 1>(S2, C);
  SB();
  k2_step<0>(S2, C);
  STAMP(p.dg, 23) STAMPR(p.dg, 28)

  const int sp = xc, ni = cr, mi = ib;
  const int grp = mi * 4 + ni;
  k2_send<8>(S2, C);
  const int tok0 = 256 * mi + 32 * sp;
  const rsrc_t rsXr = mk_rsrc(p.x, S_ * D_ * 2);
  u32x4 xr[2];
  unsigned xo[2];
#pragma unroll
  for (int i = 0; i < 2; ++i) {
    const int it = tid + 512 * i, tk = it / 18, ch = it - tk * 18;
    xo[i] = it < 576 ? ((tok0 + tk) * D_ + 144 * ni + 8 * ch) * 2u : 0xf0000000u;
  }
  bload16<0>(xr[0], xo[0], rsXr, 0);
  bload16<0>(xr[1], xo[1], rsXr, 0);
  if (w == sp) ctr_wait(C.cxa + sp * 32, 8u);
  asm volatile("s_waitcnt lgkmcnt(0)\n\ts_barrier" ::: "memory");
  STAMP(p.dg, 24)
  u32x4 v[8];
  auto fetch = [&](int c) {
#pragma unroll
    for (int src = 0; src < 8; ++src)
      bload16_sc1<0>(v[src], src == sp ? 0xf0000000u : hvoff + c * 1024u, rsB, ((grp * 8 + sp) * 8 + src) * 9216u);
  };
  auto reduce = [&](int c) {
    asm volatile("s_waitcnt vmcnt(0)" : "+v"(v[0]), "+v"(v[1]), "+v"(v[2]), "+v"(v[3]), "+v"(v[4]), "+v"(v[5]), "+v"(v[6]), "+v"(v[7]));
    f32x4 a0 = *LDSP(f32x4, lb + L2_OWN + (2 * c) * 1024u + l * 16u), a1 = *LDSP(f32x4, lb + L2_OWN + (2 * c + 1) * 1024u + l * 16u);
#pragma unroll
    for (int src = 0; src < 8; ++src) {
      a0 += f32x4{bf_lo(v[src][0]), bf_hi(v[src][0]), bf_lo(v[src][1]), bf_hi(v[src][1])};
      a1 += f32x4{bf_lo(v[src][2]), bf_hi(v[src][2]), bf_lo(v[src][3]), bf_hi(v[src][3])};
    }
    *LDSP(f32x4, lb + L2_OUT + (c16 * 148 + 16 * c + 4 * g) * 4) = a0;
    *LDSP(f32x4, lb + L2_OUT + ((16 + c16) * 148 + 16 * c + 4 * g) * 4) = a1;
  };
  if (w < XA) {
    fetch(w);
    asm volatile("s_waitcnt vmcnt(8)\n\ts_barrier" ::: "memory");
  } else {
    asm volatile("s_waitcnt vmcnt(0)\n\ts_barrier" ::: "memory");
  }
  gu32* const cxb = ctr + (CT_X2 + grp * 8) * 32;
  if (w == 7) {
    if (l < 8) ctr_add(cxb + l * 32);
    ctr_wait(cxb + sp * 32, 8u);
  }
  if (w < XA) reduce(w);
  asm volatile("s_waitcnt vmcnt(0) lgkmcnt(0)\n\ts_barrier" ::: "memory");
  STAMP(p.dg, 25) STAMPR(p.dg, 29)
  if (w >= XA - 1) { fetch(w + 1); reduce(w + 1); }
  asm volatile("s_waitcnt vmcnt(0)" : "+v"(xr[0]), "+v"(xr[1]));
  __syncthreads();
#pragma unroll
  for (int i = 0; i < 2; ++i) {
    const int it = tid + 512 * i;
    if (it < 576) {
      const int tk = it / 18, ch = it - tk * 18;
      const f32x4 a = *LDSP(f32x4, lb + L2_OUT + (tk * 148 + 8 * ch) * 4), c = *LDSP(f32x4, lb + L2_OUT + (tk * 148 + 8 * ch + 4) * 4);
      u32x4 ov;
      ov[0] = pack2(a[0] + bf_lo(xr[i][0]), a[1] + bf_hi(xr[i][0]));
      ov[1] = pack2(a[2] + bf_lo(xr[i][1]), a[3] + bf_hi(xr[i][1]));
      ov[2] = pack2(c[0] + bf_lo(xr[i][2]), c[1] + bf_hi(xr[i][2]));
      ov[3] = pack2(c[2] + bf_lo(xr[i][3]), c[3] + bf_hi(xr[i][3]));
      *(u32x4*)((char*)p.out + xo[i]) = ov;
    }
  }
  STAMP(p.dg, 26) STAMPR(p.dg, 31) STAMP_DUMP(p.dg)
  asm volatile("98765:");
}

extern "C" void kernel_launch(void* const* d_in, const int* in_sizes, int n_in, void* d_out, int out_size,
                              void* d_ws, size_t ws_size, hipStream_t stream) {
  const uint16_t* x = (const uint16_t*)d_in[0];
  const void* wqkv = d_in[1];
  const uint16_t* wo = (const uint16_t*)d_in[2];
  const void* wg = d_in[4];
  const void* wu = d_in[5];
  const void* wd = d_in[6];
  char* ws = (char*)d_ws + (WS_TOP ? ((ws_size - (OFF_CT + SZ_CT)) & ~(size_t)0x1fffff) : 0);

  k0_ln<<<256, 512, 0, stream>>>(x, (u32x4*)(ws + OFF_XF), (const uint16_t*)wqkv, (u32x4*)(ws + OFF_WQ),
                                 (unsigned*)(ws + OFF_CT));
  KFP p;
  p.xf = ws + OFF_XF; p.wq = ws + OFF_WQ; p.wg = wg; p.wu = wu; p.wd = wd; p.wo = wo; p.x = x;
  p.hf = ws + OFF_HF; p.kv = ws + OFF_KV; p.xb = ws + OFF_XB; p.ctr = ws + OFF_CT; p.out = d_out;
  kf_main<<<256, 512, 0, stream>>>(p);
}
```
